```python
import torch

def _softmax(x, axis=-1):
    return torch.softmax(x, dim=axis)
def _gelu(x):
    return torch.nn.functional.gelu(x, approximate="tanh")
def _silu(x):
    return torch.nn.functional.silu(x)
def _sigmoid(x):
    return torch.sigmoid(x)
def _layernorm(x, axis=-1, eps=1e-5):
    m = x.mean(dim=axis, keepdim=True)
    v = x.var(dim=axis, keepdim=True, unbiased=False)
    return (x - m) / torch.sqrt(v + eps)
def _scatter_add(tgt, idx, src, axis=0):
    return tgt.clone().index_add_(axis, idx, src)
def _segment_sum(src, seg, n):
    out = torch.zeros((n,) + src.shape[1:], dtype=src.dtype)
    return out.index_add_(0, seg, src)
def _pad(x, pw):
    flat = tuple(v for pair in reversed(pw) for v in pair)
    return torch.nn.functional.pad(x, flat)

kernel_name = 'sequential_preLN__spectral_gfnet__mixer_token_mlp__prompt'

def setup_inputs():

    _w = (lambda a: (lambda t: t.to(torch.bfloat16) if t.is_floating_point() else t)(torch.as_tensor(a)))
    def _A_setup():
        B, S, D = 1, 2048, 512
        Df = D // 2 + 1
        return {
            "x": _w((lambda *s: torch.randn(*s, dtype=torch.bfloat16) * 0.05)(B, S, D)),
            "w_r": _w((lambda *s: torch.randn(*s, dtype=torch.bfloat16) * 0.2)(S, Df) * 0.02),
            "w_i": _w((lambda *s: torch.randn(*s, dtype=torch.bfloat16) * 0.2)(S, Df) * 0.02),
        }
    _A_in = _A_setup()

    def _B_setup():
        B, S, D, F = 1, 2048, 512, 1376
        return {
            "x": _w((lambda *s: torch.randn(*s, dtype=torch.bfloat16) * 0.05)(B, S, D)),
            "w1": _w((lambda *s: torch.randn(*s, dtype=torch.bfloat16) * 0.4)(S, F) * (S ** -0.5)),
            "b1": _w((lambda *s: torch.randn(*s, dtype=torch.bfloat16) * 3.2)(F) * 0.02),
            "w2": _w((lambda *s: torch.randn(*s, dtype=torch.bfloat16) * 0.2)(F, S) * (F ** -0.5)),
            "b2": _w((lambda *s: torch.randn(*s, dtype=torch.bfloat16) * 0.4)(S) * 0.02),
        }
    _B_in = _B_setup()

    return {
        "A_x": _A_in["x"],
        "A_w_r": _A_in["w_r"],
        "A_w_i": _A_in["w_i"],
        "B_x": _B_in["x"],
        "B_w1": _B_in["w1"],
        "B_b1": _B_in["b1"],
        "B_w2": _B_in["w2"],
        "B_b2": _B_in["b2"],
    }

def reference(A_x, A_w_r, A_w_i, B_x, B_w1, B_b1, B_w2, B_b2):
    def _A_ref(x, w_r, w_i):
        B, S, D = x.shape
        X = (lambda x, axes=(-2,-1): torch.fft.rfft2(x.float(), dim=axes))(x, axes=(-2, -1))
        W = w_r + 1j * w_i
        X = X * W
        y = (lambda x, s, axes=(-2,-1): torch.fft.irfft2(x, s=s, dim=axes).to(torch.bfloat16))(X, (S, D), axes=(-2, -1))
        return y

    def _B_ref(x, w1, b1, w2, b2):
        B, S, D = x.shape
        h = _layernorm(x, axis=-1)
        u = torch.einsum("bsd,sf->bdf", h, w1) + b1
        u = _gelu(u)
        y = torch.einsum("bdf,fs->bds", u, w2) + b2
        return (lambda x, *d: x.permute(*d))(y, 0, 2, 1)

    x = A_x
    h = x + _A_ref(_layernorm(x, axis=-1), A_w_r, A_w_i)
    h2 = h + _B_ref(_layernorm(h, axis=-1), B_w1, B_b1, B_w2, B_b2)
    return h2
```

```cpp
#include <hip/hip_runtime.h>
#include <hip/hip_bf16.h>
typedef unsigned short u16;
constexpr int S=2048, D=512, DF=257, F=1376;
__device__ __forceinline__ float bf2f(u16 b){ return __uint_as_float(((unsigned)b)<<16); }
__device__ __forceinline__ u16 f2bf(float f){ __hip_bfloat16 h=__float2bfloat16(f); return *(u16*)&h; }
__device__ __forceinline__ float rbf(float f){ return bf2f(f2bf(f)); }

__device__ float blocksum(float v, float* red){
  for(int o=32;o>0;o>>=1) v+=__shfl_xor(v,o);
  __syncthreads();
  if((threadIdx.x&63)==0) red[threadIdx.x>>6]=v;
  __syncthreads();
  float s=0; for(int i=0;i<(int)(blockDim.x>>6);i++) s+=red[i];
  return s;
}
__device__ float ln(float x, float* red){
  float m=blocksum(x,red)/D; float dv=x-m; float v=blocksum(dv*dv,red)/D;
  float mb=rbf(m), vb=rbf(v); float sd=rbf(sqrtf(rbf(vb+1e-5f)));
  return rbf(rbf(x-mb)/sd);
}
__global__ void kA(const u16* x, float2* X1){
  __shared__ float z[D]; __shared__ float red[8]; __shared__ float2 tw[D];
  int s=blockIdx.x,t=threadIdx.x;
  z[t]=ln(bf2f(x[s*D+t]),red);
  float sn,cs; sincospif(-2.0f*t/D,&sn,&cs); tw[t]=make_float2(cs,sn);
  __syncthreads();
  if(t<DF){ float re=0,im=0; for(int d=0;d<D;d++){ float2 w=tw[(d*t)&(D-1)]; re+=z[d]*w.x; im+=z[d]*w.y;} X1[(size_t)t*S+s]=make_float2(re,im);}
}
__global__ void kB(const float2* X1,const u16* wr,const u16* wi,float2* Y1){
  __shared__ float2 a[S]; __shared__ float2 b[S]; __shared__ float2 tw[S];
  int k=blockIdx.x;
  for(int i=threadIdx.x;i<S;i+=blockDim.x){ a[i]=X1[(size_t)k*S+i]; float sn,cs; sincospif(-2.0f*i/S,&sn,&cs); tw[i]=make_float2(cs,sn);}
  __syncthreads();
  for(int ks=threadIdx.x;ks<S;ks+=blockDim.x){ float re=0,im=0; for(int s=0;s<S;s++){float2 w=tw[(s*ks)&(S-1)]; float2 v=a[s]; re+=v.x*w.x-v.y*w.y; im+=v.x*w.y+v.y*w.x;}
    float r=bf2f(wr[ks*DF+k]), i=bf2f(wi[ks*DF+k]); b[ks]=make_float2(re*r-im*i,re*i+im*r);}
  __syncthreads();
  for(int s=threadIdx.x;s<S;s+=blockDim.x){ float re=0,im=0; for(int ks=0;ks<S;ks++){float2 w=tw[(s*ks)&(S-1)]; float2 v=b[ks]; re+=v.x*w.x+v.y*w.y; im+=-v.x*w.y+v.y*w.x;}
    Y1[(size_t)s*DF+k]=make_float2(re/S,im/S);}
}
__global__ void kC(const u16* x,const float2* Y1,u16* h,u16* hn){
  __shared__ float2 q[DF]; __shared__ float red[8]; __shared__ float2 tw[D];
  int s=blockIdx.x,t=threadIdx.x;
  if(t<DF) q[t]=Y1[(size_t)s*DF+t];
  float sn,cs; sincospif(2.0f*t/D,&sn,&cs); tw[t]=make_float2(cs,sn);
  __syncthreads();
  float acc=q[0].x+((t&1)?-q[256].x:q[256].x);
  for(int k=1;k<256;k++){ float2 w=tw[(k*t)&(D-1)]; acc+=2.f*(q[k].x*w.x-q[k].y*w.y);}
  float y=rbf(acc/D);
  float hv=rbf(bf2f(x[s*D+t])+y);
  h[s*D+t]=f2bf(hv);
  float l1=ln(hv,red); float l2=ln(l1,red);
  hn[s*D+t]=f2bf(l2);
}
__global__ void kD(const u16* hn,const u16* w1,const u16* b1,u16* u){
  int d=blockIdx.x;
  for(int f=threadIdx.x;f<F;f+=blockDim.x){ float acc=0; for(int s=0;s<S;s++) acc+=bf2f(hn[s*D+d])*bf2f(w1[s*F+f]);
    float v=rbf(rbf(acc)+bf2f(b1[f])); float g=0.5f*v*(1.f+tanhf(0.7978845608f*(v+0.044715f*v*v*v))); u[d*F+f]=f2bf(g);}
}
__global__ void kE(const u16* u,const u16* w2,const u16* b2,const u16* h,u16* out){
  int d=blockIdx.x;
  for(int s=threadIdx.x;s<S;s+=blockDim.x){ float acc=0; for(int f=0;f<F;f++) acc+=bf2f(u[d*F+f])*bf2f(w2[f*S+s]);
    float v=rbf(rbf(acc)+bf2f(b2[s])); out[s*D+d]=f2bf(bf2f(h[s*D+d])+v);}
}
extern "C" void kernel_launch(void* const* d_in, const int* in_sizes, int n_in, void* d_out, int out_size, void* d_ws, size_t ws_size, hipStream_t stream){
  const u16* x=(const u16*)d_in[0]; const u16* wr=(const u16*)d_in[1]; const u16* wi=(const u16*)d_in[2];
  const u16* w1=(const u16*)d_in[4]; const u16* b1=(const u16*)d_in[5]; const u16* w2=(const u16*)d_in[6]; const u16* b2=(const u16*)d_in[7];
  char* ws=(char*)d_ws;
  float2* X1=(float2*)ws; float2* Y1=(float2*)(ws+(8<<20)); u16* h=(u16*)(ws+(16<<20)); u16* hn=(u16*)(ws+(20<<20)); u16* u=(u16*)(ws+(24<<20));
  kA<<<S,512,0,stream>>>(x,X1);
  kB<<<DF,1024,0,stream>>>(X1,wr,wi,Y1);
  kC<<<S,512,0,stream>>>(x,Y1,h,hn);
  kD<<<D,256,0,stream>>>(hn,w1,b1,u);
  kE<<<D,256,0,stream>>>(u,w2,b2,h,(u16*)d_out);
}
```

```cpp
#include <hip/hip_runtime.h>

typedef unsigned short u16;
typedef unsigned u32;
typedef u32 v4u __attribute__((ext_vector_type(4)));
typedef u32 v2u __attribute__((ext_vector_type(2)));
typedef short v4s __attribute__((ext_vector_type(4)));
typedef float f32x2 __attribute__((ext_vector_type(2)));
typedef float f32x4 __attribute__((ext_vector_type(4)));
typedef __bf16 bf16x2 __attribute__((ext_vector_type(2)));
typedef __bf16 bf16x8 __attribute__((ext_vector_type(8)));
#define LAS __attribute__((address_space(3)))
#define GAS __attribute__((address_space(1)))
#define DEV __device__ __forceinline__

constexpr int S = 2048, D = 512, DF = 257, F = 1376;
constexpr int OFF_X1 = 2 << 20, OFF_Y1 = 6 << 20, OFF_HN = 8 << 20, OFF_H = 10 << 20, OFF_U = 12 << 20, WS_USED = 14 << 20;
constexpr int SC1 = 16;
constexpr int SPIN_CAP = 1 << 16;
constexpr int CODE_LINES = 128;

constexpr int L_X = 0;
constexpr int L_P = 8192;
constexpr int L_Q = L_P + 9216;
constexpr int L_R = L_Q + 8704;
constexpr int L_END = L_R + 8320;
constexpr int LDS_BYTES = 131072 + 1024;

DEV float asf(u32 v) { return __builtin_bit_cast(float, v); }
DEV u32 asu(float v) { return __builtin_bit_cast(u32, v); }
DEV u32 pk(float lo, float hi) { f32x2 v = {lo, hi}; return __builtin_bit_cast(u32, __builtin_convertvector(v, bf16x2)); }
DEV float blo(u32 v) { return asf(v << 16); }
DEV float bhi(u32 v) { return asf(v & 0xFFFF0000u); }
DEV f32x4 mfma(v4u a, v4u b, f32x4 c) {
  return __builtin_amdgcn_mfma_f32_16x16x32_bf16(__builtin_bit_cast(bf16x8, a), __builtin_bit_cast(bf16x8, b), c, 0, 0, 0);
}
DEV float dot2(u32 a, u32 b, float c) {
  return __builtin_amdgcn_fdot2_f32_bf16(__builtin_bit_cast(bf16x2, a), __builtin_bit_cast(bf16x2, b), c, false);
}
DEV float fsin(float rev) { return __builtin_amdgcn_sinf(rev); }
DEV float fcos(float rev) { return __builtin_amdgcn_cosf(rev); }
DEV v2u trread(LAS unsigned char* p) { return __builtin_bit_cast(v2u, __builtin_amdgcn_ds_read_tr16_b64_v4i16((LAS v4s*)p)); }
DEV u32 umax3(u32 a, u32 b, u32 c) { return max(max(a, b), c); }
DEV u32 clean(u32 v) { return min(v, 0xFFFFFFFEu); }
template <int CTRL, int RM> DEV float dppadd(float v) {
  return v + asf((u32)__builtin_amdgcn_update_dpp(0, (int)asu(v), CTRL, RM, 0xf, true));
}
DEV void wavesum2(float& s, float& q) {
  auto sw = __builtin_amdgcn_permlane32_swap(asu(s), asu(q), false, false);
  u32 a = sw[0], b = sw[1];
  float e = asf(a) + asf(b);
  e = dppadd<0x128, 0xf>(e);
  e = dppadd<0x124, 0xf>(e);
  e = dppadd<0x122, 0xf>(e);
  e = dppadd<0x121, 0xf>(e);
  e = dppadd<0x142, 0xa>(e);
  s = asf((u32)__builtin_amdgcn_readlane((int)asu(e), 31));
  q = asf((u32)__builtin_amdgcn_readlane((int)asu(e), 63));
}
#define MEMF asm volatile("" ::: "memory")
#define SBAR __builtin_amdgcn_sched_barrier(0)
#define LBAR() do { asm volatile("s_waitcnt lgkmcnt(0)" ::: "memory"); __builtin_amdgcn_s_barrier(); MEMF; } while (0)
#define DRAIN() asm volatile("s_waitcnt vmcnt(0)" ::: "memory")

#define ST(i)

struct __attribute__((packed, aligned(2))) U16x8 { v4u v; };

DEV void transpose8x4(const v2u (&r)[8], v4u (&f)[4]) {
#pragma unroll
  for (int p = 0; p < 4; p++) {
    f[0][p] = __builtin_amdgcn_perm(r[2 * p + 1].x, r[2 * p].x, 0x05040100);
    f[1][p] = __builtin_amdgcn_perm(r[2 * p + 1].x, r[2 * p].x, 0x07060302);
    f[2][p] = __builtin_amdgcn_perm(r[2 * p + 1].y, r[2 * p].y, 0x05040100);
    f[3][p] = __builtin_amdgcn_perm(r[2 * p + 1].y, r[2 * p].y, 0x07060302);
  }
}

__global__ __launch_bounds__(512, 2) void fused_kernel(const u16* __restrict__ x, const u16* __restrict__ wr, const u16* __restrict__ wi,
                                                       const u16* __restrict__ w1, const u16* __restrict__ b1, const u16* __restrict__ w2,
                                                       const u16* __restrict__ b2, u16* __restrict__ out, unsigned char* ws) {
  extern __shared__ __attribute__((aligned(16))) unsigned char smem_[];
  LAS unsigned char* L = (LAS unsigned char*)smem_;
  ST(0);
  const int t = threadIdx.x, lane = t & 63, lo = lane & 15, g = lane >> 4;
  const int w = __builtin_amdgcn_readfirstlane(t >> 6);
  const int b = blockIdx.x, c = (b & 7) * 32 + (b >> 3);
  const __amdgpu_buffer_rsrc_t rs = __builtin_amdgcn_make_buffer_rsrc((void*)ws, 0, WS_USED, 0x00020000);
  const __amdgpu_buffer_rsrc_t rw1 = __builtin_amdgcn_make_buffer_rsrc((void*)w1, 0, S * F * 2, 0x00020000);
  const __amdgpu_buffer_rsrc_t rw2 = __builtin_amdgcn_make_buffer_rsrc((void*)w2, 0, S * F * 2, 0x00020000);

  const v4u xv = *(const v4u*)(x + (256 * w + c) * D + lane * 8);
  const u32 tag = __builtin_amdgcn_raw_buffer_load_b32(rs, 0, 0, SC1) + 1u;
  u32 pf = 0;
  if (t < CODE_LINES) {
    unsigned long long pc;
    asm volatile("s_getpc_b64 %0" : "=s"(pc));
    pf = *(const GAS u32*)((pc & ~127ull) + t * 128);
  }
  const int k1B = c >> 5, blkB = c & 31;
  const v4u wv = ((const U16x8*)(((t >> 8) ? wi : wr) + (k1B + 8 * (t & 255)) * DF + 8 * blkB + 1))->v;

  const int xc = b & 7, xi = b >> 3;
  const int q1 = xc >> 1, tl = q1 * 30 + (q1 ? 1 : 0) + xi;
  const bool g1on = xi < (q1 ? 30 : 31);
  const int db = tl / 11, fg = (xc & 1) * 11 + tl % 11;
  const int nst = db == 10 ? 2 : 3;
  {
    const v4u sent = {~0u, ~0u, ~0u, ~0u};
    __builtin_amdgcn_raw_buffer_store_b128(sent, rs, OFF_Y1 + c * 8192 + t * 16, 0, SC1);
    __builtin_amdgcn_raw_buffer_store_b128(sent, rs, OFF_HN + c * 8192 + t * 16, 0, SC1);
    __builtin_amdgcn_raw_buffer_store_b128(sent, rs, OFF_H + (256 * w + c) * 1024 + lane * 16, 0, SC1);
    if (g1on && (w >> 1) < nst)
      __builtin_amdgcn_raw_buffer_store_b128(sent, rs, OFF_U + ((3 * db + (w >> 1)) * 44 + 2 * fg) * 1024 + (t & 127) * 16, 0, SC1);
  }
  SBAR;

  const float flo = (float)lo, fg4 = (float)(4 * g);
  v4u F32re, F32im;
#pragma unroll
  for (int p = 0; p < 4; p++) {
    const float a0 = fg4 + (float)(16 * (p >> 1) + 2 * (p & 1));
    const float r0 = a0 * flo * (1.f / 32), r1 = (a0 + 1.f) * flo * (1.f / 32);
    F32re[p] = pk(fcos(r0), fcos(r1));
    const u32 im = pk(-fsin(r0), -fsin(r1));
    F32im[p] = lo == 0 ? 0xBF803F80u : im;
  }
  float twc[4], tws[4];
  v4u F16re, F16im, SPre, SPim, Sre, Sim;
  const float szero = ((lo >> 3) == (g >> 1)) ? 1.f : 0.f;
#pragma unroll
  for (int r = 0; r < 4; r++) {
    const float bb = fg4 + (float)r;
    const float th = bb * flo * (1.f / 512);
    twc[r] = fcos(th); tws[r] = fsin(th);
    const float ph = bb * flo * (1.f / 16);
    const float cc = fcos(ph), ss = fsin(ph);
    F16re[r] = pk(cc, ss); F16im[r] = pk(-ss, cc);
    const float ps = bb * (flo + 1.f) * (1.f / 32);
    const u32 sp = pk(fcos(ps), -fsin(ps));
    SPre[r] = (lo & 1) ? (sp & 0xFFFFu) : (sp << 16);
    SPim[r] = (lo & 1) ? (sp >> 16) : (sp & 0xFFFF0000u);
    const float al = (float)((lo & 7) * (256 * (4 * (g & 1) + r) + c)) * (1.f / 2048);
    const float ca = fcos(al) * szero, sa = fsin(al) * szero;
    Sre[r] = pk(ca, sa); Sim[r] = pk(-sa, ca);
  }
  const int col0 = lo == 0 ? 64 * g + 15 : (g < 2 ? lo + 128 * g - 1 : 511 - lo - 128 * g);
  const int cstep = lo == 0 ? 16 : (g < 2 ? 32 : -32);
  SBAR;

  *(LAS v4u*)(L + L_X + w * 1024 + lane * 16) = xv;
  ST(1);
  {
    float s = 0.f, q = 0.f;
#pragma unroll
    for (int i = 0; i < 4; i++) { s = dot2(xv[i], 0x3F803F80u, s); q = dot2(xv[i], xv[i], q); }
    const v2u xa0 = trread(L + L_X + w * 1024 + lane * 8), xa1 = trread(L + L_X + w * 1024 + 512 + lane * 8);
    const v4u xa = {xa0.x, xa0.y, xa1.x, xa1.y};
    const f32x4 z4 = {0.f, 0.f, 0.f, 0.f};
    f32x4 Tre = mfma(xa, F32re, z4), Tim = mfma(xa, F32im, z4);
    wavesum2(s, q);
    const float mean = s * (1.f / D);
    const float var = fmaxf(q * (1.f / D) - mean * mean, 0.f);
    const float rstd = __builtin_amdgcn_rsqf(var + 1e-5f);
    const float msub = lo == 0 ? 32.f * mean : 0.f;
    v4u P;
#pragma unroll
    for (int r = 0; r < 4; r++) {
      const float wc = twc[r] * rstd, wsn = tws[r] * rstd;
      const float re = Tre[r] - msub, im = Tim[r];
      P[r] = pk(re * wc + im * wsn, im * wc - re * wsn);
    }
    const u32 sg = lo >= 8 ? 0x80008000u : 0u;
    const v4u F16imc = {F16im[0] ^ sg, F16im[1] ^ sg, F16im[2] ^ sg, F16im[3] ^ sg};
    const f32x4 Zre = mfma(F16re, P, z4), Zim = mfma(F16imc, P, z4), Yre = mfma(SPre, P, z4), Yim = mfma(SPim, P, z4);
    LAS u32* Zb = (LAS u32*)(L + L_P);
#pragma unroll
    for (int r = 0; r < 4; r++) {
      const u32 zg = pk(Zre[r], Zim[r]), zs = pk(Yre[r], Yim[r]);
      Zb[w * 260 + col0 + r * cstep] = lo == 0 ? zs : zg;
    }
    *(LAS v4u*)(L + L_R + t * 16) = wv;
    asm volatile("" ::"v"(pf));
    DRAIN();
    LBAR();
    v4u za;
#pragma unroll
    for (int j = 0; j < 4; j++) za[j] = Zb[(4 * (g & 1) + j) * 260 + 32 * w + 16 * (g >> 1) + lo];
    const f32x4 Ore = mfma(za, Sre, z4), Oim = mfma(za, Sim, z4);
    const int cB = (lo & 7) * 32 + 4 * w + 2 * (lo >> 3) + (g >> 1);
    const int o = OFF_X1 + c * 16384 + ((((cB >> 2) + c) & 63) << 7) + (cB & 3) * 32 + (g & 1) * 16;
    const v4u s0 = {pk(Ore[0], Oim[0]), tag, pk(Ore[1], Oim[1]), tag}, s1 = {pk(Ore[2], Oim[2]), tag, pk(Ore[3], Oim[3]), tag};
    __builtin_amdgcn_raw_buffer_store_b128(s0, rs, o, 0, SC1);
    __builtin_amdgcn_raw_buffer_store_b128(s1, rs, o + 8192, 0, SC1);
  }
  ST(2);
  SBAR;

  v4u W1[8][4];
  {
    const int vo = (32 * w + g) * (F * 2) + min(128 * fg + 8 * lo, F * 2 - 8);
#pragma unroll
    for (int j = 0; j < 8; j++) {
      v2u r[8];
#pragma unroll
      for (int i = 0; i < 8; i++) r[i] = __builtin_amdgcn_raw_buffer_load_b64(rw1, vo, (256 * i + 4 * j) * (F * 2), 0);
      transpose8x4(r, W1[j]);
    }
  }
  float t2c[4], t2s[4], fWr[4], fWi[4];
#pragma unroll
  for (int r = 0; r < 4; r++) {
    const float th = (fg4 + (float)r) * flo * (1.f / 256);
    t2c[r] = fcos(th); t2s[r] = fsin(th);
    const int k2 = lo + 16 * (4 * g + r);
    fWr[r] = blo(*(LAS u16*)(L + L_R + k2 * 16 + w * 2)) * (2.f / (S * D));
    fWi[r] = blo(*(LAS u16*)(L + L_R + 4096 + k2 * 16 + w * 2)) * (2.f / (S * D));
  }
  SBAR;
  DRAIN();
  ST(3);
  const int uB = t >> 1, hfB = t & 1;
  const int n2B = 16 * (4 * (uB >> 6) + (uB & 3)) + ((uB >> 2) & 15);
  {
    const int o = OFF_X1 + n2B * 16384 + ((((c >> 2) + n2B) & 63) << 7) + (c & 3) * 32 + hfB * 16;
    v4u a0, a1;
    for (int sp = 0; sp < SPIN_CAP; sp++) {
      MEMF;
      a0 = __builtin_amdgcn_raw_buffer_load_b128(rs, o, 0, SC1);
      a1 = __builtin_amdgcn_raw_buffer_load_b128(rs, o + 8192, 0, SC1);
      const u32 bad = (a0.y ^ tag) | (a0.w ^ tag) | (a1.y ^ tag) | (a1.w ^ tag);
      if (__all(bad == 0)) break;
    }
    ST(4);
    LAS u32* Bb = (LAS u32*)(L + L_Q);
    Bb[hfB * 1040 + uB] = a0.x; Bb[hfB * 1040 + 256 + uB] = a0.z; Bb[hfB * 1040 + 512 + uB] = a1.x; Bb[hfB * 1040 + 768 + uB] = a1.z;
    LBAR();
    if (b == 0 && t == 0) __builtin_amdgcn_raw_buffer_store_b32(tag, rs, 0, 0, SC1);
    LAS v4u* colp = (LAS v4u*)(Bb + (w >> 2) * 1040 + (w & 3) * 256) + lane;
    v4u P = *colp;
    const f32x4 z4 = {0.f, 0.f, 0.f, 0.f};
    f32x4 re = mfma(P, F16re, z4), im = mfma(P, F16im, z4);
#pragma unroll
    for (int r = 0; r < 4; r++) P[r] = pk(re[r] * t2c[r] + im[r] * t2s[r], im[r] * t2c[r] - re[r] * t2s[r]);
    re = mfma(F16re, P, z4); im = mfma(F16im, P, z4);
#pragma unroll
    for (int r = 0; r < 4; r++) P[r] = pk(re[r] * fWi[r] + im[r] * fWr[r], re[r] * fWr[r] - im[r] * fWi[r]);
    im = mfma(P, F16re, z4); re = mfma(P, F16im, z4);
#pragma unroll
    for (int r = 0; r < 4; r++) P[r] = pk(im[r] * t2c[r] + re[r] * t2s[r], re[r] * t2c[r] - im[r] * t2s[r]);
    im = mfma(F16re, P, z4); re = mfma(F16im, P, z4);
#pragma unroll
    for (int r = 0; r < 4; r++) P[r] = clean(pk(re[r], im[r]));
    *colp = P;
    LBAR();
    const v4u y = {Bb[hfB * 1040 + uB], Bb[hfB * 1040 + 256 + uB], Bb[hfB * 1040 + 512 + uB], Bb[hfB * 1040 + 768 + uB]};
    __builtin_amdgcn_raw_buffer_store_b128(y, rs, OFF_Y1 + c * 8192 + ((((n2B >> 2) + c) & 63) << 7) + (n2B & 3) * 32 + hfB * 16, 0, SC1);
  }
  ST(5);
  SBAR;
  v4u Ire, Iim, Gre, Gim, Hre, Him, E0, E1;
#pragma unroll
  for (int r = 0; r < 4; r++) {
    const float al = (float)((4 * (g & 1) + r) * (256 * (lo & 7) + c)) * (1.f / 2048);
    const float ca = fcos(al) * szero, sa = fsin(al) * szero;
    Ire[r] = pk(ca, -sa); Iim[r] = pk(sa, ca);
    Gre[r] = F16re[r] ^ (g < 2 ? 0x80000000u : 0u);
    Gim[r] = F16im[r] ^ (g < 2 ? 0x00008000u : 0x80008000u);
    const float ps = flo * (fg4 + (float)(r + 1)) * (1.f / 32);
    const float wg = (r == 3 && g == 3) ? 0.5f : 1.f;
    const u32 sp = pk(fcos(ps) * wg, -fsin(ps));
    Hre[r] = (r & 1) ? sp : 0u;
    Him[r] = (r & 1) ? 0u : sp;
    const float be = flo * (fg4 + (float)r) * (1.f / 32);
    u32 e = pk(fcos(be), -fsin(be));
    if (r == 0) e = g == 0 ? ((lo & 1) ? 0xBF803F80u : 0x3F803F80u) : e;
    E0[r] = e;
    E1[r] = (r & 1) ? e ^ 0x80008000u : e;
  }
  SBAR;
  DRAIN();
  ST(6);
  {
    const int pB = (lane >> 3) * 32 + 4 * w + ((lane >> 1) & 3);
    const int o = OFF_Y1 + pB * 8192 + ((((c >> 2) + pB) & 63) << 7) + (c & 3) * 32 + (lane & 1) * 16;
    v4u a;
    for (int sp = 0; sp < SPIN_CAP; sp++) {
      MEMF;
      a = __builtin_amdgcn_raw_buffer_load_b128(rs, o, 0, SC1);
      if (__all(max(umax3(a.x, a.y, a.z), a.w) != ~0u)) break;
    }
    ST(7);
    LAS u32* Cb = (LAS u32*)(L + L_P) + w * 288;
    *(LAS v4u*)(Cb + (lane >> 3) * 36 + (lane & 7) * 4) = a;
    v4u P;
#pragma unroll
    for (int j = 0; j < 4; j++) P[j] = Cb[(4 * (g & 1) + j) * 36 + 16 * (g >> 1) + lo];
    const f32x4 z4 = {0.f, 0.f, 0.f, 0.f};
    f32x4 re = mfma(P, Ire, z4), im = mfma(P, Iim, z4);
    LAS u32* Qb = (LAS u32*)(L + L_R);
#pragma unroll
    for (int r = 0; r < 4; r++) P[r] = pk(re[r], im[r]);
    *(LAS v4u*)(Qb + (lo & 7) * 260 + 32 * w + 16 * (lo >> 3) + 4 * g) = P;
    const v2u x0 = *(LAS v2u*)(L + L_X + w * 1024 + 32 * lo + 8 * g), x1 = *(LAS v2u*)(L + L_X + w * 1024 + 512 + 32 * lo + 8 * g);
    LBAR();
#pragma unroll
    for (int r = 0; r < 4; r++) P[r] = Qb[w * 260 + col0 + r * cstep];
    re = mfma(P, Gre, z4); im = mfma(P, Gim, z4);
    const f32x4 sre = mfma(P, Hre, z4), sim = mfma(P, Him, z4);
    re[0] = g == 0 ? sre[0] : re[0];
    im[0] = g == 0 ? sim[0] : im[0];
#pragma unroll
    for (int r = 0; r < 4; r++) P[r] = pk(re[r] * twc[r] - im[r] * tws[r], im[r] * twc[r] + re[r] * tws[r]);
    const f32x4 y0 = mfma(P, E0, z4), y1 = mfma(P, E1, z4);
    v4u h;
    h[0] = pk(blo(x0.x) + y0[0], bhi(x0.x) + y0[1]); h[1] = pk(blo(x0.y) + y0[2], bhi(x0.y) + y0[3]);
    h[2] = pk(blo(x1.x) + y1[0], bhi(x1.x) + y1[1]); h[3] = pk(blo(x1.y) + y1[2], bhi(x1.y) + y1[3]);
    float s = 0.f, q = 0.f;
#pragma unroll
    for (int i = 0; i < 4; i++) { s = dot2(h[i], 0x3F803F80u, s); q = dot2(h[i], h[i], q); }
    wavesum2(s, q);
    const float mean = s * (1.f / D);
    const float var = fmaxf(q * (1.f / D) - mean * mean, 0.f);
    const float r1 = __builtin_amdgcn_rsqf(var + 1e-5f);
    const float sc = r1 * __builtin_amdgcn_rsqf(var * r1 * r1 + 1e-5f);
    const float mo = -mean * sc;
    v2u n0, n1;
    n0.x = pk(blo(h[0]) * sc + mo, bhi(h[0]) * sc + mo); n0.y = pk(blo(h[1]) * sc + mo, bhi(h[1]) * sc + mo);
    n1.x = pk(blo(h[2]) * sc + mo, bhi(h[2]) * sc + mo); n1.y = pk(blo(h[3]) * sc + mo, bhi(h[3]) * sc + mo);
    *(LAS v2u*)(L + L_Q + w * 1088 + 32 * lo + 8 * g) = n0;
    *(LAS v2u*)(L + L_Q + w * 1088 + 512 + 32 * lo + 8 * g) = n1;
    *(LAS v2u*)(L + L_X + w * 1024 + 32 * lo + 8 * g) = v2u{clean(h[0]), clean(h[1])};
    *(LAS v2u*)(L + L_X + w * 1024 + 512 + 32 * lo + 8 * g) = v2u{clean(h[2]), clean(h[3])};
    LBAR();
    LAS unsigned char* tp = L + L_Q + (lo >> 2) * 1088 + 128 * w + 32 * g + 8 * (lo & 3);
    const v2u t0 = trread(tp), t1 = trread(tp + 4 * 1088);
    const v4u hv = *(LAS v4u*)(L + L_X + w * 1024 + lane * 16);
    const int pos = 64 * w + 16 * ((lane >> 1) & 3) + 2 * (lane >> 3) + (lane & 1);
    const v4u hn = {clean(t0.x), clean(t0.y), clean(t1.x), clean(t1.y)};
    __builtin_amdgcn_raw_buffer_store_b128(hn, rs, OFF_HN + c * 8192 + pos * 16, 0, SC1);
    __builtin_amdgcn_raw_buffer_store_b128(hv, rs, OFF_H + (256 * w + c) * 1024 + lane * 16, 0, SC1);
  }
  ST(8);
  SBAR;
  const int f0 = 64 * fg + 16 * (w & 3) + 8 * (lane & 1) + 2 * (w >> 2) + ((lane >> 1) & 1);
  const bool fok = f0 < F;
  const float bia0 = blo(b1[fok ? f0 : 0]), bia1 = blo(b1[fok ? f0 + 4 : 0]);
  LBAR();
  DRAIN();
  ST(9);
  if (g1on) {
    const int ho = OFF_HN + (32 * w + g) * 8192 + (48 * db + lo) * 16;
    v4u A[2][8];
#pragma unroll
    for (int st = 0; st < 2; st++)
#pragma unroll
      for (int j = 0; j < 8; j++) A[st][j] = __builtin_amdgcn_raw_buffer_load_b128(rs, ho + st * 256, j * 32768, SC1);
    SBAR;
#pragma unroll
    for (int st = 0; st < 3; st++) {
      if (st < nst) {
        f32x4 acc[4];
        for (int sp = 0; sp < SPIN_CAP; sp++) {
          u32 mx = 0;
#pragma unroll
          for (int cc = 0; cc < 4; cc++) acc[cc] = f32x4{0.f, 0.f, 0.f, 0.f};
#pragma unroll
          for (int j = 0; j < 8; j++) {
            const v4u a = A[st & 1][j];
#pragma unroll
            for (int cc = 0; cc < 4; cc++) acc[cc] = mfma(W1[j][cc], a, acc[cc]);
            mx = umax3(umax3(mx, a.x, a.y), a.z, a.w);
          }
          if (__all(mx != ~0u)) break;
          MEMF;
#pragma unroll
          for (int j = 0; j < 8; j++) A[st & 1][j] = __builtin_amdgcn_raw_buffer_load_b128(rs, ho + st * 256, j * 32768, SC1);
        }
        if (st == 0 && nst == 3) {
#pragma unroll
          for (int j = 0; j < 8; j++) A[0][j] = __builtin_amdgcn_raw_buffer_load_b128(rs, ho + 512, j * 32768, SC1);
        }
        LAS unsigned char* R = L + st * 33792;
#pragma unroll
        for (int cc = 0; cc < 4; cc++) *(LAS f32x4*)(R + w * 4224 + (cc >> 1) * 1024 + (cc & 1) * 2176 + lane * 16) = acc[cc];
        LBAR();
        LAS unsigned char* rp = R + (w >> 2) * 1024 + ((lane >> 1) & 1) * 2176 + ((w & 3) * 16 + (lane >> 2)) * 16 + (lane & 1) * 8;
        f32x2 v = {bia0, bia1};
#pragma unroll
        for (int ww = 0; ww < 8; ww++) v += *(LAS f32x2*)(rp + ww * 4224);
        float o2[2];
#pragma unroll
        for (int e = 0; e < 2; e++) {
          const float u = v[e];
          const float z = u * (u * u * (-0.044715f * 2.f * 0.7978845608f * 1.4426950409f) + (-2.f * 0.7978845608f * 1.4426950409f));
          o2[e] = u * __builtin_amdgcn_rcpf(1.f + __builtin_amdgcn_exp2f(z));
        }
        const u32 uo = fok ? clean(pk(o2[0], o2[1])) : 0u;
        v4u ov;
        ov.x = uo;
        ov.y = (u32)__builtin_amdgcn_update_dpp(0, (int)uo, 0x55, 0xf, 0xf, true);
        ov.z = (u32)__builtin_amdgcn_update_dpp(0, (int)uo, 0xAA, 0xf, 0xf, true);
        ov.w = (u32)__builtin_amdgcn_update_dpp(0, (int)uo, 0xFF, 0xf, 0xf, true);
        if ((lane & 3) == 0) __builtin_amdgcn_raw_buffer_store_b128(ov, rs, OFF_U + ((3 * db + st) * 44 + 2 * fg) * 1024 + t * 4, 0, SC1);
      }
    }
  }
  ST(10);
  SBAR;
  {
    const int d2 = 2 * (xc >> 1) + (xi >> 4), sb = 16 * (xc & 1) + (xi & 15);
    const int j0 = w < 4 ? 6 * w : 4 + 5 * w;
    const bool six = w < 4;
    v4u W2[6][4];
    {
      const int vo = 16 * g * (S * 2) + 128 * sb + 8 * lo;
#pragma unroll
      for (int j = 0; j < 6; j++) {
        if (j < 5 || six) {
          const int jg = j0 + j;
          const int fb = 64 * (jg >> 1) + 2 * (jg & 1);
          v2u r[8];
#pragma unroll
          for (int e = 0; e < 8; e++) r[e] = __builtin_amdgcn_raw_buffer_load_b64(rw2, vo + (fb + 4 * (e & 3) + (e >> 2)) * (S * 2), 0, 0);
          transpose8x4(r, W2[j]);
        }
      }
    }
    const int sl = t >> 3, q8 = t & 7;
    const int so = 64 * sb + 4 * (sl & 15) + (sl >> 4);
    const float bias2 = blo(b2[so]);
    LBAR();
    DRAIN();
    ST(11);
    const int uo = OFF_U + (4 * d2 * 44 + j0) * 1024 + lane * 16;
    v4u A[4][6];
#pragma unroll
    for (int st = 0; st < 4; st++)
#pragma unroll
      for (int j = 0; j < 6; j++)
        if (j < 5 || six) A[st][j] = __builtin_amdgcn_raw_buffer_load_b128(rs, uo, (st * 44 + j) * 1024, SC1);
    const int hoff = OFF_H + so * 1024 + d2 * 128 + q8 * 16;
    v4u hv = __builtin_amdgcn_raw_buffer_load_b128(rs, hoff, 0, SC1);
    SBAR;
    f32x2 res[4];
#pragma unroll
    for (int st = 0; st < 4; st++) {
      f32x4 acc[4];
      for (int sp = 0; sp < SPIN_CAP; sp++) {
        u32 mx = 0;
#pragma unroll
        for (int cc = 0; cc < 4; cc++) acc[cc] = f32x4{0.f, 0.f, 0.f, 0.f};
#pragma unroll
        for (int j = 0; j < 6; j++) {
          if (j < 5 || six) {
            const v4u a = A[st][j];
#pragma unroll
            for (int cc = 0; cc < 4; cc++) acc[cc] = mfma(a, W2[j][cc], acc[cc]);
            mx = umax3(umax3(mx, a.x, a.y), a.z, a.w);
          }
        }
        if (__all(mx != ~0u)) break;
        MEMF;
#pragma unroll
        for (int j = 0; j < 6; j++)
          if (j < 5 || six) A[st][j] = __builtin_amdgcn_raw_buffer_load_b128(rs, uo, (st * 44 + j) * 1024, SC1);
      }
      LAS unsigned char* R = L + st * 32768;
#pragma unroll
      for (int cc = 0; cc < 4; cc++) *(LAS f32x4*)(R + w * 4096 + cc * 1024 + g * 256 + ((lo ^ (4 * g)) * 16)) = acc[cc];
      LBAR();
      LAS unsigned char* rp = R + (sl >> 4) * 1024 + (q8 >> 1) * 256 + (((sl & 15) ^ (4 * (q8 >> 1))) * 16) + (q8 & 1) * 8;
      f32x2 v = {bias2, bias2};
#pragma unroll
      for (int ww = 0; ww < 8; ww++) v += *(LAS f32x2*)(rp + ww * 4096);
      res[st] = v;
    }
    for (int sp = 0; sp < SPIN_CAP; sp++) {
      if (__all(max(umax3(hv.x, hv.y, hv.z), hv.w) != ~0u)) break;
      MEMF;
      hv = __builtin_amdgcn_raw_buffer_load_b128(rs, hoff, 0, SC1);
    }
    v4u o;
#pragma unroll
    for (int st = 0; st < 4; st++) o[st] = pk(blo(hv[st]) + res[st][0], bhi(hv[st]) + res[st][1]);
    *(v4u*)(out + so * D + d2 * 64 + q8 * 8) = o;
  }
  ST(12);
}

extern "C" void kernel_launch(void* const* d_in, const int* in_sizes, int n_in, void* d_out, int out_size, void* d_ws, size_t ws_size,
                              hipStream_t stream) {
  (void)hipFuncSetAttribute((const void*)fused_kernel, hipFuncAttributeMaxDynamicSharedMemorySize, LDS_BYTES);
  fused_kernel<<<256, 512, LDS_BYTES, stream>>>((const u16*)d_in[0], (const u16*)d_in[1], (const u16*)d_in[2], (const u16*)d_in[4],
                                                (const u16*)d_in[5], (const u16*)d_in[6], (const u16*)d_in[7], (u16*)d_out,
                                                (unsigned char*)d_ws);
}
```

```cpp
#define NDEP 1
#include <hip/hip_runtime.h>

typedef unsigned short u16;
typedef unsigned u32;
typedef u32 v4u __attribute__((ext_vector_type(4)));
typedef u32 v2u __attribute__((ext_vector_type(2)));
typedef short v4s __attribute__((ext_vector_type(4)));
typedef float f32x2 __attribute__((ext_vector_type(2)));
typedef float f32x4 __attribute__((ext_vector_type(4)));
typedef __bf16 bf16x2 __attribute__((ext_vector_type(2)));
typedef __bf16 bf16x8 __attribute__((ext_vector_type(8)));
#define LAS __attribute__((address_space(3)))
#define GAS __attribute__((address_space(1)))
#define DEV __device__ __forceinline__

constexpr int S = 2048, D = 512, DF = 257, F = 1376;
constexpr int OFF_X1 = 2 << 20, OFF_Y1 = 6 << 20, OFF_HN = 8 << 20, OFF_H = 10 << 20, OFF_U = 12 << 20, WS_USED = 14 << 20;
constexpr int SC1 = 16;
constexpr int SPIN_CAP = 1 << 16;
constexpr int CODE_LINES = 128;

constexpr int L_X = 0;
constexpr int L_P = 8192;
constexpr int L_Q = L_P + 9216;
constexpr int L_R = L_Q + 8704;
constexpr int L_END = L_R + 8320;
constexpr int LDS_BYTES = 131072 + 1024;

DEV float asf(u32 v) { return __builtin_bit_cast(float, v); }
DEV u32 asu(float v) { return __builtin_bit_cast(u32, v); }
DEV u32 pk(float lo, float hi) { f32x2 v = {lo, hi}; return __builtin_bit_cast(u32, __builtin_convertvector(v, bf16x2)); }
DEV float blo(u32 v) { return asf(v << 16); }
DEV float bhi(u32 v) { return asf(v & 0xFFFF0000u); }
DEV f32x4 mfma(v4u a, v4u b, f32x4 c) {
  return __builtin_amdgcn_mfma_f32_16x16x32_bf16(__builtin_bit_cast(bf16x8, a), __builtin_bit_cast(bf16x8, b), c, 0, 0, 0);
}
DEV float dot2(u32 a, u32 b, float c) {
  return __builtin_amdgcn_fdot2_f32_bf16(__builtin_bit_cast(bf16x2, a), __builtin_bit_cast(bf16x2, b), c, false);
}
DEV float fsin(float rev) { return __builtin_amdgcn_sinf(rev); }
DEV float fcos(float rev) { return __builtin_amdgcn_cosf(rev); }
DEV v2u trread(LAS unsigned char* p) { return __builtin_bit_cast(v2u, __builtin_amdgcn_ds_read_tr16_b64_v4i16((LAS v4s*)p)); }
DEV u32 umax3(u32 a, u32 b, u32 c) { return max(max(a, b), c); }
DEV u32 clean(u32 v) { return min(v, 0xFFFFFFFEu); }
template <int CTRL, int RM> DEV float dppadd(float v) {
  return v + asf((u32)__builtin_amdgcn_update_dpp(0, (int)asu(v), CTRL, RM, 0xf, true));
}
DEV void wavesum2(float& s, float& q) {
  auto sw = __builtin_amdgcn_permlane32_swap(asu(s), asu(q), false, false);
  u32 a = sw[0], b = sw[1];
  float e = asf(a) + asf(b);
  e = dppadd<0x128, 0xf>(e);
  e = dppadd<0x124, 0xf>(e);
  e = dppadd<0x122, 0xf>(e);
  e = dppadd<0x121, 0xf>(e);
  e = dppadd<0x142, 0xa>(e);
  s = asf((u32)__builtin_amdgcn_readlane((int)asu(e), 31));
  q = asf((u32)__builtin_amdgcn_readlane((int)asu(e), 63));
}
#define MEMF asm volatile("" ::: "memory")
#define SBAR __builtin_amdgcn_sched_barrier(0)
#define LBAR() do { asm volatile("s_waitcnt lgkmcnt(0)" ::: "memory"); __builtin_amdgcn_s_barrier(); MEMF; } while (0)
#define DRAIN() asm volatile("s_waitcnt vmcnt(0)" ::: "memory")

#define ST(i)

struct __attribute__((packed, aligned(2))) U16x8 { v4u v; };

DEV void transpose8x4(const v2u (&r)[8], v4u (&f)[4]) {
#pragma unroll
  for (int p = 0; p < 4; p++) {
    f[0][p] = __builtin_amdgcn_perm(r[2 * p + 1].x, r[2 * p].x, 0x05040100);
    f[1][p] = __builtin_amdgcn_perm(r[2 * p + 1].x, r[2 * p].x, 0x07060302);
    f[2][p] = __builtin_amdgcn_perm(r[2 * p + 1].y, r[2 * p].y, 0x05040100);
    f[3][p] = __builtin_amdgcn_perm(r[2 * p + 1].y, r[2 * p].y, 0x07060302);
  }
}

__global__ __launch_bounds__(512, 2) void fused_kernel(const u16* __restrict__ x, const u16* __restrict__ wr, const u16* __restrict__ wi,
                                                       const u16* __restrict__ w1, const u16* __restrict__ b1, const u16* __restrict__ w2,
                                                       const u16* __restrict__ b2, u16* __restrict__ out, unsigned char* ws, const u32* bx) {
  extern __shared__ __attribute__((aligned(16))) unsigned char smem_[];
  LAS unsigned char* L = (LAS unsigned char*)smem_;
  ST(0);
  const int t = threadIdx.x, lane = t & 63, lo = lane & 15, g = lane >> 4;
  const int w = __builtin_amdgcn_readfirstlane(t >> 6);
  const int b = blockIdx.x, c = (b & 7) * 32 + (b >> 3);
  const __amdgpu_buffer_rsrc_t rs = __builtin_amdgcn_make_buffer_rsrc((void*)ws, 0, WS_USED, 0x00020000);
  const __amdgpu_buffer_rsrc_t rw1 = __builtin_amdgcn_make_buffer_rsrc((void*)w1, 0, S * F * 2, 0x00020000);
  const __amdgpu_buffer_rsrc_t rw2 = __builtin_amdgcn_make_buffer_rsrc((void*)w2, 0, S * F * 2, 0x00020000);

  {
    u32 dep = 0;
#pragma unroll
    for (int i = 0; i < NDEP; i++) {
      u32 v = bx[(i * 2048 + b * 8 + w) * 32 + (lane & 31) + dep];
      asm volatile("v_and_b32 %0, 0, %1" : "=v"(dep) : "v"(v));
    }
    asm volatile("s_waitcnt vmcnt(0)" ::"v"(dep) : "memory");
  }
  SBAR;
  const v4u xv = *(const v4u*)(x + (256 * w + c) * D + lane * 8);
  const u32 tag = __builtin_amdgcn_raw_buffer_load_b32(rs, 0, 0, SC1) + 1u;
  u32 pf = 0;
  if (t < CODE_LINES) {
    unsigned long long pc;
    asm volatile("s_getpc_b64 %0" : "=s"(pc));
    pf = *(const GAS u32*)((pc & ~127ull) + t * 128);
  }
  const int k1B = c >> 5, blkB = c & 31;
  const v4u wv = ((const U16x8*)(((t >> 8) ? wi : wr) + (k1B + 8 * (t & 255)) * DF + 8 * blkB + 1))->v;

  const int xc = b & 7, xi = b >> 3;
  const int q1 = xc >> 1, tl = q1 * 30 + (q1 ? 1 : 0) + xi;
  const bool g1on = xi < (q1 ? 30 : 31);
  const int db = tl / 11, fg = (xc & 1) * 11 + tl % 11;
  const int nst = db == 10 ? 2 : 3;
  {
    const v4u sent = {~0u, ~0u, ~0u, ~0u};
    __builtin_amdgcn_raw_buffer_store_b128(sent, rs, OFF_Y1 + c * 8192 + t * 16, 0, SC1);
    __builtin_amdgcn_raw_buffer_store_b128(sent, rs, OFF_HN + c * 8192 + t * 16, 0, SC1);
    __builtin_amdgcn_raw_buffer_store_b128(sent, rs, OFF_H + (256 * w + c) * 1024 + lane * 16, 0, SC1);
    if (g1on && (w >> 1) < nst)
      __builtin_amdgcn_raw_buffer_store_b128(sent, rs, OFF_U + ((3 * db + (w >> 1)) * 44 + 2 * fg) * 1024 + (t & 127) * 16, 0, SC1);
  }
  SBAR;

  const float flo = (float)lo, fg4 = (float)(4 * g);
  v4u F32re, F32im;
#pragma unroll
  for (int p = 0; p < 4; p++) {
    const float a0 = fg4 + (float)(16 * (p >> 1) + 2 * (p & 1));
    const float r0 = a0 * flo * (1.f / 32), r1 = (a0 + 1.f) * flo * (1.f / 32);
    F32re[p] = pk(fcos(r0), fcos(r1));
    const u32 im = pk(-fsin(r0), -fsin(r1));
    F32im[p] = lo == 0 ? 0xBF803F80u : im;
  }
  float twc[4], tws[4];
  v4u F16re, F16im, SPre, SPim, Sre, Sim;
  const float szero = ((lo >> 3) == (g >> 1)) ? 1.f : 0.f;
#pragma unroll
  for (int r = 0; r < 4; r++) {
    const float bb = fg4 + (float)r;
    const float th = bb * flo * (1.f / 512);
    twc[r] = fcos(th); tws[r] = fsin(th);
    const float ph = bb * flo * (1.f / 16);
    const float cc = fcos(ph), ss = fsin(ph);
    F16re[r] = pk(cc, ss); F16im[r] = pk(-ss, cc);
    const float ps = bb * (flo + 1.f) * (1.f / 32);
    const u32 sp = pk(fcos(ps), -fsin(ps));
    SPre[r] = (lo & 1) ? (sp & 0xFFFFu) : (sp << 16);
    SPim[r] = (lo & 1) ? (sp >> 16) : (sp & 0xFFFF0000u);
    const float al = (float)((lo & 7) * (256 * (4 * (g & 1) + r) + c)) * (1.f / 2048);
    const float ca = fcos(al) * szero, sa = fsin(al) * szero;
    Sre[r] = pk(ca, sa); Sim[r] = pk(-sa, ca);
  }
  const int col0 = lo == 0 ? 64 * g + 15 : (g < 2 ? lo + 128 * g - 1 : 511 - lo - 128 * g);
  const int cstep = lo == 0 ? 16 : (g < 2 ? 32 : -32);
  SBAR;

  *(LAS v4u*)(L + L_X + w * 1024 + lane * 16) = xv;
  ST(1);
  {
    float s = 0.f, q = 0.f;
#pragma unroll
    for (int i = 0; i < 4; i++) { s = dot2(xv[i], 0x3F803F80u, s); q = dot2(xv[i], xv[i], q); }
    const v2u xa0 = trread(L + L_X + w * 1024 + lane * 8), xa1 = trread(L + L_X + w * 1024 + 512 + lane * 8);
    const v4u xa = {xa0.x, xa0.y, xa1.x, xa1.y};
    const f32x4 z4 = {0.f, 0.f, 0.f, 0.f};
    f32x4 Tre = mfma(xa, F32re, z4), Tim = mfma(xa, F32im, z4);
    wavesum2(s, q);
    const float mean = s * (1.f / D);
    const float var = fmaxf(q * (1.f / D) - mean * mean, 0.f);
    const float rstd = __builtin_amdgcn_rsqf(var + 1e-5f);
    const float msub = lo == 0 ? 32.f * mean : 0.f;
    v4u P;
#pragma unroll
    for (int r = 0; r < 4; r++) {
      const float wc = twc[r] * rstd, wsn = tws[r] * rstd;
      const float re = Tre[r] - msub, im = Tim[r];
      P[r] = pk(re * wc + im * wsn, im * wc - re * wsn);
    }
    const u32 sg = lo >= 8 ? 0x80008000u : 0u;
    const v4u F16imc = {F16im[0] ^ sg, F16im[1] ^ sg, F16im[2] ^ sg, F16im[3] ^ sg};
    const f32x4 Zre = mfma(F16re, P, z4), Zim = mfma(F16imc, P, z4), Yre = mfma(SPre, P, z4), Yim = mfma(SPim, P, z4);
    LAS u32* Zb = (LAS u32*)(L + L_P);
#pragma unroll
    for (int r = 0; r < 4; r++) {
      const u32 zg = pk(Zre[r], Zim[r]), zs = pk(Yre[r], Yim[r]);
      Zb[w * 260 + col0 + r * cstep] = lo == 0 ? zs : zg;
    }
    *(LAS v4u*)(L + L_R + t * 16) = wv;
    asm volatile("" ::"v"(pf));
    DRAIN();
    LBAR();
    v4u za;
#pragma unroll
    for (int j = 0; j < 4; j++) za[j] = Zb[(4 * (g & 1) + j) * 260 + 32 * w + 16 * (g >> 1) + lo];
    const f32x4 Ore = mfma(za, Sre, z4), Oim = mfma(za, Sim, z4);
    const int cB = (lo & 7) * 32 + 4 * w + 2 * (lo >> 3) + (g >> 1);
    const int o = OFF_X1 + c * 16384 + ((((cB >> 2) + c) & 63) << 7) + (cB & 3) * 32 + (g & 1) * 16;
    const v4u s0 = {pk(Ore[0], Oim[0]), tag, pk(Ore[1], Oim[1]), tag}, s1 = {pk(Ore[2], Oim[2]), tag, pk(Ore[3], Oim[3]), tag};
    __builtin_amdgcn_raw_buffer_store_b128(s0, rs, o, 0, SC1);
    __builtin_amdgcn_raw_buffer_store_b128(s1, rs, o + 8192, 0, SC1);
  }
  ST(2);
  SBAR;

  v4u W1[8][4];
  {
    const int vo = (32 * w + g) * (F * 2) + min(128 * fg + 8 * lo, F * 2 - 8);
#pragma unroll
    for (int j = 0; j < 8; j++) {
      v2u r[8];
#pragma unroll
      for (int i = 0; i < 8; i++) r[i] = __builtin_amdgcn_raw_buffer_load_b64(rw1, vo, (256 * i + 4 * j) * (F * 2), 0);
      transpose8x4(r, W1[j]);
    }
  }
  float t2c[4], t2s[4], fWr[4], fWi[4];
#pragma unroll
  for (int r = 0; r < 4; r++) {
    const float th = (fg4 + (float)r) * flo * (1.f / 256);
    t2c[r] = fcos(th); t2s[r] = fsin(th);
    const int k2 = lo + 16 * (4 * g + r);
    fWr[r] = blo(*(LAS u16*)(L + L_R + k2 * 16 + w * 2)) * (2.f / (S * D));
    fWi[r] = blo(*(LAS u16*)(L + L_R + 4096 + k2 * 16 + w * 2)) * (2.f / (S * D));
  }
  SBAR;
  DRAIN();
  ST(3);
  const int uB = t >> 1, hfB = t & 1;
  const int n2B = 16 * (4 * (uB >> 6) + (uB & 3)) + ((uB >> 2) & 15);
  {
    const int o = OFF_X1 + n2B * 16384 + ((((c >> 2) + n2B) & 63) << 7) + (c & 3) * 32 + hfB * 16;
    v4u a0, a1;
    for (int sp = 0; sp < SPIN_CAP; sp++) {
      MEMF;
      a0 = __builtin_amdgcn_raw_buffer_load_b128(rs, o, 0, SC1);
      a1 = __builtin_amdgcn_raw_buffer_load_b128(rs, o + 8192, 0, SC1);
      const u32 bad = (a0.y ^ tag) | (a0.w ^ tag) | (a1.y ^ tag) | (a1.w ^ tag);
      if (__all(bad == 0)) break;
    }
    ST(4);
    LAS u32* Bb = (LAS u32*)(L + L_Q);
    Bb[hfB * 1040 + uB] = a0.x; Bb[hfB * 1040 + 256 + uB] = a0.z; Bb[hfB * 1040 + 512 + uB] = a1.x; Bb[hfB * 1040 + 768 + uB] = a1.z;
    LBAR();
    if (b == 0 && t == 0) __builtin_amdgcn_raw_buffer_store_b32(tag, rs, 0, 0, SC1);
    LAS v4u* colp = (LAS v4u*)(Bb + (w >> 2) * 1040 + (w & 3) * 256) + lane;
    v4u P = *colp;
    const f32x4 z4 = {0.f, 0.f, 0.f, 0.f};
    f32x4 re = mfma(P, F16re, z4), im = mfma(P, F16im, z4);
#pragma unroll
    for (int r = 0; r < 4; r++) P[r] = pk(re[r] * t2c[r] + im[r] * t2s[r], im[r] * t2c[r] - re[r] * t2s[r]);
    re = mfma(F16re, P, z4); im = mfma(F16im, P, z4);
#pragma unroll
    for (int r = 0; r < 4; r++) P[r] = pk(re[r] * fWi[r] + im[r] * fWr[r], re[r] * fWr[r] - im[r] * fWi[r]);
    im = mfma(P, F16re, z4); re = mfma(P, F16im, z4);
#pragma unroll
    for (int r = 0; r < 4; r++) P[r] = pk(im[r] * t2c[r] + re[r] * t2s[r], re[r] * t2c[r] - im[r] * t2s[r]);
    im = mfma(F16re, P, z4); re = mfma(F16im, P, z4);
#pragma unroll
    for (int r = 0; r < 4; r++) P[r] = clean(pk(re[r], im[r]));
    *colp = P;
    LBAR();
    const v4u y = {Bb[hfB * 1040 + uB], Bb[hfB * 1040 + 256 + uB], Bb[hfB * 1040 + 512 + uB], Bb[hfB * 1040 + 768 + uB]};
    __builtin_amdgcn_raw_buffer_store_b128(y, rs, OFF_Y1 + c * 8192 + ((((n2B >> 2) + c) & 63) << 7) + (n2B & 3) * 32 + hfB * 16, 0, SC1);
  }
  ST(5);
  SBAR;
  v4u Ire, Iim, Gre, Gim, Hre, Him, E0, E1;
#pragma unroll
  for (int r = 0; r < 4; r++) {
    const float al = (float)((4 * (g & 1) + r) * (256 * (lo & 7) + c)) * (1.f / 2048);
    const float ca = fcos(al) * szero, sa = fsin(al) * szero;
    Ire[r] = pk(ca, -sa); Iim[r] = pk(sa, ca);
    Gre[r] = F16re[r] ^ (g < 2 ? 0x80000000u : 0u);
    Gim[r] = F16im[r] ^ (g < 2 ? 0x00008000u : 0x80008000u);
    const float ps = flo * (fg4 + (float)(r + 1)) * (1.f / 32);
    const float wg = (r == 3 && g == 3) ? 0.5f : 1.f;
    const u32 sp = pk(fcos(ps) * wg, -fsin(ps));
    Hre[r] = (r & 1) ? sp : 0u;
    Him[r] = (r & 1) ? 0u : sp;
    const float be = flo * (fg4 + (float)r) * (1.f / 32);
    u32 e = pk(fcos(be), -fsin(be));
    if (r == 0) e = g == 0 ? ((lo & 1) ? 0xBF803F80u : 0x3F803F80u) : e;
    E0[r] = e;
    E1[r] = (r & 1) ? e ^ 0x80008000u : e;
  }
  SBAR;
  DRAIN();
  ST(6);
  {
    const int pB = (lane >> 3) * 32 + 4 * w + ((lane >> 1) & 3);
    const int o = OFF_Y1 + pB * 8192 + ((((c >> 2) + pB) & 63) << 7) + (c & 3) * 32 + (lane & 1) * 16;
    v4u a;
    for (int sp = 0; sp < SPIN_CAP; sp++) {
      MEMF;
      a = __builtin_amdgcn_raw_buffer_load_b128(rs, o, 0, SC1);
      if (__all(max(umax3(a.x, a.y, a.z), a.w) != ~0u)) break;
    }
    ST(7);
    LAS u32* Cb = (LAS u32*)(L + L_P) + w * 288;
    *(LAS v4u*)(Cb + (lane >> 3) * 36 + (lane & 7) * 4) = a;
    v4u P;
#pragma unroll
    for (int j = 0; j < 4; j++) P[j] = Cb[(4 * (g & 1) + j) * 36 + 16 * (g >> 1) + lo];
    const f32x4 z4 = {0.f, 0.f, 0.f, 0.f};
    f32x4 re = mfma(P, Ire, z4), im = mfma(P, Iim, z4);
    LAS u32* Qb = (LAS u32*)(L + L_R);
#pragma unroll
    for (int r = 0; r < 4; r++) P[r] = pk(re[r], im[r]);
    *(LAS v4u*)(Qb + (lo & 7) * 260 + 32 * w + 16 * (lo >> 3) + 4 * g) = P;
    const v2u x0 = *(LAS v2u*)(L + L_X + w * 1024 + 32 * lo + 8 * g), x1 = *(LAS v2u*)(L + L_X + w * 1024 + 512 + 32 * lo + 8 * g);
    LBAR();
#pragma unroll
    for (int r = 0; r < 4; r++) P[r] = Qb[w * 260 + col0 + r * cstep];
    re = mfma(P, Gre, z4); im = mfma(P, Gim, z4);
    const f32x4 sre = mfma(P, Hre, z4), sim = mfma(P, Him, z4);
    re[0] = g == 0 ? sre[0] : re[0];
    im[0] = g == 0 ? sim[0] : im[0];
#pragma unroll
    for (int r = 0; r < 4; r++) P[r] = pk(re[r] * twc[r] - im[r] * tws[r], im[r] * twc[r] + re[r] * tws[r]);
    const f32x4 y0 = mfma(P, E0, z4), y1 = mfma(P, E1, z4);
    v4u h;
    h[0] = pk(blo(x0.x) + y0[0], bhi(x0.x) + y0[1]); h[1] = pk(blo(x0.y) + y0[2], bhi(x0.y) + y0[3]);
    h[2] = pk(blo(x1.x) + y1[0], bhi(x1.x) + y1[1]); h[3] = pk(blo(x1.y) + y1[2], bhi(x1.y) + y1[3]);
    float s = 0.f, q = 0.f;
#pragma unroll
    for (int i = 0; i < 4; i++) { s = dot2(h[i], 0x3F803F80u, s); q = dot2(h[i], h[i], q); }
    wavesum2(s, q);
    const float mean = s * (1.f / D);
    const float var = fmaxf(q * (1.f / D) - mean * mean, 0.f);
    const float r1 = __builtin_amdgcn_rsqf(var + 1e-5f);
    const float sc = r1 * __builtin_amdgcn_rsqf(var * r1 * r1 + 1e-5f);
    const float mo = -mean * sc;
    v2u n0, n1;
    n0.x = pk(blo(h[0]) * sc + mo, bhi(h[0]) * sc + mo); n0.y = pk(blo(h[1]) * sc + mo, bhi(h[1]) * sc + mo);
    n1.x = pk(blo(h[2]) * sc + mo, bhi(h[2]) * sc + mo); n1.y = pk(blo(h[3]) * sc + mo, bhi(h[3]) * sc + mo);
    *(LAS v2u*)(L + L_Q + w * 1088 + 32 * lo + 8 * g) = n0;
    *(LAS v2u*)(L + L_Q + w * 1088 + 512 + 32 * lo + 8 * g) = n1;
    *(LAS v2u*)(L + L_X + w * 1024 + 32 * lo + 8 * g) = v2u{clean(h[0]), clean(h[1])};
    *(LAS v2u*)(L + L_X + w * 1024 + 512 + 32 * lo + 8 * g) = v2u{clean(h[2]), clean(h[3])};
    LBAR();
    LAS unsigned char* tp = L + L_Q + (lo >> 2) * 1088 + 128 * w + 32 * g + 8 * (lo & 3);
    const v2u t0 = trread(tp), t1 = trread(tp + 4 * 1088);
    const v4u hv = *(LAS v4u*)(L + L_X + w * 1024 + lane * 16);
    const int pos = 64 * w + 16 * ((lane >> 1) & 3) + 2 * (lane >> 3) + (lane & 1);
    const v4u hn = {clean(t0.x), clean(t0.y), clean(t1.x), clean(t1.y)};
    __builtin_amdgcn_raw_buffer_store_b128(hn, rs, OFF_HN + c * 8192 + pos * 16, 0, SC1);
    __builtin_amdgcn_raw_buffer_store_b128(hv, rs, OFF_H + (256 * w + c) * 1024 + lane * 16, 0, SC1);
  }
  ST(8);
  SBAR;
  const int f0 = 64 * fg + 16 * (w & 3) + 8 * (lane & 1) + 2 * (w >> 2) + ((lane >> 1) & 1);
  const bool fok = f0 < F;
  const float bia0 = blo(b1[fok ? f0 : 0]), bia1 = blo(b1[fok ? f0 + 4 : 0]);
  LBAR();
  DRAIN();
  ST(9);
  if (g1on) {
    const int ho = OFF_HN + (32 * w + g) * 8192 + (48 * db + lo) * 16;
    v4u A[2][8];
#pragma unroll
    for (int st = 0; st < 2; st++)
#pragma unroll
      for (int j = 0; j < 8; j++) A[st][j] = __builtin_amdgcn_raw_buffer_load_b128(rs, ho + st * 256, j * 32768, SC1);
    SBAR;
#pragma unroll
    for (int st = 0; st < 3; st++) {
      if (st < nst) {
        f32x4 acc[4];
        for (int sp = 0; sp < SPIN_CAP; sp++) {
          u32 mx = 0;
#pragma unroll
          for (int cc = 0; cc < 4; cc++) acc[cc] = f32x4{0.f, 0.f, 0.f, 0.f};
#pragma unroll
          for (int j = 0; j < 8; j++) {
            const v4u a = A[st & 1][j];
#pragma unroll
            for (int cc = 0; cc < 4; cc++) acc[cc] = mfma(W1[j][cc], a, acc[cc]);
            mx = umax3(umax3(mx, a.x, a.y), a.z, a.w);
          }
          if (__all(mx != ~0u)) break;
          MEMF;
#pragma unroll
          for (int j = 0; j < 8; j++) A[st & 1][j] = __builtin_amdgcn_raw_buffer_load_b128(rs, ho + st * 256, j * 32768, SC1);
        }
        if (st == 0 && nst == 3) {
#pragma unroll
          for (int j = 0; j < 8; j++) A[0][j] = __builtin_amdgcn_raw_buffer_load_b128(rs, ho + 512, j * 32768, SC1);
        }
        LAS unsigned char* R = L + st * 33792;
#pragma unroll
        for (int cc = 0; cc < 4; cc++) *(LAS f32x4*)(R + w * 4224 + (cc >> 1) * 1024 + (cc & 1) * 2176 + lane * 16) = acc[cc];
        LBAR();
        LAS unsigned char* rp = R + (w >> 2) * 1024 + ((lane >> 1) & 1) * 2176 + ((w & 3) * 16 + (lane >> 2)) * 16 + (lane & 1) * 8;
        f32x2 v = {bia0, bia1};
#pragma unroll
        for (int ww = 0; ww < 8; ww++) v += *(LAS f32x2*)(rp + ww * 4224);
        float o2[2];
#pragma unroll
        for (int e = 0; e < 2; e++) {
          const float u = v[e];
          const float z = u * (u * u * (-0.044715f * 2.f * 0.7978845608f * 1.4426950409f) + (-2.f * 0.7978845608f * 1.4426950409f));
          o2[e] = u * __builtin_amdgcn_rcpf(1.f + __builtin_amdgcn_exp2f(z));
        }
        const u32 uo = fok ? clean(pk(o2[0], o2[1])) : 0u;
        v4u ov;
        ov.x = uo;
        ov.y = (u32)__builtin_amdgcn_update_dpp(0, (int)uo, 0x55, 0xf, 0xf, true);
        ov.z = (u32)__builtin_amdgcn_update_dpp(0, (int)uo, 0xAA, 0xf, 0xf, true);
        ov.w = (u32)__builtin_amdgcn_update_dpp(0, (int)uo, 0xFF, 0xf, 0xf, true);
        if ((lane & 3) == 0) __builtin_amdgcn_raw_buffer_store_b128(ov, rs, OFF_U + ((3 * db + st) * 44 + 2 * fg) * 1024 + t * 4, 0, SC1);
      }
    }
  }
  ST(10);
  SBAR;
  {
    const int d2 = 2 * (xc >> 1) + (xi >> 4), sb = 16 * (xc & 1) + (xi & 15);
    const int j0 = w < 4 ? 6 * w : 4 + 5 * w;
    const bool six = w < 4;
    v4u W2[6][4];
    {
      const int vo = 16 * g * (S * 2) + 128 * sb + 8 * lo;
#pragma unroll
      for (int j = 0; j < 6; j++) {
        if (j < 5 || six) {
          const int jg = j0 + j;
          const int fb = 64 * (jg >> 1) + 2 * (jg & 1);
          v2u r[8];
#pragma unroll
          for (int e = 0; e < 8; e++) r[e] = __builtin_amdgcn_raw_buffer_load_b64(rw2, vo + (fb + 4 * (e & 3) + (e >> 2)) * (S * 2), 0, 0);
          transpose8x4(r, W2[j]);
        }
      }
    }
    const int sl = t >> 3, q8 = t & 7;
    const int so = 64 * sb + 4 * (sl & 15) + (sl >> 4);
    const float bias2 = blo(b2[so]);
    LBAR();
    DRAIN();
    ST(11);
    const int uo = OFF_U + (4 * d2 * 44 + j0) * 1024 + lane * 16;
    v4u A[4][6];
#pragma unroll
    for (int st = 0; st < 4; st++)
#pragma unroll
      for (int j = 0; j < 6; j++)
        if (j < 5 || six) A[st][j] = __builtin_amdgcn_raw_buffer_load_b128(rs, uo, (st * 44 + j) * 1024, SC1);
    const int hoff = OFF_H + so * 1024 + d2 * 128 + q8 * 16;
    v4u hv = __builtin_amdgcn_raw_buffer_load_b128(rs, hoff, 0, SC1);
    SBAR;
    f32x2 res[4];
#pragma unroll
    for (int st = 0; st < 4; st++) {
      f32x4 acc[4];
      for (int sp = 0; sp < SPIN_CAP; sp++) {
        u32 mx = 0;
#pragma unroll
        for (int cc = 0; cc < 4; cc++) acc[cc] = f32x4{0.f, 0.f, 0.f, 0.f};
#pragma unroll
        for (int j = 0; j < 6; j++) {
          if (j < 5 || six) {
            const v4u a = A[st][j];
#pragma unroll
            for (int cc = 0; cc < 4; cc++) acc[cc] = mfma(a, W2[j][cc], acc[cc]);
            mx = umax3(umax3(mx, a.x, a.y), a.z, a.w);
          }
        }
        if (__all(mx != ~0u)) break;
        MEMF;
#pragma unroll
        for (int j = 0; j < 6; j++)
          if (j < 5 || six) A[st][j] = __builtin_amdgcn_raw_buffer_load_b128(rs, uo, (st * 44 + j) * 1024, SC1);
      }
      LAS unsigned char* R = L + st * 32768;
#pragma unroll
      for (int cc = 0; cc < 4; cc++) *(LAS f32x4*)(R + w * 4096 + cc * 1024 + g * 256 + ((lo ^ (4 * g)) * 16)) = acc[cc];
      LBAR();
      LAS unsigned char* rp = R + (sl >> 4) * 1024 + (q8 >> 1) * 256 + (((sl & 15) ^ (4 * (q8 >> 1))) * 16) + (q8 & 1) * 8;
      f32x2 v = {bias2, bias2};
#pragma unroll
      for (int ww = 0; ww < 8; ww++) v += *(LAS f32x2*)(rp + ww * 4096);
      res[st] = v;
    }
    for (int sp = 0; sp < SPIN_CAP; sp++) {
      if (__all(max(umax3(hv.x, hv.y, hv.z), hv.w) != ~0u)) break;
      MEMF;
      hv = __builtin_amdgcn_raw_buffer_load_b128(rs, hoff, 0, SC1);
    }
    v4u o;
#pragma unroll
    for (int st = 0; st < 4; st++) o[st] = pk(blo(hv[st]) + res[st][0], bhi(hv[st]) + res[st][1]);
    *(v4u*)(out + so * D + d2 * 64 + q8 * 8) = o;
  }
  ST(12);
}

extern "C" void kernel_launch(void* const* d_in, const int* in_sizes, int n_in, void* d_out, int out_size, void* d_ws, size_t ws_size,
                              hipStream_t stream) {
  (void)hipFuncSetAttribute((const void*)fused_kernel, hipFuncAttributeMaxDynamicSharedMemorySize, LDS_BYTES);
  fused_kernel<<<256, 512, LDS_BYTES, stream>>>((const u16*)d_in[0], (const u16*)d_in[1], (const u16*)d_in[2], (const u16*)d_in[4],
                                                (const u16*)d_in[5], (const u16*)d_in[6], (const u16*)d_in[7], (u16*)d_out,
                                                (unsigned char*)d_ws, (const u32*)d_in[3]);
}
```

```cpp
#include <hip/hip_runtime.h>
#include <cstdio>

typedef unsigned short u16;
typedef unsigned u32;
typedef u32 v4u __attribute__((ext_vector_type(4)));
typedef u32 v2u __attribute__((ext_vector_type(2)));
typedef short v4s __attribute__((ext_vector_type(4)));
typedef float f32x2 __attribute__((ext_vector_type(2)));
typedef float f32x4 __attribute__((ext_vector_type(4)));
typedef __bf16 bf16x2 __attribute__((ext_vector_type(2)));
typedef __bf16 bf16x8 __attribute__((ext_vector_type(8)));
#define LAS __attribute__((address_space(3)))
#define GAS __attribute__((address_space(1)))
#define DEV __device__ __forceinline__

constexpr int S = 2048, D = 512, DF = 257, F = 1376;
constexpr int OFF_X1 = 2 << 20, OFF_Y1 = 6 << 20, OFF_HN = 8 << 20, OFF_H = 10 << 20, OFF_U = 12 << 20, WS_USED = 14 << 20;
constexpr int SC1 = 16;
constexpr int SPIN_CAP = 1 << 16;
constexpr int CODE_LINES = 128;

constexpr int L_X = 0;
constexpr int L_P = 8192;
constexpr int L_Q = L_P + 9216;
constexpr int L_R = L_Q + 8704;
constexpr int L_END = L_R + 8320;
constexpr int LDS_BYTES = 131072 + 1024;

DEV float asf(u32 v) { return __builtin_bit_cast(float, v); }
DEV u32 asu(float v) { return __builtin_bit_cast(u32, v); }
DEV u32 pk(float lo, float hi) { f32x2 v = {lo, hi}; return __builtin_bit_cast(u32, __builtin_convertvector(v, bf16x2)); }
DEV float blo(u32 v) { return asf(v << 16); }
DEV float bhi(u32 v) { return asf(v & 0xFFFF0000u); }
DEV f32x4 mfma(v4u a, v4u b, f32x4 c) {
  return __builtin_amdgcn_mfma_f32_16x16x32_bf16(__builtin_bit_cast(bf16x8, a), __builtin_bit_cast(bf16x8, b), c, 0, 0, 0);
}
DEV float dot2(u32 a, u32 b, float c) {
  return __builtin_amdgcn_fdot2_f32_bf16(__builtin_bit_cast(bf16x2, a), __builtin_bit_cast(bf16x2, b), c, false);
}
DEV float fsin(float rev) { return __builtin_amdgcn_sinf(rev); }
DEV float fcos(float rev) { return __builtin_amdgcn_cosf(rev); }
DEV v2u trread(LAS unsigned char* p) { return __builtin_bit_cast(v2u, __builtin_amdgcn_ds_read_tr16_b64_v4i16((LAS v4s*)p)); }
DEV u32 umax3(u32 a, u32 b, u32 c) { return max(max(a, b), c); }
DEV u32 clean(u32 v) { return min(v, 0xFFFFFFFEu); }
template <int CTRL, int RM> DEV float dppadd(float v) {
  return v + asf((u32)__builtin_amdgcn_update_dpp(0, (int)asu(v), CTRL, RM, 0xf, true));
}
DEV void wavesum2(float& s, float& q) {
  auto sw = __builtin_amdgcn_permlane32_swap(asu(s), asu(q), false, false);
  u32 a = sw[0], b = sw[1];
  float e = asf(a) + asf(b);
  e = dppadd<0x128, 0xf>(e);
  e = dppadd<0x124, 0xf>(e);
  e = dppadd<0x122, 0xf>(e);
  e = dppadd<0x121, 0xf>(e);
  e = dppadd<0x142, 0xa>(e);
  s = asf((u32)__builtin_amdgcn_readlane((int)asu(e), 31));
  q = asf((u32)__builtin_amdgcn_readlane((int)asu(e), 63));
}
#define MEMF asm volatile("" ::: "memory")
#define SBAR __builtin_amdgcn_sched_barrier(0)
#define LBAR() do { asm volatile("s_waitcnt lgkmcnt(0)" ::: "memory"); __builtin_amdgcn_s_barrier(); MEMF; } while (0)
#define DRAIN() asm volatile("s_waitcnt vmcnt(0)" ::: "memory")

#define ST(i)

struct __attribute__((packed, aligned(2))) U16x8 { v4u v; };

DEV void transpose8x4(const v2u (&r)[8], v4u (&f)[4]) {
#pragma unroll
  for (int p = 0; p < 4; p++) {
    f[0][p] = __builtin_amdgcn_perm(r[2 * p + 1].x, r[2 * p].x, 0x05040100);
    f[1][p] = __builtin_amdgcn_perm(r[2 * p + 1].x, r[2 * p].x, 0x07060302);
    f[2][p] = __builtin_amdgcn_perm(r[2 * p + 1].y, r[2 * p].y, 0x05040100);
    f[3][p] = __builtin_amdgcn_perm(r[2 * p + 1].y, r[2 * p].y, 0x07060302);
  }
}

__global__ __launch_bounds__(512, 2) void fused_kernel(const u16* __restrict__ x, const u16* __restrict__ wr, const u16* __restrict__ wi,
                                                       const u16* __restrict__ w1, const u16* __restrict__ b1, const u16* __restrict__ w2,
                                                       const u16* __restrict__ b2, u16* __restrict__ out, unsigned char* ws) {
  extern __shared__ __attribute__((aligned(16))) unsigned char smem_[];
  LAS unsigned char* L = (LAS unsigned char*)smem_;
  ST(0);
  const int t = threadIdx.x, lane = t & 63, lo = lane & 15, g = lane >> 4;
  const int w = __builtin_amdgcn_readfirstlane(t >> 6);
  const int b = blockIdx.x, c = (b & 7) * 32 + (b >> 3);
  const __amdgpu_buffer_rsrc_t rs = __builtin_amdgcn_make_buffer_rsrc((void*)ws, 0, WS_USED, 0x00020000);
  const __amdgpu_buffer_rsrc_t rw1 = __builtin_amdgcn_make_buffer_rsrc((void*)w1, 0, S * F * 2, 0x00020000);
  const __amdgpu_buffer_rsrc_t rw2 = __builtin_amdgcn_make_buffer_rsrc((void*)w2, 0, S * F * 2, 0x00020000);

  const v4u xv = *(const v4u*)(x + (256 * w + c) * D + lane * 8);
  const u32 tag = __builtin_amdgcn_raw_buffer_load_b32(rs, 0, 0, SC1) + 1u;
  u32 pf = 0;
  if (t < CODE_LINES) {
    unsigned long long pc;
    asm volatile("s_getpc_b64 %0" : "=s"(pc));
    pf = *(const GAS u32*)((pc & ~127ull) + t * 128);
  }
  const int k1B = c >> 5, blkB = c & 31;
  const v4u wv = ((const U16x8*)(((t >> 8) ? wi : wr) + (k1B + 8 * (t & 255)) * DF + 8 * blkB + 1))->v;

  const int xc = b & 7, xi = b >> 3;
  const int q1 = xc >> 1, tl = q1 * 30 + (q1 ? 1 : 0) + xi;
  const bool g1on = xi < (q1 ? 30 : 31);
  const int db = tl / 11, fg = (xc & 1) * 11 + tl % 11;
  const int nst = db == 10 ? 2 : 3;
  {
    const v4u sent = {~0u, ~0u, ~0u, ~0u};
    __builtin_amdgcn_raw_buffer_store_b128(sent, rs, OFF_Y1 + c * 8192 + t * 16, 0, SC1);
    __builtin_amdgcn_raw_buffer_store_b128(sent, rs, OFF_HN + c * 8192 + t * 16, 0, SC1);
    __builtin_amdgcn_raw_buffer_store_b128(sent, rs, OFF_H + (256 * w + c) * 1024 + lane * 16, 0, SC1);
    if (g1on && (w >> 1) < nst)
      __builtin_amdgcn_raw_buffer_store_b128(sent, rs, OFF_U + ((3 * db + (w >> 1)) * 44 + 2 * fg) * 1024 + (t & 127) * 16, 0, SC1);
  }
  SBAR;

  const float flo = (float)lo, fg4 = (float)(4 * g);
  v4u F32re, F32im;
#pragma unroll
  for (int p = 0; p < 4; p++) {
    const float a0 = fg4 + (float)(16 * (p >> 1) + 2 * (p & 1));
    const float r0 = a0 * flo * (1.f / 32), r1 = (a0 + 1.f) * flo * (1.f / 32);
    F32re[p] = pk(fcos(r0), fcos(r1));
    const u32 im = pk(-fsin(r0), -fsin(r1));
    F32im[p] = lo == 0 ? 0xBF803F80u : im;
  }
  float twc[4], tws[4];
  v4u F16re, F16im, SPre, SPim, Sre, Sim;
  const float szero = ((lo >> 3) == (g >> 1)) ? 1.f : 0.f;
#pragma unroll
  for (int r = 0; r < 4; r++) {
    const float bb = fg4 + (float)r;
    const float th = bb * flo * (1.f / 512);
    twc[r] = fcos(th); tws[r] = fsin(th);
    const float ph = bb * flo * (1.f / 16);
    const float cc = fcos(ph), ss = fsin(ph);
    F16re[r] = pk(cc, ss); F16im[r] = pk(-ss, cc);
    const float ps = bb * (flo + 1.f) * (1.f / 32);
    const u32 sp = pk(fcos(ps), -fsin(ps));
    SPre[r] = (lo & 1) ? (sp & 0xFFFFu) : (sp << 16);
    SPim[r] = (lo & 1) ? (sp >> 16) : (sp & 0xFFFF0000u);
    const float al = (float)((lo & 7) * (256 * (4 * (g & 1) + r) + c)) * (1.f / 2048);
    const float ca = fcos(al) * szero, sa = fsin(al) * szero;
    Sre[r] = pk(ca, sa); Sim[r] = pk(-sa, ca);
  }
  const int col0 = lo == 0 ? 64 * g + 15 : (g < 2 ? lo + 128 * g - 1 : 511 - lo - 128 * g);
  const int cstep = lo == 0 ? 16 : (g < 2 ? 32 : -32);
  SBAR;

  *(LAS v4u*)(L + L_X + w * 1024 + lane * 16) = xv;
  ST(1);
  {
    float s = 0.f, q = 0.f;
#pragma unroll
    for (int i = 0; i < 4; i++) { s = dot2(xv[i], 0x3F803F80u, s); q = dot2(xv[i], xv[i], q); }
    const v2u xa0 = trread(L + L_X + w * 1024 + lane * 8), xa1 = trread(L + L_X + w * 1024 + 512 + lane * 8);
    const v4u xa = {xa0.x, xa0.y, xa1.x, xa1.y};
    const f32x4 z4 = {0.f, 0.f, 0.f, 0.f};
    f32x4 Tre = mfma(xa, F32re, z4), Tim = mfma(xa, F32im, z4);
    wavesum2(s, q);
    const float mean = s * (1.f / D);
    const float var = fmaxf(q * (1.f / D) - mean * mean, 0.f);
    const float rstd = __builtin_amdgcn_rsqf(var + 1e-5f);
    const float msub = lo == 0 ? 32.f * mean : 0.f;
    v4u P;
#pragma unroll
    for (int r = 0; r < 4; r++) {
      const float wc = twc[r] * rstd, wsn = tws[r] * rstd;
      const float re = Tre[r] - msub, im = Tim[r];
      P[r] = pk(re * wc + im * wsn, im * wc - re * wsn);
    }
    const u32 sg = lo >= 8 ? 0x80008000u : 0u;
    const v4u F16imc = {F16im[0] ^ sg, F16im[1] ^ sg, F16im[2] ^ sg, F16im[3] ^ sg};
    const f32x4 Zre = mfma(F16re, P, z4), Zim = mfma(F16imc, P, z4), Yre = mfma(SPre, P, z4), Yim = mfma(SPim, P, z4);
    LAS u32* Zb = (LAS u32*)(L + L_P);
#pragma unroll
    for (int r = 0; r < 4; r++) {
      const u32 zg = pk(Zre[r], Zim[r]), zs = pk(Yre[r], Yim[r]);
      Zb[w * 260 + col0 + r * cstep] = lo == 0 ? zs : zg;
    }
    *(LAS v4u*)(L + L_R + t * 16) = wv;
    asm volatile("" ::"v"(pf));
    DRAIN();
    LBAR();
    v4u za;
#pragma unroll
    for (int j = 0; j < 4; j++) za[j] = Zb[(4 * (g & 1) + j) * 260 + 32 * w + 16 * (g >> 1) + lo];
    const f32x4 Ore = mfma(za, Sre, z4), Oim = mfma(za, Sim, z4);
    const int cB = (lo & 7) * 32 + 4 * w + 2 * (lo >> 3) + (g >> 1);
    const int o = OFF_X1 + c * 16384 + ((((cB >> 2) + c) & 63) << 7) + (cB & 3) * 32 + (g & 1) * 16;
    const v4u s0 = {pk(Ore[0], Oim[0]), tag, pk(Ore[1], Oim[1]), tag}, s1 = {pk(Ore[2], Oim[2]), tag, pk(Ore[3], Oim[3]), tag};
    __builtin_amdgcn_raw_buffer_store_b128(s0, rs, o, 0, SC1);
    __builtin_amdgcn_raw_buffer_store_b128(s1, rs, o + 8192, 0, SC1);
  }
  ST(2);
  SBAR;

  v4u W1[8][4];
  {
    const int vo = (32 * w + g) * (F * 2) + min(128 * fg + 8 * lo, F * 2 - 8);
#pragma unroll
    for (int j = 0; j < 8; j++) {
      v2u r[8];
#pragma unroll
      for (int i = 0; i < 8; i++) r[i] = __builtin_amdgcn_raw_buffer_load_b64(rw1, vo, (256 * i + 4 * j) * (F * 2), 0);
      transpose8x4(r, W1[j]);
    }
  }
  float t2c[4], t2s[4], fWr[4], fWi[4];
#pragma unroll
  for (int r = 0; r < 4; r++) {
    const float th = (fg4 + (float)r) * flo * (1.f / 256);
    t2c[r] = fcos(th); t2s[r] = fsin(th);
    const int k2 = lo + 16 * (4 * g + r);
    fWr[r] = blo(*(LAS u16*)(L + L_R + k2 * 16 + w * 2)) * (2.f / (S * D));
    fWi[r] = blo(*(LAS u16*)(L + L_R + 4096 + k2 * 16 + w * 2)) * (2.f / (S * D));
  }
  SBAR;
  DRAIN();
  ST(3);
  const int uB = t >> 1, hfB = t & 1;
  const int n2B = 16 * (4 * (uB >> 6) + (uB & 3)) + ((uB >> 2) & 15);
  {
    const int o = OFF_X1 + n2B * 16384 + ((((c >> 2) + n2B) & 63) << 7) + (c & 3) * 32 + hfB * 16;
    v4u a0, a1;
    for (int sp = 0; sp < SPIN_CAP; sp++) {
      MEMF;
      a0 = __builtin_amdgcn_raw_buffer_load_b128(rs, o, 0, SC1);
      a1 = __builtin_amdgcn_raw_buffer_load_b128(rs, o + 8192, 0, SC1);
      const u32 bad = (a0.y ^ tag) | (a0.w ^ tag) | (a1.y ^ tag) | (a1.w ^ tag);
      if (__all(bad == 0)) break;
    }
    ST(4);
    LAS u32* Bb = (LAS u32*)(L + L_Q);
    Bb[hfB * 1040 + uB] = a0.x; Bb[hfB * 1040 + 256 + uB] = a0.z; Bb[hfB * 1040 + 512 + uB] = a1.x; Bb[hfB * 1040 + 768 + uB] = a1.z;
    LBAR();
    if (b == 0 && t == 0) __builtin_amdgcn_raw_buffer_store_b32(tag, rs, 0, 0, SC1);
    LAS v4u* colp = (LAS v4u*)(Bb + (w >> 2) * 1040 + (w & 3) * 256) + lane;
    v4u P = *colp;
    const f32x4 z4 = {0.f, 0.f, 0.f, 0.f};
    f32x4 re = mfma(P, F16re, z4), im = mfma(P, F16im, z4);
#pragma unroll
    for (int r = 0; r < 4; r++) P[r] = pk(re[r] * t2c[r] + im[r] * t2s[r], im[r] * t2c[r] - re[r] * t2s[r]);
    re = mfma(F16re, P, z4); im = mfma(F16im, P, z4);
#pragma unroll
    for (int r = 0; r < 4; r++) P[r] = pk(re[r] * fWi[r] + im[r] * fWr[r], re[r] * fWr[r] - im[r] * fWi[r]);
    im = mfma(P, F16re, z4); re = mfma(P, F16im, z4);
#pragma unroll
    for (int r = 0; r < 4; r++) P[r] = pk(im[r] * t2c[r] + re[r] * t2s[r], re[r] * t2c[r] - im[r] * t2s[r]);
    im = mfma(F16re, P, z4); re = mfma(F16im, P, z4);
#pragma unroll
    for (int r = 0; r < 4; r++) P[r] = clean(pk(re[r], im[r]));
    *colp = P;
    LBAR();
    const v4u y = {Bb[hfB * 1040 + uB], Bb[hfB * 1040 + 256 + uB], Bb[hfB * 1040 + 512 + uB], Bb[hfB * 1040 + 768 + uB]};
    __builtin_amdgcn_raw_buffer_store_b128(y, rs, OFF_Y1 + c * 8192 + ((((n2B >> 2) + c) & 63) << 7) + (n2B & 3) * 32 + hfB * 16, 0, SC1);
  }
  ST(5);
  SBAR;
  v4u Ire, Iim, Gre, Gim, Hre, Him, E0, E1;
#pragma unroll
  for (int r = 0; r < 4; r++) {
    const float al = (float)((4 * (g & 1) + r) * (256 * (lo & 7) + c)) * (1.f / 2048);
    const float ca = fcos(al) * szero, sa = fsin(al) * szero;
    Ire[r] = pk(ca, -sa); Iim[r] = pk(sa, ca);
    Gre[r] = F16re[r] ^ (g < 2 ? 0x80000000u : 0u);
    Gim[r] = F16im[r] ^ (g < 2 ? 0x00008000u : 0x80008000u);
    const float ps = flo * (fg4 + (float)(r + 1)) * (1.f / 32);
    const float wg = (r == 3 && g == 3) ? 0.5f : 1.f;
    const u32 sp = pk(fcos(ps) * wg, -fsin(ps));
    Hre[r] = (r & 1) ? sp : 0u;
    Him[r] = (r & 1) ? 0u : sp;
    const float be = flo * (fg4 + (float)r) * (1.f / 32);
    u32 e = pk(fcos(be), -fsin(be));
    if (r == 0) e = g == 0 ? ((lo & 1) ? 0xBF803F80u : 0x3F803F80u) : e;
    E0[r] = e;
    E1[r] = (r & 1) ? e ^ 0x80008000u : e;
  }
  SBAR;
  DRAIN();
  ST(6);
  {
    const int pB = (lane >> 3) * 32 + 4 * w + ((lane >> 1) & 3);
    const int o = OFF_Y1 + pB * 8192 + ((((c >> 2) + pB) & 63) << 7) + (c & 3) * 32 + (lane & 1) * 16;
    v4u a;
    for (int sp = 0; sp < SPIN_CAP; sp++) {
      MEMF;
      a = __builtin_amdgcn_raw_buffer_load_b128(rs, o, 0, SC1);
      if (__all(max(umax3(a.x, a.y, a.z), a.w) != ~0u)) break;
    }
    ST(7);
    LAS u32* Cb = (LAS u32*)(L + L_P) + w * 288;
    *(LAS v4u*)(Cb + (lane >> 3) * 36 + (lane & 7) * 4) = a;
    v4u P;
#pragma unroll
    for (int j = 0; j < 4; j++) P[j] = Cb[(4 * (g & 1) + j) * 36 + 16 * (g >> 1) + lo];
    const f32x4 z4 = {0.f, 0.f, 0.f, 0.f};
    f32x4 re = mfma(P, Ire, z4), im = mfma(P, Iim, z4);
    LAS u32* Qb = (LAS u32*)(L + L_R);
#pragma unroll
    for (int r = 0; r < 4; r++) P[r] = pk(re[r], im[r]);
    *(LAS v4u*)(Qb + (lo & 7) * 260 + 32 * w + 16 * (lo >> 3) + 4 * g) = P;
    const v2u x0 = *(LAS v2u*)(L + L_X + w * 1024 + 32 * lo + 8 * g), x1 = *(LAS v2u*)(L + L_X + w * 1024 + 512 + 32 * lo + 8 * g);
    LBAR();
#pragma unroll
    for (int r = 0; r < 4; r++) P[r] = Qb[w * 260 + col0 + r * cstep];
    re = mfma(P, Gre, z4); im = mfma(P, Gim, z4);
    const f32x4 sre = mfma(P, Hre, z4), sim = mfma(P, Him, z4);
    re[0] = g == 0 ? sre[0] : re[0];
    im[0] = g == 0 ? sim[0] : im[0];
#pragma unroll
    for (int r = 0; r < 4; r++) P[r] = pk(re[r] * twc[r] - im[r] * tws[r], im[r] * twc[r] + re[r] * tws[r]);
    const f32x4 y0 = mfma(P, E0, z4), y1 = mfma(P, E1, z4);
    v4u h;
    h[0] = pk(blo(x0.x) + y0[0], bhi(x0.x) + y0[1]); h[1] = pk(blo(x0.y) + y0[2], bhi(x0.y) + y0[3]);
    h[2] = pk(blo(x1.x) + y1[0], bhi(x1.x) + y1[1]); h[3] = pk(blo(x1.y) + y1[2], bhi(x1.y) + y1[3]);
    float s = 0.f, q = 0.f;
#pragma unroll
    for (int i = 0; i < 4; i++) { s = dot2(h[i], 0x3F803F80u, s); q = dot2(h[i], h[i], q); }
    wavesum2(s, q);
    const float mean = s * (1.f / D);
    const float var = fmaxf(q * (1.f / D) - mean * mean, 0.f);
    const float r1 = __builtin_amdgcn_rsqf(var + 1e-5f);
    const float sc = r1 * __builtin_amdgcn_rsqf(var * r1 * r1 + 1e-5f);
    const float mo = -mean * sc;
    v2u n0, n1;
    n0.x = pk(blo(h[0]) * sc + mo, bhi(h[0]) * sc + mo); n0.y = pk(blo(h[1]) * sc + mo, bhi(h[1]) * sc + mo);
    n1.x = pk(blo(h[2]) * sc + mo, bhi(h[2]) * sc + mo); n1.y = pk(blo(h[3]) * sc + mo, bhi(h[3]) * sc + mo);
    *(LAS v2u*)(L + L_Q + w * 1088 + 32 * lo + 8 * g) = n0;
    *(LAS v2u*)(L + L_Q + w * 1088 + 512 + 32 * lo + 8 * g) = n1;
    *(LAS v2u*)(L + L_X + w * 1024 + 32 * lo + 8 * g) = v2u{clean(h[0]), clean(h[1])};
    *(LAS v2u*)(L + L_X + w * 1024 + 512 + 32 * lo + 8 * g) = v2u{clean(h[2]), clean(h[3])};
    LBAR();
    LAS unsigned char* tp = L + L_Q + (lo >> 2) * 1088 + 128 * w + 32 * g + 8 * (lo & 3);
    const v2u t0 = trread(tp), t1 = trread(tp + 4 * 1088);
    const v4u hv = *(LAS v4u*)(L + L_X + w * 1024 + lane * 16);
    const int pos = 64 * w + 16 * ((lane >> 1) & 3) + 2 * (lane >> 3) + (lane & 1);
    const v4u hn = {clean(t0.x), clean(t0.y), clean(t1.x), clean(t1.y)};
    __builtin_amdgcn_raw_buffer_store_b128(hn, rs, OFF_HN + c * 8192 + pos * 16, 0, SC1);
    __builtin_amdgcn_raw_buffer_store_b128(hv, rs, OFF_H + (256 * w + c) * 1024 + lane * 16, 0, SC1);
  }
  ST(8);
  SBAR;
  const int f0 = 64 * fg + 16 * (w & 3) + 8 * (lane & 1) + 2 * (w >> 2) + ((lane >> 1) & 1);
  const bool fok = f0 < F;
  const float bia0 = blo(b1[fok ? f0 : 0]), bia1 = blo(b1[fok ? f0 + 4 : 0]);
  LBAR();
  DRAIN();
  ST(9);
  if (g1on) {
    const int ho = OFF_HN + (32 * w + g) * 8192 + (48 * db + lo) * 16;
    v4u A[2][8];
#pragma unroll
    for (int st = 0; st < 2; st++)
#pragma unroll
      for (int j = 0; j < 8; j++) A[st][j] = __builtin_amdgcn_raw_buffer_load_b128(rs, ho + st * 256, j * 32768, SC1);
    SBAR;
#pragma unroll
    for (int st = 0; st < 3; st++) {
      if (st < nst) {
        f32x4 acc[4];
        for (int sp = 0; sp < SPIN_CAP; sp++) {
          u32 mx = 0;
#pragma unroll
          for (int cc = 0; cc < 4; cc++) acc[cc] = f32x4{0.f, 0.f, 0.f, 0.f};
#pragma unroll
          for (int j = 0; j < 8; j++) {
            const v4u a = A[st & 1][j];
#pragma unroll
            for (int cc = 0; cc < 4; cc++) acc[cc] = mfma(W1[j][cc], a, acc[cc]);
            mx = umax3(umax3(mx, a.x, a.y), a.z, a.w);
          }
          if (__all(mx != ~0u)) break;
          MEMF;
#pragma unroll
          for (int j = 0; j < 8; j++) A[st & 1][j] = __builtin_amdgcn_raw_buffer_load_b128(rs, ho + st * 256, j * 32768, SC1);
        }
        if (st == 0 && nst == 3) {
#pragma unroll
          for (int j = 0; j < 8; j++) A[0][j] = __builtin_amdgcn_raw_buffer_load_b128(rs, ho + 512, j * 32768, SC1);
        }
        LAS unsigned char* R = L + st * 33792;
#pragma unroll
        for (int cc = 0; cc < 4; cc++) *(LAS f32x4*)(R + w * 4224 + (cc >> 1) * 1024 + (cc & 1) * 2176 + lane * 16) = acc[cc];
        LBAR();
        LAS unsigned char* rp = R + (w >> 2) * 1024 + ((lane >> 1) & 1) * 2176 + ((w & 3) * 16 + (lane >> 2)) * 16 + (lane & 1) * 8;
        f32x2 v = {bia0, bia1};
#pragma unroll
        for (int ww = 0; ww < 8; ww++) v += *(LAS f32x2*)(rp + ww * 4224);
        float o2[2];
#pragma unroll
        for (int e = 0; e < 2; e++) {
          const float u = v[e];
          const float z = u * (u * u * (-0.044715f * 2.f * 0.7978845608f * 1.4426950409f) + (-2.f * 0.7978845608f * 1.4426950409f));
          o2[e] = u * __builtin_amdgcn_rcpf(1.f + __builtin_amdgcn_exp2f(z));
        }
        const u32 uo = fok ? clean(pk(o2[0], o2[1])) : 0u;
        v4u ov;
        ov.x = uo;
        ov.y = (u32)__builtin_amdgcn_update_dpp(0, (int)uo, 0x55, 0xf, 0xf, true);
        ov.z = (u32)__builtin_amdgcn_update_dpp(0, (int)uo, 0xAA, 0xf, 0xf, true);
        ov.w = (u32)__builtin_amdgcn_update_dpp(0, (int)uo, 0xFF, 0xf, 0xf, true);
        if ((lane & 3) == 0) __builtin_amdgcn_raw_buffer_store_b128(ov, rs, OFF_U + ((3 * db + st) * 44 + 2 * fg) * 1024 + t * 4, 0, SC1);
      }
    }
  }
  ST(10);
  SBAR;
  {
    const int d2 = 2 * (xc >> 1) + (xi >> 4), sb = 16 * (xc & 1) + (xi & 15);
    const int j0 = w < 4 ? 6 * w : 4 + 5 * w;
    const bool six = w < 4;
    v4u W2[6][4];
    {
      const int vo = 16 * g * (S * 2) + 128 * sb + 8 * lo;
#pragma unroll
      for (int j = 0; j < 6; j++) {
        if (j < 5 || six) {
          const int jg = j0 + j;
          const int fb = 64 * (jg >> 1) + 2 * (jg & 1);
          v2u r[8];
#pragma unroll
          for (int e = 0; e < 8; e++) r[e] = __builtin_amdgcn_raw_buffer_load_b64(rw2, vo + (fb + 4 * (e & 3) + (e >> 2)) * (S * 2), 0, 0);
          transpose8x4(r, W2[j]);
        }
      }
    }
    const int sl = t >> 3, q8 = t & 7;
    const int so = 64 * sb + 4 * (sl & 15) + (sl >> 4);
    const float bias2 = blo(b2[so]);
    LBAR();
    DRAIN();
    ST(11);
    const int uo = OFF_U + (4 * d2 * 44 + j0) * 1024 + lane * 16;
    v4u A[4][6];
#pragma unroll
    for (int st = 0; st < 4; st++)
#pragma unroll
      for (int j = 0; j < 6; j++)
        if (j < 5 || six) A[st][j] = __builtin_amdgcn_raw_buffer_load_b128(rs, uo, (st * 44 + j) * 1024, SC1);
    const int hoff = OFF_H + so * 1024 + d2 * 128 + q8 * 16;
    v4u hv = __builtin_amdgcn_raw_buffer_load_b128(rs, hoff, 0, SC1);
    SBAR;
    f32x2 res[4];
#pragma unroll
    for (int st = 0; st < 4; st++) {
      f32x4 acc[4];
      for (int sp = 0; sp < SPIN_CAP; sp++) {
        u32 mx = 0;
#pragma unroll
        for (int cc = 0; cc < 4; cc++) acc[cc] = f32x4{0.f, 0.f, 0.f, 0.f};
#pragma unroll
        for (int j = 0; j < 6; j++) {
          if (j < 5 || six) {
            const v4u a = A[st][j];
#pragma unroll
            for (int cc = 0; cc < 4; cc++) acc[cc] = mfma(a, W2[j][cc], acc[cc]);
            mx = umax3(umax3(mx, a.x, a.y), a.z, a.w);
          }
        }
        if (__all(mx != ~0u)) break;
        MEMF;
#pragma unroll
        for (int j = 0; j < 6; j++)
          if (j < 5 || six) A[st][j] = __builtin_amdgcn_raw_buffer_load_b128(rs, uo, (st * 44 + j) * 1024, SC1);
      }
      LAS unsigned char* R = L + st * 32768;
#pragma unroll
      for (int cc = 0; cc < 4; cc++) *(LAS f32x4*)(R + w * 4096 + cc * 1024 + g * 256 + ((lo ^ (4 * g)) * 16)) = acc[cc];
      LBAR();
      LAS unsigned char* rp = R + (sl >> 4) * 1024 + (q8 >> 1) * 256 + (((sl & 15) ^ (4 * (q8 >> 1))) * 16) + (q8 & 1) * 8;
      f32x2 v = {bias2, bias2};
#pragma unroll
      for (int ww = 0; ww < 8; ww++) v += *(LAS f32x2*)(rp + ww * 4096);
      res[st] = v;
    }
    for (int sp = 0; sp < SPIN_CAP; sp++) {
      if (__all(max(umax3(hv.x, hv.y, hv.z), hv.w) != ~0u)) break;
      MEMF;
      hv = __builtin_amdgcn_raw_buffer_load_b128(rs, hoff, 0, SC1);
    }
    v4u o;
#pragma unroll
    for (int st = 0; st < 4; st++) o[st] = pk(blo(hv[st]) + res[st][0], bhi(hv[st]) + res[st][1]);
    *(v4u*)(out + so * D + d2 * 64 + q8 * 8) = o;
  }
  ST(12);
}

extern "C" void kernel_launch(void* const* d_in, const int* in_sizes, int n_in, void* d_out, int out_size, void* d_ws, size_t ws_size,
                              hipStream_t stream) {
  for(int i=0;i<n_in;i++) fprintf(stderr,"in%d %p\n",i,d_in[i]); fprintf(stderr,"out %p ws %p\n",d_out,d_ws);
  (void)hipFuncSetAttribute((const void*)fused_kernel, hipFuncAttributeMaxDynamicSharedMemorySize, LDS_BYTES);
  fused_kernel<<<256, 512, LDS_BYTES, stream>>>((const u16*)d_in[0], (const u16*)d_in[1], (const u16*)d_in[2], (const u16*)d_in[4],
                                                (const u16*)d_in[5], (const u16*)d_in[6], (const u16*)d_in[7], (u16*)d_out,
                                                (unsigned char*)d_ws);
}
```

```cpp
#define CHAN
#define I0 7
#define I1 8
#include <hip/hip_runtime.h>

typedef unsigned short u16;
typedef unsigned u32;
typedef u32 v4u __attribute__((ext_vector_type(4)));
typedef u32 v2u __attribute__((ext_vector_type(2)));
typedef short v4s __attribute__((ext_vector_type(4)));
typedef float f32x2 __attribute__((ext_vector_type(2)));
typedef float f32x4 __attribute__((ext_vector_type(4)));
typedef __bf16 bf16x2 __attribute__((ext_vector_type(2)));
typedef __bf16 bf16x8 __attribute__((ext_vector_type(8)));
#define LAS __attribute__((address_space(3)))
#define GAS __attribute__((address_space(1)))
#define DEV __device__ __forceinline__

constexpr int S = 2048, D = 512, DF = 257, F = 1376;
constexpr int OFF_X1 = 2 << 20, OFF_Y1 = 6 << 20, OFF_HN = 8 << 20, OFF_H = 10 << 20, OFF_U = 12 << 20, WS_USED = 14 << 20;
constexpr int SC1 = 16;
constexpr int SPIN_CAP = 1 << 16;
constexpr int CODE_LINES = 128;

constexpr int L_X = 0;
constexpr int L_P = 8192;
constexpr int L_Q = L_P + 9216;
constexpr int L_R = L_Q + 8704;
constexpr int L_END = L_R + 8320;
constexpr int LDS_BYTES = 131072 + 1024;

DEV float asf(u32 v) { return __builtin_bit_cast(float, v); }
DEV u32 asu(float v) { return __builtin_bit_cast(u32, v); }
DEV u32 pk(float lo, float hi) { f32x2 v = {lo, hi}; return __builtin_bit_cast(u32, __builtin_convertvector(v, bf16x2)); }
DEV float blo(u32 v) { return asf(v << 16); }
DEV float bhi(u32 v) { return asf(v & 0xFFFF0000u); }
DEV f32x4 mfma(v4u a, v4u b, f32x4 c) {
  return __builtin_amdgcn_mfma_f32_16x16x32_bf16(__builtin_bit_cast(bf16x8, a), __builtin_bit_cast(bf16x8, b), c, 0, 0, 0);
}
DEV float dot2(u32 a, u32 b, float c) {
  return __builtin_amdgcn_fdot2_f32_bf16(__builtin_bit_cast(bf16x2, a), __builtin_bit_cast(bf16x2, b), c, false);
}
DEV float fsin(float rev) { return __builtin_amdgcn_sinf(rev); }
DEV float fcos(float rev) { return __builtin_amdgcn_cosf(rev); }
DEV v2u trread(LAS unsigned char* p) { return __builtin_bit_cast(v2u, __builtin_amdgcn_ds_read_tr16_b64_v4i16((LAS v4s*)p)); }
DEV u32 umax3(u32 a, u32 b, u32 c) { return max(max(a, b), c); }
DEV u32 clean(u32 v) { return min(v, 0xFFFFFFFEu); }
template <int CTRL, int RM> DEV float dppadd(float v) {
  return v + asf((u32)__builtin_amdgcn_update_dpp(0, (int)asu(v), CTRL, RM, 0xf, true));
}
DEV void wavesum2(float& s, float& q) {
  auto sw = __builtin_amdgcn_permlane32_swap(asu(s), asu(q), false, false);
  u32 a = sw[0], b = sw[1];
  float e = asf(a) + asf(b);
  e = dppadd<0x128, 0xf>(e);
  e = dppadd<0x124, 0xf>(e);
  e = dppadd<0x122, 0xf>(e);
  e = dppadd<0x121, 0xf>(e);
  e = dppadd<0x142, 0xa>(e);
  s = asf((u32)__builtin_amdgcn_readlane((int)asu(e), 31));
  q = asf((u32)__builtin_amdgcn_readlane((int)asu(e), 63));
}
#define MEMF asm volatile("" ::: "memory")
#define SBAR __builtin_amdgcn_sched_barrier(0)
#define LBAR() do { asm volatile("s_waitcnt lgkmcnt(0)" ::: "memory"); __builtin_amdgcn_s_barrier(); MEMF; } while (0)
#define DRAIN() asm volatile("s_waitcnt vmcnt(0)" ::: "memory")

#define ST(i) do { if ((i) == I0 || (i) == I1) { unsigned long long t_; asm volatile("s_memrealtime %0\n\ts_waitcnt lgkmcnt(0)" : "=s"(t_)::"memory"); if ((i) == I0) c0_ = t_; else c1_ = t_; } } while (0)

struct __attribute__((packed, aligned(2))) U16x8 { v4u v; };

DEV void transpose8x4(const v2u (&r)[8], v4u (&f)[4]) {
#pragma unroll
  for (int p = 0; p < 4; p++) {
    f[0][p] = __builtin_amdgcn_perm(r[2 * p + 1].x, r[2 * p].x, 0x05040100);
    f[1][p] = __builtin_amdgcn_perm(r[2 * p + 1].x, r[2 * p].x, 0x07060302);
    f[2][p] = __builtin_amdgcn_perm(r[2 * p + 1].y, r[2 * p].y, 0x05040100);
    f[3][p] = __builtin_amdgcn_perm(r[2 * p + 1].y, r[2 * p].y, 0x07060302);
  }
}

__global__ __launch_bounds__(512, 2) void fused_kernel(const u16* __restrict__ x, const u16* __restrict__ wr, const u16* __restrict__ wi,
                                                       const u16* __restrict__ w1, const u16* __restrict__ b1, const u16* __restrict__ w2,
                                                       const u16* __restrict__ b2, u16* __restrict__ out, unsigned char* ws) {
  extern __shared__ __attribute__((aligned(16))) unsigned char smem_[];
  LAS unsigned char* L = (LAS unsigned char*)smem_;
  unsigned long long c0_, c1_;
  ST(0);
  const int t = threadIdx.x, lane = t & 63, lo = lane & 15, g = lane >> 4;
  const int w = __builtin_amdgcn_readfirstlane(t >> 6);
  const int b = blockIdx.x, c = (b & 7) * 32 + (b >> 3);
  const __amdgpu_buffer_rsrc_t rs = __builtin_amdgcn_make_buffer_rsrc((void*)ws, 0, WS_USED, 0x00020000);
  const __amdgpu_buffer_rsrc_t rw1 = __builtin_amdgcn_make_buffer_rsrc((void*)w1, 0, S * F * 2, 0x00020000);
  const __amdgpu_buffer_rsrc_t rw2 = __builtin_amdgcn_make_buffer_rsrc((void*)w2, 0, S * F * 2, 0x00020000);

  const v4u xv = *(const v4u*)(x + (256 * w + c) * D + lane * 8);
  const u32 tag = __builtin_amdgcn_raw_buffer_load_b32(rs, 0, 0, SC1) + 1u;
  u32 pf = 0;
  if (t < CODE_LINES) {
    unsigned long long pc;
    asm volatile("s_getpc_b64 %0" : "=s"(pc));
    pf = *(const GAS u32*)((pc & ~127ull) + t * 128);
  }
  const int k1B = c >> 5, blkB = c & 31;
  const v4u wv = ((const U16x8*)(((t >> 8) ? wi : wr) + (k1B + 8 * (t & 255)) * DF + 8 * blkB + 1))->v;

  const int xc = b & 7, xi = b >> 3;
  const int q1 = xc >> 1, tl = q1 * 30 + (q1 ? 1 : 0) + xi;
  const bool g1on = xi < (q1 ? 30 : 31);
  const int db = tl / 11, fg = (xc & 1) * 11 + tl % 11;
  const int nst = db == 10 ? 2 : 3;
  {
    const v4u sent = {~0u, ~0u, ~0u, ~0u};
    __builtin_amdgcn_raw_buffer_store_b128(sent, rs, OFF_Y1 + c * 8192 + t * 16, 0, SC1);
    __builtin_amdgcn_raw_buffer_store_b128(sent, rs, OFF_HN + c * 8192 + t * 16, 0, SC1);
    __builtin_amdgcn_raw_buffer_store_b128(sent, rs, OFF_H + (256 * w + c) * 1024 + lane * 16, 0, SC1);
    if (g1on && (w >> 1) < nst)
      __builtin_amdgcn_raw_buffer_store_b128(sent, rs, OFF_U + ((3 * db + (w >> 1)) * 44 + 2 * fg) * 1024 + (t & 127) * 16, 0, SC1);
  }
  SBAR;

  const float flo = (float)lo, fg4 = (float)(4 * g);
  v4u F32re, F32im;
#pragma unroll
  for (int p = 0; p < 4; p++) {
    const float a0 = fg4 + (float)(16 * (p >> 1) + 2 * (p & 1));
    const float r0 = a0 * flo * (1.f / 32), r1 = (a0 + 1.f) * flo * (1.f / 32);
    F32re[p] = pk(fcos(r0), fcos(r1));
    const u32 im = pk(-fsin(r0), -fsin(r1));
    F32im[p] = lo == 0 ? 0xBF803F80u : im;
  }
  float twc[4], tws[4];
  v4u F16re, F16im, SPre, SPim, Sre, Sim;
  const float szero = ((lo >> 3) == (g >> 1)) ? 1.f : 0.f;
#pragma unroll
  for (int r = 0; r < 4; r++) {
    const float bb = fg4 + (float)r;
    const float th = bb * flo * (1.f / 512);
    twc[r] = fcos(th); tws[r] = fsin(th);
    const float ph = bb * flo * (1.f / 16);
    const float cc = fcos(ph), ss = fsin(ph);
    F16re[r] = pk(cc, ss); F16im[r] = pk(-ss, cc);
    const float ps = bb * (flo + 1.f) * (1.f / 32);
    const u32 sp = pk(fcos(ps), -fsin(ps));
    SPre[r] = (lo & 1) ? (sp & 0xFFFFu) : (sp << 16);
    SPim[r] = (lo & 1) ? (sp >> 16) : (sp & 0xFFFF0000u);
    const float al = (float)((lo & 7) * (256 * (4 * (g & 1) + r) + c)) * (1.f / 2048);
    const float ca = fcos(al) * szero, sa = fsin(al) * szero;
    Sre[r] = pk(ca, sa); Sim[r] = pk(-sa, ca);
  }
  const int col0 = lo == 0 ? 64 * g + 15 : (g < 2 ? lo + 128 * g - 1 : 511 - lo - 128 * g);
  const int cstep = lo == 0 ? 16 : (g < 2 ? 32 : -32);
  SBAR;

  *(LAS v4u*)(L + L_X + w * 1024 + lane * 16) = xv;
  ST(1);
  {
    float s = 0.f, q = 0.f;
#pragma unroll
    for (int i = 0; i < 4; i++) { s = dot2(xv[i], 0x3F803F80u, s); q = dot2(xv[i], xv[i], q); }
    const v2u xa0 = trread(L + L_X + w * 1024 + lane * 8), xa1 = trread(L + L_X + w * 1024 + 512 + lane * 8);
    const v4u xa = {xa0.x, xa0.y, xa1.x, xa1.y};
    const f32x4 z4 = {0.f, 0.f, 0.f, 0.f};
    f32x4 Tre = mfma(xa, F32re, z4), Tim = mfma(xa, F32im, z4);
    wavesum2(s, q);
    const float mean = s * (1.f / D);
    const float var = fmaxf(q * (1.f / D) - mean * mean, 0.f);
    const float rstd = __builtin_amdgcn_rsqf(var + 1e-5f);
    const float msub = lo == 0 ? 32.f * mean : 0.f;
    v4u P;
#pragma unroll
    for (int r = 0; r < 4; r++) {
      const float wc = twc[r] * rstd, wsn = tws[r] * rstd;
      const float re = Tre[r] - msub, im = Tim[r];
      P[r] = pk(re * wc + im * wsn, im * wc - re * wsn);
    }
    const u32 sg = lo >= 8 ? 0x80008000u : 0u;
    const v4u F16imc = {F16im[0] ^ sg, F16im[1] ^ sg, F16im[2] ^ sg, F16im[3] ^ sg};
    const f32x4 Zre = mfma(F16re, P, z4), Zim = mfma(F16imc, P, z4), Yre = mfma(SPre, P, z4), Yim = mfma(SPim, P, z4);
    LAS u32* Zb = (LAS u32*)(L + L_P);
#pragma unroll
    for (int r = 0; r < 4; r++) {
      const u32 zg = pk(Zre[r], Zim[r]), zs = pk(Yre[r], Yim[r]);
      Zb[w * 260 + col0 + r * cstep] = lo == 0 ? zs : zg;
    }
    *(LAS v4u*)(L + L_R + t * 16) = wv;
    asm volatile("" ::"v"(pf));
    DRAIN();
    LBAR();
    v4u za;
#pragma unroll
    for (int j = 0; j < 4; j++) za[j] = Zb[(4 * (g & 1) + j) * 260 + 32 * w + 16 * (g >> 1) + lo];
    const f32x4 Ore = mfma(za, Sre, z4), Oim = mfma(za, Sim, z4);
    const int cB = (lo & 7) * 32 + 4 * w + 2 * (lo >> 3) + (g >> 1);
    const int o = OFF_X1 + c * 16384 + ((((cB >> 2) + c) & 63) << 7) + (cB & 3) * 32 + (g & 1) * 16;
    const v4u s0 = {pk(Ore[0], Oim[0]), tag, pk(Ore[1], Oim[1]), tag}, s1 = {pk(Ore[2], Oim[2]), tag, pk(Ore[3], Oim[3]), tag};
    __builtin_amdgcn_raw_buffer_store_b128(s0, rs, o, 0, SC1);
    __builtin_amdgcn_raw_buffer_store_b128(s1, rs, o + 8192, 0, SC1);
  }
  ST(2);
  SBAR;

  v4u W1[8][4];
  {
    const int vo = (32 * w + g) * (F * 2) + min(128 * fg + 8 * lo, F * 2 - 8);
#pragma unroll
    for (int j = 0; j < 8; j++) {
      v2u r[8];
#pragma unroll
      for (int i = 0; i < 8; i++) r[i] = __builtin_amdgcn_raw_buffer_load_b64(rw1, vo, (256 * i + 4 * j) * (F * 2), 0);
      transpose8x4(r, W1[j]);
    }
  }
  float t2c[4], t2s[4], fWr[4], fWi[4];
#pragma unroll
  for (int r = 0; r < 4; r++) {
    const float th = (fg4 + (float)r) * flo * (1.f / 256);
    t2c[r] = fcos(th); t2s[r] = fsin(th);
    const int k2 = lo + 16 * (4 * g + r);
    fWr[r] = blo(*(LAS u16*)(L + L_R + k2 * 16 + w * 2)) * (2.f / (S * D));
    fWi[r] = blo(*(LAS u16*)(L + L_R + 4096 + k2 * 16 + w * 2)) * (2.f / (S * D));
  }
  SBAR;
  DRAIN();
  ST(3);
  const int uB = t >> 1, hfB = t & 1;
  const int n2B = 16 * (4 * (uB >> 6) + (uB & 3)) + ((uB >> 2) & 15);
  {
    const int o = OFF_X1 + n2B * 16384 + ((((c >> 2) + n2B) & 63) << 7) + (c & 3) * 32 + hfB * 16;
    v4u a0, a1;
    for (int sp = 0; sp < SPIN_CAP; sp++) {
      MEMF;
      a0 = __builtin_amdgcn_raw_buffer_load_b128(rs, o, 0, SC1);
      a1 = __builtin_amdgcn_raw_buffer_load_b128(rs, o + 8192, 0, SC1);
      const u32 bad = (a0.y ^ tag) | (a0.w ^ tag) | (a1.y ^ tag) | (a1.w ^ tag);
      if (__all(bad == 0)) break;
    }
    ST(4);
    LAS u32* Bb = (LAS u32*)(L + L_Q);
    Bb[hfB * 1040 + uB] = a0.x; Bb[hfB * 1040 + 256 + uB] = a0.z; Bb[hfB * 1040 + 512 + uB] = a1.x; Bb[hfB * 1040 + 768 + uB] = a1.z;
    LBAR();
    if (b == 0 && t == 0) __builtin_amdgcn_raw_buffer_store_b32(tag, rs, 0, 0, SC1);
    LAS v4u* colp = (LAS v4u*)(Bb + (w >> 2) * 1040 + (w & 3) * 256) + lane;
    v4u P = *colp;
    const f32x4 z4 = {0.f, 0.f, 0.f, 0.f};
    f32x4 re = mfma(P, F16re, z4), im = mfma(P, F16im, z4);
#pragma unroll
    for (int r = 0; r < 4; r++) P[r] = pk(re[r] * t2c[r] + im[r] * t2s[r], im[r] * t2c[r] - re[r] * t2s[r]);
    re = mfma(F16re, P, z4); im = mfma(F16im, P, z4);
#pragma unroll
    for (int r = 0; r < 4; r++) P[r] = pk(re[r] * fWi[r] + im[r] * fWr[r], re[r] * fWr[r] - im[r] * fWi[r]);
    im = mfma(P, F16re, z4); re = mfma(P, F16im, z4);
#pragma unroll
    for (int r = 0; r < 4; r++) P[r] = pk(im[r] * t2c[r] + re[r] * t2s[r], re[r] * t2c[r] - im[r] * t2s[r]);
    im = mfma(F16re, P, z4); re = mfma(F16im, P, z4);
#pragma unroll
    for (int r = 0; r < 4; r++) P[r] = clean(pk(re[r], im[r]));
    *colp = P;
    LBAR();
    const v4u y = {Bb[hfB * 1040 + uB], Bb[hfB * 1040 + 256 + uB], Bb[hfB * 1040 + 512 + uB], Bb[hfB * 1040 + 768 + uB]};
    __builtin_amdgcn_raw_buffer_store_b128(y, rs, OFF_Y1 + c * 8192 + ((((n2B >> 2) + c) & 63) << 7) + (n2B & 3) * 32 + hfB * 16, 0, SC1);
  }
  ST(5);
  SBAR;
  v4u Ire, Iim, Gre, Gim, Hre, Him, E0, E1;
#pragma unroll
  for (int r = 0; r < 4; r++) {
    const float al = (float)((4 * (g & 1) + r) * (256 * (lo & 7) + c)) * (1.f / 2048);
    const float ca = fcos(al) * szero, sa = fsin(al) * szero;
    Ire[r] = pk(ca, -sa); Iim[r] = pk(sa, ca);
    Gre[r] = F16re[r] ^ (g < 2 ? 0x80000000u : 0u);
    Gim[r] = F16im[r] ^ (g < 2 ? 0x00008000u : 0x80008000u);
    const float ps = flo * (fg4 + (float)(r + 1)) * (1.f / 32);
    const float wg = (r == 3 && g == 3) ? 0.5f : 1.f;
    const u32 sp = pk(fcos(ps) * wg, -fsin(ps));
    Hre[r] = (r & 1) ? sp : 0u;
    Him[r] = (r & 1) ? 0u : sp;
    const float be = flo * (fg4 + (float)r) * (1.f / 32);
    u32 e = pk(fcos(be), -fsin(be));
    if (r == 0) e = g == 0 ? ((lo & 1) ? 0xBF803F80u : 0x3F803F80u) : e;
    E0[r] = e;
    E1[r] = (r & 1) ? e ^ 0x80008000u : e;
  }
  SBAR;
  DRAIN();
  ST(6);
  {
    const int pB = (lane >> 3) * 32 + 4 * w + ((lane >> 1) & 3);
    const int o = OFF_Y1 + pB * 8192 + ((((c >> 2) + pB) & 63) << 7) + (c & 3) * 32 + (lane & 1) * 16;
    v4u a;
    for (int sp = 0; sp < SPIN_CAP; sp++) {
      MEMF;
      a = __builtin_amdgcn_raw_buffer_load_b128(rs, o, 0, SC1);
      if (__all(max(umax3(a.x, a.y, a.z), a.w) != ~0u)) break;
    }
    ST(7);
    LAS u32* Cb = (LAS u32*)(L + L_P) + w * 288;
    *(LAS v4u*)(Cb + (lane >> 3) * 36 + (lane & 7) * 4) = a;
    v4u P;
#pragma unroll
    for (int j = 0; j < 4; j++) P[j] = Cb[(4 * (g & 1) + j) * 36 + 16 * (g >> 1) + lo];
    const f32x4 z4 = {0.f, 0.f, 0.f, 0.f};
    f32x4 re = mfma(P, Ire, z4), im = mfma(P, Iim, z4);
    LAS u32* Qb = (LAS u32*)(L + L_R);
#pragma unroll
    for (int r = 0; r < 4; r++) P[r] = pk(re[r], im[r]);
    *(LAS v4u*)(Qb + (lo & 7) * 260 + 32 * w + 16 * (lo >> 3) + 4 * g) = P;
    const v2u x0 = *(LAS v2u*)(L + L_X + w * 1024 + 32 * lo + 8 * g), x1 = *(LAS v2u*)(L + L_X + w * 1024 + 512 + 32 * lo + 8 * g);
    LBAR();
#pragma unroll
    for (int r = 0; r < 4; r++) P[r] = Qb[w * 260 + col0 + r * cstep];
    re = mfma(P, Gre, z4); im = mfma(P, Gim, z4);
    const f32x4 sre = mfma(P, Hre, z4), sim = mfma(P, Him, z4);
    re[0] = g == 0 ? sre[0] : re[0];
    im[0] = g == 0 ? sim[0] : im[0];
#pragma unroll
    for (int r = 0; r < 4; r++) P[r] = pk(re[r] * twc[r] - im[r] * tws[r], im[r] * twc[r] + re[r] * tws[r]);
    const f32x4 y0 = mfma(P, E0, z4), y1 = mfma(P, E1, z4);
    v4u h;
    h[0] = pk(blo(x0.x) + y0[0], bhi(x0.x) + y0[1]); h[1] = pk(blo(x0.y) + y0[2], bhi(x0.y) + y0[3]);
    h[2] = pk(blo(x1.x) + y1[0], bhi(x1.x) + y1[1]); h[3] = pk(blo(x1.y) + y1[2], bhi(x1.y) + y1[3]);
    float s = 0.f, q = 0.f;
#pragma unroll
    for (int i = 0; i < 4; i++) { s = dot2(h[i], 0x3F803F80u, s); q = dot2(h[i], h[i], q); }
    wavesum2(s, q);
    const float mean = s * (1.f / D);
    const float var = fmaxf(q * (1.f / D) - mean * mean, 0.f);
    const float r1 = __builtin_amdgcn_rsqf(var + 1e-5f);
    const float sc = r1 * __builtin_amdgcn_rsqf(var * r1 * r1 + 1e-5f);
    const float mo = -mean * sc;
    v2u n0, n1;
    n0.x = pk(blo(h[0]) * sc + mo, bhi(h[0]) * sc + mo); n0.y = pk(blo(h[1]) * sc + mo, bhi(h[1]) * sc + mo);
    n1.x = pk(blo(h[2]) * sc + mo, bhi(h[2]) * sc + mo); n1.y = pk(blo(h[3]) * sc + mo, bhi(h[3]) * sc + mo);
    *(LAS v2u*)(L + L_Q + w * 1088 + 32 * lo + 8 * g) = n0;
    *(LAS v2u*)(L + L_Q + w * 1088 + 512 + 32 * lo + 8 * g) = n1;
    *(LAS v2u*)(L + L_X + w * 1024 + 32 * lo + 8 * g) = v2u{clean(h[0]), clean(h[1])};
    *(LAS v2u*)(L + L_X + w * 1024 + 512 + 32 * lo + 8 * g) = v2u{clean(h[2]), clean(h[3])};
    LBAR();
    LAS unsigned char* tp = L + L_Q + (lo >> 2) * 1088 + 128 * w + 32 * g + 8 * (lo & 3);
    const v2u t0 = trread(tp), t1 = trread(tp + 4 * 1088);
    const v4u hv = *(LAS v4u*)(L + L_X + w * 1024 + lane * 16);
    const int pos = 64 * w + 16 * ((lane >> 1) & 3) + 2 * (lane >> 3) + (lane & 1);
    const v4u hn = {clean(t0.x), clean(t0.y), clean(t1.x), clean(t1.y)};
    __builtin_amdgcn_raw_buffer_store_b128(hn, rs, OFF_HN + c * 8192 + pos * 16, 0, SC1);
    __builtin_amdgcn_raw_buffer_store_b128(hv, rs, OFF_H + (256 * w + c) * 1024 + lane * 16, 0, SC1);
  }
  ST(8);
  SBAR;
  const int f0 = 64 * fg + 16 * (w & 3) + 8 * (lane & 1) + 2 * (w >> 2) + ((lane >> 1) & 1);
  const bool fok = f0 < F;
  const float bia0 = blo(b1[fok ? f0 : 0]), bia1 = blo(b1[fok ? f0 + 4 : 0]);
  LBAR();
  DRAIN();
  ST(9);
  if (g1on) {
    const int ho = OFF_HN + (32 * w + g) * 8192 + (48 * db + lo) * 16;
    v4u A[2][8];
#pragma unroll
    for (int st = 0; st < 2; st++)
#pragma unroll
      for (int j = 0; j < 8; j++) A[st][j] = __builtin_amdgcn_raw_buffer_load_b128(rs, ho + st * 256, j * 32768, SC1);
    SBAR;
#pragma unroll
    for (int st = 0; st < 3; st++) {
      if (st < nst) {
        f32x4 acc[4];
        for (int sp = 0; sp < SPIN_CAP; sp++) {
          u32 mx = 0;
#pragma unroll
          for (int cc = 0; cc < 4; cc++) acc[cc] = f32x4{0.f, 0.f, 0.f, 0.f};
#pragma unroll
          for (int j = 0; j < 8; j++) {
            const v4u a = A[st & 1][j];
#pragma unroll
            for (int cc = 0; cc < 4; cc++) acc[cc] = mfma(W1[j][cc], a, acc[cc]);
            mx = umax3(umax3(mx, a.x, a.y), a.z, a.w);
          }
          if (__all(mx != ~0u)) break;
          MEMF;
#pragma unroll
          for (int j = 0; j < 8; j++) A[st & 1][j] = __builtin_amdgcn_raw_buffer_load_b128(rs, ho + st * 256, j * 32768, SC1);
        }
        if (st == 0 && nst == 3) {
#pragma unroll
          for (int j = 0; j < 8; j++) A[0][j] = __builtin_amdgcn_raw_buffer_load_b128(rs, ho + 512, j * 32768, SC1);
        }
        LAS unsigned char* R = L + st * 33792;
#pragma unroll
        for (int cc = 0; cc < 4; cc++) *(LAS f32x4*)(R + w * 4224 + (cc >> 1) * 1024 + (cc & 1) * 2176 + lane * 16) = acc[cc];
        LBAR();
        LAS unsigned char* rp = R + (w >> 2) * 1024 + ((lane >> 1) & 1) * 2176 + ((w & 3) * 16 + (lane >> 2)) * 16 + (lane & 1) * 8;
        f32x2 v = {bia0, bia1};
#pragma unroll
        for (int ww = 0; ww < 8; ww++) v += *(LAS f32x2*)(rp + ww * 4224);
        float o2[2];
#pragma unroll
        for (int e = 0; e < 2; e++) {
          const float u = v[e];
          const float z = u * (u * u * (-0.044715f * 2.f * 0.7978845608f * 1.4426950409f) + (-2.f * 0.7978845608f * 1.4426950409f));
          o2[e] = u * __builtin_amdgcn_rcpf(1.f + __builtin_amdgcn_exp2f(z));
        }
        const u32 uo = fok ? clean(pk(o2[0], o2[1])) : 0u;
        v4u ov;
        ov.x = uo;
        ov.y = (u32)__builtin_amdgcn_update_dpp(0, (int)uo, 0x55, 0xf, 0xf, true);
        ov.z = (u32)__builtin_amdgcn_update_dpp(0, (int)uo, 0xAA, 0xf, 0xf, true);
        ov.w = (u32)__builtin_amdgcn_update_dpp(0, (int)uo, 0xFF, 0xf, 0xf, true);
        if ((lane & 3) == 0) __builtin_amdgcn_raw_buffer_store_b128(ov, rs, OFF_U + ((3 * db + st) * 44 + 2 * fg) * 1024 + t * 4, 0, SC1);
      }
    }
  }
  ST(10);
  SBAR;
  {
    const int d2 = 2 * (xc >> 1) + (xi >> 4), sb = 16 * (xc & 1) + (xi & 15);
    const int j0 = w < 4 ? 6 * w : 4 + 5 * w;
    const bool six = w < 4;
    v4u W2[6][4];
    {
      const int vo = 16 * g * (S * 2) + 128 * sb + 8 * lo;
#pragma unroll
      for (int j = 0; j < 6; j++) {
        if (j < 5 || six) {
          const int jg = j0 + j;
          const int fb = 64 * (jg >> 1) + 2 * (jg & 1);
          v2u r[8];
#pragma unroll
          for (int e = 0; e < 8; e++) r[e] = __builtin_amdgcn_raw_buffer_load_b64(rw2, vo + (fb + 4 * (e & 3) + (e >> 2)) * (S * 2), 0, 0);
          transpose8x4(r, W2[j]);
        }
      }
    }
    const int sl = t >> 3, q8 = t & 7;
    const int so = 64 * sb + 4 * (sl & 15) + (sl >> 4);
    const float bias2 = blo(b2[so]);
    LBAR();
    DRAIN();
    ST(11);
    const int uo = OFF_U + (4 * d2 * 44 + j0) * 1024 + lane * 16;
    v4u A[4][6];
#pragma unroll
    for (int st = 0; st < 4; st++)
#pragma unroll
      for (int j = 0; j < 6; j++)
        if (j < 5 || six) A[st][j] = __builtin_amdgcn_raw_buffer_load_b128(rs, uo, (st * 44 + j) * 1024, SC1);
    const int hoff = OFF_H + so * 1024 + d2 * 128 + q8 * 16;
    v4u hv = __builtin_amdgcn_raw_buffer_load_b128(rs, hoff, 0, SC1);
    SBAR;
    f32x2 res[4];
#pragma unroll
    for (int st = 0; st < 4; st++) {
      f32x4 acc[4];
      for (int sp = 0; sp < SPIN_CAP; sp++) {
        u32 mx = 0;
#pragma unroll
        for (int cc = 0; cc < 4; cc++) acc[cc] = f32x4{0.f, 0.f, 0.f, 0.f};
#pragma unroll
        for (int j = 0; j < 6; j++) {
          if (j < 5 || six) {
            const v4u a = A[st][j];
#pragma unroll
            for (int cc = 0; cc < 4; cc++) acc[cc] = mfma(a, W2[j][cc], acc[cc]);
            mx = umax3(umax3(mx, a.x, a.y), a.z, a.w);
          }
        }
        if (__all(mx != ~0u)) break;
        MEMF;
#pragma unroll
        for (int j = 0; j < 6; j++)
          if (j < 5 || six) A[st][j] = __builtin_amdgcn_raw_buffer_load_b128(rs, uo, (st * 44 + j) * 1024, SC1);
      }
      LAS unsigned char* R = L + st * 32768;
#pragma unroll
      for (int cc = 0; cc < 4; cc++) *(LAS f32x4*)(R + w * 4096 + cc * 1024 + g * 256 + ((lo ^ (4 * g)) * 16)) = acc[cc];
      LBAR();
      LAS unsigned char* rp = R + (sl >> 4) * 1024 + (q8 >> 1) * 256 + (((sl & 15) ^ (4 * (q8 >> 1))) * 16) + (q8 & 1) * 8;
      f32x2 v = {bias2, bias2};
#pragma unroll
      for (int ww = 0; ww < 8; ww++) v += *(LAS f32x2*)(rp + ww * 4096);
      res[st] = v;
    }
    for (int sp = 0; sp < SPIN_CAP; sp++) {
      if (__all(max(umax3(hv.x, hv.y, hv.z), hv.w) != ~0u)) break;
      MEMF;
      hv = __builtin_amdgcn_raw_buffer_load_b128(rs, hoff, 0, SC1);
    }
    v4u o;
#pragma unroll
    for (int st = 0; st < 4; st++) o[st] = pk(blo(hv[st]) + res[st][0], bhi(hv[st]) + res[st][1]);
    *(v4u*)(out + so * D + d2 * 64 + q8 * 8) = o;
  }
  ST(12);
  if (w == 0) { unsigned long long e0 = __builtin_amdgcn_s_memrealtime(), lim = (c1_ - c0_) * 4; if (lim > 20000) lim = 20000; while (__builtin_amdgcn_s_memrealtime() - e0 < lim) __builtin_amdgcn_s_sleep(1); }
}

extern "C" void kernel_launch(void* const* d_in, const int* in_sizes, int n_in, void* d_out, int out_size, void* d_ws, size_t ws_size,
                              hipStream_t stream) {
  (void)hipFuncSetAttribute((const void*)fused_kernel, hipFuncAttributeMaxDynamicSharedMemorySize, LDS_BYTES);
  fused_kernel<<<256, 512, LDS_BYTES, stream>>>((const u16*)d_in[0], (const u16*)d_in[1], (const u16*)d_in[2], (const u16*)d_in[4],
                                                (const u16*)d_in[5], (const u16*)d_in[6], (const u16*)d_in[7], (u16*)d_out,
                                                (unsigned char*)d_ws);
}
```

```cpp
#include <hip/hip_runtime.h>

typedef unsigned short u16;
typedef unsigned u32;
typedef u32 v4u __attribute__((ext_vector_type(4)));
typedef u32 v2u __attribute__((ext_vector_type(2)));
typedef short v4s __attribute__((ext_vector_type(4)));
typedef float f32x2 __attribute__((ext_vector_type(2)));
typedef float f32x4 __attribute__((ext_vector_type(4)));
typedef __bf16 bf16x2 __attribute__((ext_vector_type(2)));
typedef __bf16 bf16x8 __attribute__((ext_vector_type(8)));
#define LAS __attribute__((address_space(3)))
#define GAS __attribute__((address_space(1)))
#define DEV __device__ __forceinline__

constexpr int S = 2048, D = 512, DF = 257, F = 1376;
constexpr int OFF_X1 = 2 << 20, OFF_Y1 = 6 << 20, OFF_HN = 8 << 20, OFF_H = 10 << 20, OFF_U = 12 << 20, WS_USED = 14 << 20;
constexpr int SC1 = 16;
constexpr int SPIN_CAP = 1 << 16;
constexpr int CODE_LINES = 128;

constexpr int L_X = 0;
constexpr int L_P = 8192;
constexpr int L_Q = L_P + 9216;
constexpr int L_R = L_Q + 8704;
constexpr int L_END = L_R + 8320;
constexpr int LDS_BYTES = 131072 + 1024;

DEV float asf(u32 v) { return __builtin_bit_cast(float, v); }
DEV u32 asu(float v) { return __builtin_bit_cast(u32, v); }
DEV u32 pk(float lo, float hi) { f32x2 v = {lo, hi}; return __builtin_bit_cast(u32, __builtin_convertvector(v, bf16x2)); }
DEV float blo(u32 v) { return asf(v << 16); }
DEV float bhi(u32 v) { return asf(v & 0xFFFF0000u); }
DEV f32x4 mfma(v4u a, v4u b, f32x4 c) {
  return __builtin_amdgcn_mfma_f32_16x16x32_bf16(__builtin_bit_cast(bf16x8, a), __builtin_bit_cast(bf16x8, b), c, 0, 0, 0);
}
DEV float dot2(u32 a, u32 b, float c) {
  return __builtin_amdgcn_fdot2_f32_bf16(__builtin_bit_cast(bf16x2, a), __builtin_bit_cast(bf16x2, b), c, false);
}
DEV float fsin(float rev) { return __builtin_amdgcn_sinf(rev); }
DEV float fcos(float rev) { return __builtin_amdgcn_cosf(rev); }
DEV v2u trread(LAS unsigned char* p) { return __builtin_bit_cast(v2u, __builtin_amdgcn_ds_read_tr16_b64_v4i16((LAS v4s*)p)); }
DEV u32 umax3(u32 a, u32 b, u32 c) { return max(max(a, b), c); }
DEV u32 clean(u32 v) { return min(v, 0xFFFFFFFEu); }
template <int CTRL, int RM> DEV float dppadd(float v) {
  return v + asf((u32)__builtin_amdgcn_update_dpp(0, (int)asu(v), CTRL, RM, 0xf, true));
}
DEV void wavesum2(float& s, float& q) {
  auto sw = __builtin_amdgcn_permlane32_swap(asu(s), asu(q), false, false);
  u32 a = sw[0], b = sw[1];
  float e = asf(a) + asf(b);
  e = dppadd<0x128, 0xf>(e);
  e = dppadd<0x124, 0xf>(e);
  e = dppadd<0x122, 0xf>(e);
  e = dppadd<0x121, 0xf>(e);
  e = dppadd<0x142, 0xa>(e);
  s = asf((u32)__builtin_amdgcn_readlane((int)asu(e), 31));
  q = asf((u32)__builtin_amdgcn_readlane((int)asu(e), 63));
}
#define MEMF asm volatile("" ::: "memory")
#define SBAR __builtin_amdgcn_sched_barrier(0)
#define LBAR() do { asm volatile("s_waitcnt lgkmcnt(0)" ::: "memory"); __builtin_amdgcn_s_barrier(); MEMF; } while (0)
#define DRAIN() asm volatile("s_waitcnt vmcnt(0)" ::: "memory")

#define ST(i)

struct __attribute__((packed, aligned(2))) U16x8 { v4u v; };

DEV void transpose8x4(const v2u (&r)[8], v4u (&f)[4]) {
#pragma unroll
  for (int p = 0; p < 4; p++) {
    f[0][p] = __builtin_amdgcn_perm(r[2 * p + 1].x, r[2 * p].x, 0x05040100);
    f[1][p] = __builtin_amdgcn_perm(r[2 * p + 1].x, r[2 * p].x, 0x07060302);
    f[2][p] = __builtin_amdgcn_perm(r[2 * p + 1].y, r[2 * p].y, 0x05040100);
    f[3][p] = __builtin_amdgcn_perm(r[2 * p + 1].y, r[2 * p].y, 0x07060302);
  }
}

__global__ __launch_bounds__(512, 2) void fused_kernel(const u16* __restrict__ x, const u16* __restrict__ wr, const u16* __restrict__ wi,
                                                       const u16* __restrict__ w1, const u16* __restrict__ b1, const u16* __restrict__ w2,
                                                       const u16* __restrict__ b2, u16* __restrict__ out, unsigned char* ws) {
  extern __shared__ __attribute__((aligned(16))) unsigned char smem_[];
  LAS unsigned char* L = (LAS unsigned char*)smem_;
  ST(0);
  const int t = threadIdx.x, lane = t & 63, lo = lane & 15, g = lane >> 4;
  const int w = __builtin_amdgcn_readfirstlane(t >> 6);
  const int b = blockIdx.x, c = (b & 7) * 32 + (b >> 3);
  const __amdgpu_buffer_rsrc_t rs = __builtin_amdgcn_make_buffer_rsrc((void*)ws, 0, WS_USED, 0x00020000);
  const __amdgpu_buffer_rsrc_t rw1 = __builtin_amdgcn_make_buffer_rsrc((void*)w1, 0, S * F * 2, 0x00020000);
  const __amdgpu_buffer_rsrc_t rw2 = __builtin_amdgcn_make_buffer_rsrc((void*)w2, 0, S * F * 2, 0x00020000);

  const v4u xv = *(const v4u*)(x + (256 * w + c) * D + lane * 8);
  const u32 tag = __builtin_amdgcn_raw_buffer_load_b32(rs, 0, 0, SC1) + 1u;
  u32 pf = 0;
  if (t < CODE_LINES) {
    unsigned long long pc;
    asm volatile("s_getpc_b64 %0" : "=s"(pc));
    pf = *(const GAS u32*)((pc & ~127ull) + t * 128);
  }
  const int k1B = c >> 5, blkB = c & 31;
  const v4u wv = ((const U16x8*)(((t >> 8) ? wi : wr) + (k1B + 8 * (t & 255)) * DF + 8 * blkB + 1))->v;

  const int xc = b & 7, xi = b >> 3;
  const int q1 = xc >> 1, tl = q1 * 30 + (q1 ? 1 : 0) + xi;
  const bool g1on = xi < (q1 ? 30 : 31);
  const int db = tl / 11, fg = (xc & 1) * 11 + tl % 11;
  const int nst = db == 10 ? 2 : 3;
  {
    const v4u sent = {~0u, ~0u, ~0u, ~0u};
    __builtin_amdgcn_raw_buffer_store_b128(sent, rs, OFF_Y1 + c * 8192 + t * 16, 0, SC1);
    __builtin_amdgcn_raw_buffer_store_b128(sent, rs, OFF_HN + c * 8192 + t * 16, 0, SC1);
    __builtin_amdgcn_raw_buffer_store_b128(sent, rs, OFF_H + (256 * w + c) * 1024 + lane * 16, 0, SC1);
    if (g1on && (w >> 1) < nst)
      __builtin_amdgcn_raw_buffer_store_b128(sent, rs, OFF_U + ((3 * db + (w >> 1)) * 44 + 2 * fg) * 1024 + (t & 127) * 16, 0, SC1);
  }
  SBAR;

  const float flo = (float)lo, fg4 = (float)(4 * g);
  v4u F32re, F32im;
#pragma unroll
  for (int p = 0; p < 4; p++) {
    const float a0 = fg4 + (float)(16 * (p >> 1) + 2 * (p & 1));
    const float r0 = a0 * flo * (1.f / 32), r1 = (a0 + 1.f) * flo * (1.f / 32);
    F32re[p] = pk(fcos(r0), fcos(r1));
    const u32 im = pk(-fsin(r0), -fsin(r1));
    F32im[p] = lo == 0 ? 0xBF803F80u : im;
  }
  float twc[4], tws[4];
  v4u F16re, F16im, SPre, SPim, Sre, Sim;
  const float szero = ((lo >> 3) == (g >> 1)) ? 1.f : 0.f;
#pragma unroll
  for (int r = 0; r < 4; r++) {
    const float bb = fg4 + (float)r;
    const float th = bb * flo * (1.f / 512);
    twc[r] = fcos(th); tws[r] = fsin(th);
    const float ph = bb * flo * (1.f / 16);
    const float cc = fcos(ph), ss = fsin(ph);
    F16re[r] = pk(cc, ss); F16im[r] = pk(-ss, cc);
    const float ps = bb * (flo + 1.f) * (1.f / 32);
    const u32 sp = pk(fcos(ps), -fsin(ps));
    SPre[r] = (lo & 1) ? (sp & 0xFFFFu) : (sp << 16);
    SPim[r] = (lo & 1) ? (sp >> 16) : (sp & 0xFFFF0000u);
    const float al = (float)((lo & 7) * (256 * (4 * (g & 1) + r) + c)) * (1.f / 2048);
    const float ca = fcos(al) * szero, sa = fsin(al) * szero;
    Sre[r] = pk(ca, sa); Sim[r] = pk(-sa, ca);
  }
  const int col0 = lo == 0 ? 64 * g + 15 : (g < 2 ? lo + 128 * g - 1 : 511 - lo - 128 * g);
  const int cstep = lo == 0 ? 16 : (g < 2 ? 32 : -32);
  SBAR;

  *(LAS v4u*)(L + L_X + w * 1024 + lane * 16) = xv;
  ST(1);
  {
    float s = 0.f, q = 0.f;
#pragma unroll
    for (int i = 0; i < 4; i++) { s = dot2(xv[i], 0x3F803F80u, s); q = dot2(xv[i], xv[i], q); }
    const v2u xa0 = trread(L + L_X + w * 1024 + lane * 8), xa1 = trread(L + L_X + w * 1024 + 512 + lane * 8);
    const v4u xa = {xa0.x, xa0.y, xa1.x, xa1.y};
    const f32x4 z4 = {0.f, 0.f, 0.f, 0.f};
    f32x4 Tre = mfma(xa, F32re, z4), Tim = mfma(xa, F32im, z4);
    wavesum2(s, q);
    const float mean = s * (1.f / D);
    const float var = fmaxf(q * (1.f / D) - mean * mean, 0.f);
    const float rstd = __builtin_amdgcn_rsqf(var + 1e-5f);
    const float msub = lo == 0 ? 32.f * mean : 0.f;
    v4u P;
#pragma unroll
    for (int r = 0; r < 4; r++) {
      const float wc = twc[r] * rstd, wsn = tws[r] * rstd;
      const float re = Tre[r] - msub, im = Tim[r];
      P[r] = pk(re * wc + im * wsn, im * wc - re * wsn);
    }
    const u32 sg = lo >= 8 ? 0x80008000u : 0u;
    const v4u F16imc = {F16im[0] ^ sg, F16im[1] ^ sg, F16im[2] ^ sg, F16im[3] ^ sg};
    const f32x4 Zre = mfma(F16re, P, z4), Zim = mfma(F16imc, P, z4), Yre = mfma(SPre, P, z4), Yim = mfma(SPim, P, z4);
    LAS u32* Zb = (LAS u32*)(L + L_P);
#pragma unroll
    for (int r = 0; r < 4; r++) {
      const u32 zg = pk(Zre[r], Zim[r]), zs = pk(Yre[r], Yim[r]);
      Zb[w * 260 + col0 + r * cstep] = lo == 0 ? zs : zg;
    }
    *(LAS v4u*)(L + L_R + t * 16) = wv;
    asm volatile("" ::"v"(pf));
    DRAIN();
    LBAR();
    v4u za;
#pragma unroll
    for (int j = 0; j < 4; j++) za[j] = Zb[(4 * (g & 1) + j) * 260 + 32 * w + 16 * (g >> 1) + lo];
    const f32x4 Ore = mfma(za, Sre, z4), Oim = mfma(za, Sim, z4);
    const int cB = (lo & 7) * 32 + 4 * w + 2 * (lo >> 3) + (g >> 1);
    const int o = OFF_X1 + c * 16384 + ((((cB >> 2) + c) & 63) << 7) + (cB & 3) * 32 + (g & 1) * 16;
    const v4u s0 = {pk(Ore[0], Oim[0]), tag, pk(Ore[1], Oim[1]), tag}, s1 = {pk(Ore[2], Oim[2]), tag, pk(Ore[3], Oim[3]), tag};
    __builtin_amdgcn_raw_buffer_store_b128(s0, rs, o, 0, SC1);
    __builtin_amdgcn_raw_buffer_store_b128(s1, rs, o + 8192, 0, SC1);
  }
  ST(2);
  SBAR;

  v4u W1[8][4];
  {
    const int vo = (32 * w + g) * (F * 2) + min(128 * fg + 8 * lo, F * 2 - 8);
#pragma unroll
    for (int j = 0; j < 8; j++) {
      v2u r[8];
#pragma unroll
      for (int i = 0; i < 8; i++) r[i] = __builtin_amdgcn_raw_buffer_load_b64(rw1, vo, (256 * i + 4 * j) * (F * 2), 0);
      transpose8x4(r, W1[j]);
    }
  }
  float t2c[4], t2s[4], fWr[4], fWi[4];
#pragma unroll
  for (int r = 0; r < 4; r++) {
    const float th = (fg4 + (float)r) * flo * (1.f / 256);
    t2c[r] = fcos(th); t2s[r] = fsin(th);
    const int k2 = lo + 16 * (4 * g + r);
    fWr[r] = blo(*(LAS u16*)(L + L_R + k2 * 16 + w * 2)) * (2.f / (S * D));
    fWi[r] = blo(*(LAS u16*)(L + L_R + 4096 + k2 * 16 + w * 2)) * (2.f / (S * D));
  }
  SBAR;
  DRAIN();
  ST(3);
  const int uB = t >> 1, hfB = t & 1;
  const int n2B = 16 * (4 * (uB >> 6) + (uB & 3)) + ((uB >> 2) & 15);
  {
    const int o = OFF_X1 + n2B * 16384 + ((((c >> 2) + n2B) & 63) << 7) + (c & 3) * 32 + hfB * 16;
    v4u a0, a1;
    for (int sp = 0; sp < SPIN_CAP; sp++) {
      MEMF;
      a0 = __builtin_amdgcn_raw_buffer_load_b128(rs, o, 0, SC1);
      a1 = __builtin_amdgcn_raw_buffer_load_b128(rs, o + 8192, 0, SC1);
      const u32 bad = (a0.y ^ tag) | (a0.w ^ tag) | (a1.y ^ tag) | (a1.w ^ tag);
      if (__all(bad == 0)) break;
    }
    ST(4);
    LAS u32* Bb = (LAS u32*)(L + L_Q);
    Bb[hfB * 1040 + uB] = a0.x; Bb[hfB * 1040 + 256 + uB] = a0.z; Bb[hfB * 1040 + 512 + uB] = a1.x; Bb[hfB * 1040 + 768 + uB] = a1.z;
    LBAR();
    if (b == 0 && t == 0) __builtin_amdgcn_raw_buffer_store_b32(tag, rs, 0, 0, SC1);
    LAS v4u* colp = (LAS v4u*)(Bb + (w >> 2) * 1040 + (w & 3) * 256) + lane;
    v4u P = *colp;
    const f32x4 z4 = {0.f, 0.f, 0.f, 0.f};
    f32x4 re = mfma(P, F16re, z4), im = mfma(P, F16im, z4);
#pragma unroll
    for (int r = 0; r < 4; r++) P[r] = pk(re[r] * t2c[r] + im[r] * t2s[r], im[r] * t2c[r] - re[r] * t2s[r]);
    re = mfma(F16re, P, z4); im = mfma(F16im, P, z4);
#pragma unroll
    for (int r = 0; r < 4; r++) P[r] = pk(re[r] * fWi[r] + im[r] * fWr[r], re[r] * fWr[r] - im[r] * fWi[r]);
    im = mfma(P, F16re, z4); re = mfma(P, F16im, z4);
#pragma unroll
    for (int r = 0; r < 4; r++) P[r] = pk(im[r] * t2c[r] + re[r] * t2s[r], re[r] * t2c[r] - im[r] * t2s[r]);
    im = mfma(F16re, P, z4); re = mfma(F16im, P, z4);
#pragma unroll
    for (int r = 0; r < 4; r++) P[r] = clean(pk(re[r], im[r]));
    *colp = P;
    LBAR();
    const v4u y = {Bb[hfB * 1040 + uB], Bb[hfB * 1040 + 256 + uB], Bb[hfB * 1040 + 512 + uB], Bb[hfB * 1040 + 768 + uB]};
    __builtin_amdgcn_raw_buffer_store_b128(y, rs, OFF_Y1 + c * 8192 + ((((n2B >> 2) + c) & 63) << 7) + (n2B & 3) * 32 + hfB * 16, 0, SC1);
  }
  ST(5);
  SBAR;
  v4u Ire, Iim, Gre, Gim, Hre, Him, E0, E1;
#pragma unroll
  for (int r = 0; r < 4; r++) {
    const float al = (float)((4 * (g & 1) + r) * (256 * (lo & 7) + c)) * (1.f / 2048);
    const float ca = fcos(al) * szero, sa = fsin(al) * szero;
    Ire[r] = pk(ca, -sa); Iim[r] = pk(sa, ca);
    Gre[r] = F16re[r] ^ (g < 2 ? 0x80000000u : 0u);
    Gim[r] = F16im[r] ^ (g < 2 ? 0x00008000u : 0x80008000u);
    const float ps = flo * (fg4 + (float)(r + 1)) * (1.f / 32);
    const float wg = (r == 3 && g == 3) ? 0.5f : 1.f;
    const u32 sp = pk(fcos(ps) * wg, -fsin(ps));
    Hre[r] = (r & 1) ? sp : 0u;
    Him[r] = (r & 1) ? 0u : sp;
    const float be = flo * (fg4 + (float)r) * (1.f / 32);
    u32 e = pk(fcos(be), -fsin(be));
    if (r == 0) e = g == 0 ? ((lo & 1) ? 0xBF803F80u : 0x3F803F80u) : e;
    E0[r] = e;
    E1[r] = (r & 1) ? e ^ 0x80008000u : e;
  }
  SBAR;
  DRAIN();
  ST(6);
  {
    const int pB = (lane >> 3) * 32 + 4 * w + ((lane >> 1) & 3);
    const int o = OFF_Y1 + pB * 8192 + ((((c >> 2) + pB) & 63) << 7) + (c & 3) * 32 + (lane & 1) * 16;
    v4u a;
    for (int sp = 0; sp < SPIN_CAP; sp++) {
      MEMF;
      a = __builtin_amdgcn_raw_buffer_load_b128(rs, o, 0, SC1);
      if (__all(max(umax3(a.x, a.y, a.z), a.w) != ~0u)) break;
    }
    ST(7);
    LAS u32* Cb = (LAS u32*)(L + L_P) + w * 288;
    *(LAS v4u*)(Cb + (lane >> 3) * 36 + (lane & 7) * 4) = a;
    v4u P;
#pragma unroll
    for (int j = 0; j < 4; j++) P[j] = Cb[(4 * (g & 1) + j) * 36 + 16 * (g >> 1) + lo];
    const f32x4 z4 = {0.f, 0.f, 0.f, 0.f};
    f32x4 re = mfma(P, Ire, z4), im = mfma(P, Iim, z4);
    LAS u32* Qb = (LAS u32*)(L + L_R);
#pragma unroll
    for (int r = 0; r < 4; r++) P[r] = pk(re[r], im[r]);
    *(LAS v4u*)(Qb + (lo & 7) * 260 + 32 * w + 16 * (lo >> 3) + 4 * g) = P;
    const v2u x0 = *(LAS v2u*)(L + L_X + w * 1024 + 32 * lo + 8 * g), x1 = *(LAS v2u*)(L + L_X + w * 1024 + 512 + 32 * lo + 8 * g);
    LBAR();
#pragma unroll
    for (int r = 0; r < 4; r++) P[r] = Qb[w * 260 + col0 + r * cstep];
    re = mfma(P, Gre, z4); im = mfma(P, Gim, z4);
    const f32x4 sre = mfma(P, Hre, z4), sim = mfma(P, Him, z4);
    re[0] = g == 0 ? sre[0] : re[0];
    im[0] = g == 0 ? sim[0] : im[0];
#pragma unroll
    for (int r = 0; r < 4; r++) P[r] = pk(re[r] * twc[r] - im[r] * tws[r], im[r] * twc[r] + re[r] * tws[r]);
    const f32x4 y0 = mfma(P, E0, z4), y1 = mfma(P, E1, z4);
    v4u h;
    h[0] = pk(blo(x0.x) + y0[0], bhi(x0.x) + y0[1]); h[1] = pk(blo(x0.y) + y0[2], bhi(x0.y) + y0[3]);
    h[2] = pk(blo(x1.x) + y1[0], bhi(x1.x) + y1[1]); h[3] = pk(blo(x1.y) + y1[2], bhi(x1.y) + y1[3]);
    float s = 0.f, q = 0.f;
#pragma unroll
    for (int i = 0; i < 4; i++) { s = dot2(h[i], 0x3F803F80u, s); q = dot2(h[i], h[i], q); }
    wavesum2(s, q);
    const float mean = s * (1.f / D);
    const float var = fmaxf(q * (1.f / D) - mean * mean, 0.f);
    const float r1 = __builtin_amdgcn_rsqf(var + 1e-5f);
    const float sc = r1 * __builtin_amdgcn_rsqf(var * r1 * r1 + 1e-5f);
    const float mo = -mean * sc;
    v2u n0, n1;
    n0.x = pk(blo(h[0]) * sc + mo, bhi(h[0]) * sc + mo); n0.y = pk(blo(h[1]) * sc + mo, bhi(h[1]) * sc + mo);
    n1.x = pk(blo(h[2]) * sc + mo, bhi(h[2]) * sc + mo); n1.y = pk(blo(h[3]) * sc + mo, bhi(h[3]) * sc + mo);
    *(LAS v2u*)(L + L_Q + w * 1088 + 32 * lo + 8 * g) = n0;
    *(LAS v2u*)(L + L_Q + w * 1088 + 512 + 32 * lo + 8 * g) = n1;
    *(LAS v2u*)(L + L_X + w * 1024 + 32 * lo + 8 * g) = v2u{clean(h[0]), clean(h[1])};
    *(LAS v2u*)(L + L_X + w * 1024 + 512 + 32 * lo + 8 * g) = v2u{clean(h[2]), clean(h[3])};
    LBAR();
    LAS unsigned char* tp = L + L_Q + (lo >> 2) * 1088 + 128 * w + 32 * g + 8 * (lo & 3);
    const v2u t0 = trread(tp), t1 = trread(tp + 4 * 1088);
    const v4u hv = *(LAS v4u*)(L + L_X + w * 1024 + lane * 16);
    const int pos = 64 * w + 16 * ((lane >> 1) & 3) + 2 * (lane >> 3) + (lane & 1);
    const v4u hn = {clean(t0.x), clean(t0.y), clean(t1.x), clean(t1.y)};
    __builtin_amdgcn_raw_buffer_store_b128(hn, rs, OFF_HN + c * 8192 + pos * 16, 0, SC1);
    __builtin_amdgcn_raw_buffer_store_b128(hv, rs, OFF_H + (256 * w + c) * 1024 + lane * 16, 0, SC1);
  }
  ST(8);
  SBAR;
  const int f0 = 64 * fg + 16 * (w & 3) + 8 * (lane & 1) + 2 * (w >> 2) + ((lane >> 1) & 1);
  const bool fok = f0 < F;
  const float bia0 = blo(b1[fok ? f0 : 0]), bia1 = blo(b1[fok ? f0 + 4 : 0]);
  LBAR();
  DRAIN();
  ST(9);
  if (g1on) {
    const int ho = OFF_HN + (32 * w + g) * 8192 + (48 * db + lo) * 16;
    v4u A[2][8];
#pragma unroll
    for (int st = 0; st < 2; st++)
#pragma unroll
      for (int j = 0; j < 8; j++) A[st][j] = __builtin_amdgcn_raw_buffer_load_b128(rs, ho + st * 256, j * 32768, SC1);
    SBAR;
#pragma unroll
    for (int st = 0; st < 3; st++) {
      if (st < nst) {
        f32x4 acc[4];
        for (int sp = 0; sp < SPIN_CAP; sp++) {
          u32 mx = 0;
#pragma unroll
          for (int cc = 0; cc < 4; cc++) acc[cc] = f32x4{0.f, 0.f, 0.f, 0.f};
#pragma unroll
          for (int j = 0; j < 8; j++) {
            const v4u a = A[st & 1][j];
#pragma unroll
            for (int cc = 0; cc < 4; cc++) acc[cc] = mfma(W1[j][cc], a, acc[cc]);
            mx = umax3(umax3(mx, a.x, a.y), a.z, a.w);
          }
          if (__all(mx != ~0u)) break;
          MEMF;
#pragma unroll
          for (int j = 0; j < 8; j++) A[st & 1][j] = __builtin_amdgcn_raw_buffer_load_b128(rs, ho + st * 256, j * 32768, SC1);
        }
        if (st == 0 && nst == 3) {
#pragma unroll
          for (int j = 0; j < 8; j++) A[0][j] = __builtin_amdgcn_raw_buffer_load_b128(rs, ho + 512, j * 32768, SC1);
        }
        LAS unsigned char* R = L + st * 33792;
#pragma unroll
        for (int cc = 0; cc < 4; cc++) *(LAS f32x4*)(R + w * 4224 + (cc >> 1) * 1024 + (cc & 1) * 2176 + lane * 16) = acc[cc];
        LBAR();
        LAS unsigned char* rp = R + (w >> 2) * 1024 + ((lane >> 1) & 1) * 2176 + ((w & 3) * 16 + (lane >> 2)) * 16 + (lane & 1) * 8;
        f32x2 v = {bia0, bia1};
#pragma unroll
        for (int ww = 0; ww < 8; ww++) v += *(LAS f32x2*)(rp + ww * 4224);
        float o2[2];
#pragma unroll
        for (int e = 0; e < 2; e++) {
          const float u = v[e];
          const float z = u * (u * u * (-0.044715f * 2.f * 0.7978845608f * 1.4426950409f) + (-2.f * 0.7978845608f * 1.4426950409f));
          o2[e] = u * __builtin_amdgcn_rcpf(1.f + __builtin_amdgcn_exp2f(z));
        }
        const u32 uo = fok ? clean(pk(o2[0], o2[1])) : 0u;
        v4u ov;
        ov.x = uo;
        ov.y = (u32)__builtin_amdgcn_update_dpp(0, (int)uo, 0x55, 0xf, 0xf, true);
        ov.z = (u32)__builtin_amdgcn_update_dpp(0, (int)uo, 0xAA, 0xf, 0xf, true);
        ov.w = (u32)__builtin_amdgcn_update_dpp(0, (int)uo, 0xFF, 0xf, 0xf, true);
        if ((lane & 3) == 0) __builtin_amdgcn_raw_buffer_store_b128(ov, rs, OFF_U + ((3 * db + st) * 44 + 2 * fg) * 1024 + t * 4, 0, SC1);
      }
    }
  }
  ST(10);
  SBAR;
  {
    const int d2 = 2 * (xc >> 1) + (xi >> 4), sb = 16 * (xc & 1) + (xi & 15);
    const int j0 = w < 4 ? 6 * w : 4 + 5 * w;
    const bool six = w < 4;
    v4u W2[6][4];
    {
      const int vo = 16 * g * (S * 2) + 128 * sb + 8 * lo;
#pragma unroll
      for (int j = 0; j < 6; j++) {
        if (j < 5 || six) {
          const int jg = j0 + j;
          const int fb = 64 * (jg >> 1) + 2 * (jg & 1);
          v2u r[8];
#pragma unroll
          for (int e = 0; e < 8; e++) r[e] = __builtin_amdgcn_raw_buffer_load_b64(rw2, vo + (fb + 4 * (e & 3) + (e >> 2)) * (S * 2), 0, 0);
          transpose8x4(r, W2[j]);
        }
      }
    }
    const int sl = t >> 3, q8 = t & 7;
    const int so = 64 * sb + 4 * (sl & 15) + (sl >> 4);
    const float bias2 = blo(b2[so]);
    LBAR();
    DRAIN();
    ST(11);
    const int uo = OFF_U + (4 * d2 * 44 + j0) * 1024 + lane * 16;
    v4u A[4][6];
#pragma unroll
    for (int st = 0; st < 4; st++)
#pragma unroll
      for (int j = 0; j < 6; j++)
        if (j < 5 || six) A[st][j] = __builtin_amdgcn_raw_buffer_load_b128(rs, uo, (st * 44 + j) * 1024, SC1);
    const int hoff = OFF_H + so * 1024 + d2 * 128 + q8 * 16;
    v4u hv = __builtin_amdgcn_raw_buffer_load_b128(rs, hoff, 0, SC1);
    SBAR;
    f32x2 res[4];
#pragma unroll
    for (int st = 0; st < 4; st++) {
      f32x4 acc[4];
      for (int sp = 0; sp < SPIN_CAP; sp++) {
        u32 mx = 0;
#pragma unroll
        for (int cc = 0; cc < 4; cc++) acc[cc] = f32x4{0.f, 0.f, 0.f, 0.f};
#pragma unroll
        for (int j = 0; j < 6; j++) {
          if (j < 5 || six) {
            const v4u a = A[st][j];
#pragma unroll
            for (int cc = 0; cc < 4; cc++) acc[cc] = mfma(a, W2[j][cc], acc[cc]);
            mx = umax3(umax3(mx, a.x, a.y), a.z, a.w);
          }
        }
        if (__all(mx != ~0u)) break;
        MEMF;
#pragma unroll
        for (int j = 0; j < 6; j++)
          if (j < 5 || six) A[st][j] = __builtin_amdgcn_raw_buffer_load_b128(rs, uo, (st * 44 + j) * 1024, SC1);
      }
      LAS unsigned char* R = L + st * 32768;
#pragma unroll
      for (int cc = 0; cc < 4; cc++) *(LAS f32x4*)(R + w * 4096 + cc * 1024 + g * 256 + ((lo ^ (4 * g)) * 16)) = acc[cc];
      LBAR();
      LAS unsigned char* rp = R + (sl >> 4) * 1024 + (q8 >> 1) * 256 + (((sl & 15) ^ (4 * (q8 >> 1))) * 16) + (q8 & 1) * 8;
      f32x2 v = {bias2, bias2};
#pragma unroll
      for (int ww = 0; ww < 8; ww++) v += *(LAS f32x2*)(rp + ww * 4096);
      res[st] = v;
    }
    for (int sp = 0; sp < SPIN_CAP; sp++) {
      if (__all(max(umax3(hv.x, hv.y, hv.z), hv.w) != ~0u)) break;
      MEMF;
      hv = __builtin_amdgcn_raw_buffer_load_b128(rs, hoff, 0, SC1);
    }
    v4u o;
#pragma unroll
    for (int st = 0; st < 4; st++) o[st] = pk(blo(hv[st]) + res[st][0], bhi(hv[st]) + res[st][1]);
    __builtin_amdgcn_raw_buffer_store_b128(o, __builtin_amdgcn_make_buffer_rsrc((void*)out, 0, S * D * 2, 0x00020000), (so * D + d2 * 64 + q8 * 8) * 2, 0, SC1);
  }
  ST(12);
}

extern "C" void kernel_launch(void* const* d_in, const int* in_sizes, int n_in, void* d_out, int out_size, void* d_ws, size_t ws_size,
                              hipStream_t stream) {
  (void)hipFuncSetAttribute((const void*)fused_kernel, hipFuncAttributeMaxDynamicSharedMemorySize, LDS_BYTES);
  fused_kernel<<<256, 512, LDS_BYTES, stream>>>((const u16*)d_in[0], (const u16*)d_in[1], (const u16*)d_in[2], (const u16*)d_in[4],
                                                (const u16*)d_in[5], (const u16*)d_in[6], (const u16*)d_in[7], (u16*)d_out,
                                                (unsigned char*)d_ws);
}
```

```cpp
#define MULT 0
#include <hip/hip_runtime.h>

typedef unsigned short u16;
typedef unsigned u32;
typedef u32 v4u __attribute__((ext_vector_type(4)));
typedef u32 v2u __attribute__((ext_vector_type(2)));
typedef short v4s __attribute__((ext_vector_type(4)));
typedef float f32x2 __attribute__((ext_vector_type(2)));
typedef float f32x4 __attribute__((ext_vector_type(4)));
typedef __bf16 bf16x2 __attribute__((ext_vector_type(2)));
typedef __bf16 bf16x8 __attribute__((ext_vector_type(8)));
#define LAS __attribute__((address_space(3)))
#define GAS __attribute__((address_space(1)))
#define DEV __device__ __forceinline__

constexpr int S = 2048, D = 512, DF = 257, F = 1376;
constexpr int OFF_X1 = 2 << 20, OFF_Y1 = 6 << 20, OFF_HN = 8 << 20, OFF_H = 10 << 20, OFF_U = 12 << 20, WS_USED = 14 << 20;
constexpr int SC1 = 16;
constexpr int SPIN_CAP = 1 << 16;
constexpr int CODE_LINES = 128;

constexpr int L_X = 0;
constexpr int L_P = 8192;
constexpr int L_Q = L_P + 9216;
constexpr int L_R = L_Q + 8704;
constexpr int L_END = L_R + 8320;
constexpr int LDS_BYTES = 131072 + 1024;

DEV float asf(u32 v) { return __builtin_bit_cast(float, v); }
DEV u32 asu(float v) { return __builtin_bit_cast(u32, v); }
DEV u32 pk(float lo, float hi) { f32x2 v = {lo, hi}; return __builtin_bit_cast(u32, __builtin_convertvector(v, bf16x2)); }
DEV float blo(u32 v) { return asf(v << 16); }
DEV float bhi(u32 v) { return asf(v & 0xFFFF0000u); }
DEV f32x4 mfma(v4u a, v4u b, f32x4 c) {
  return __builtin_amdgcn_mfma_f32_16x16x32_bf16(__builtin_bit_cast(bf16x8, a), __builtin_bit_cast(bf16x8, b), c, 0, 0, 0);
}
DEV float dot2(u32 a, u32 b, float c) {
  return __builtin_amdgcn_fdot2_f32_bf16(__builtin_bit_cast(bf16x2, a), __builtin_bit_cast(bf16x2, b), c, false);
}
DEV float fsin(float rev) { return __builtin_amdgcn_sinf(rev); }
DEV float fcos(float rev) { return __builtin_amdgcn_cosf(rev); }
DEV v2u trread(LAS unsigned char* p) { return __builtin_bit_cast(v2u, __builtin_amdgcn_ds_read_tr16_b64_v4i16((LAS v4s*)p)); }
DEV u32 umax3(u32 a, u32 b, u32 c) { return max(max(a, b), c); }
DEV u32 clean(u32 v) { return min(v, 0xFFFFFFFEu); }
template <int CTRL, int RM> DEV float dppadd(float v) {
  return v + asf((u32)__builtin_amdgcn_update_dpp(0, (int)asu(v), CTRL, RM, 0xf, true));
}
DEV void wavesum2(float& s, float& q) {
  auto sw = __builtin_amdgcn_permlane32_swap(asu(s), asu(q), false, false);
  u32 a = sw[0], b = sw[1];
  float e = asf(a) + asf(b);
  e = dppadd<0x128, 0xf>(e);
  e = dppadd<0x124, 0xf>(e);
  e = dppadd<0x122, 0xf>(e);
  e = dppadd<0x121, 0xf>(e);
  e = dppadd<0x142, 0xa>(e);
  s = asf((u32)__builtin_amdgcn_readlane((int)asu(e), 31));
  q = asf((u32)__builtin_amdgcn_readlane((int)asu(e), 63));
}
#define MEMF asm volatile("" ::: "memory")
#define SBAR __builtin_amdgcn_sched_barrier(0)
#define LBAR() do { asm volatile("s_waitcnt lgkmcnt(0)" ::: "memory"); __builtin_amdgcn_s_barrier(); MEMF; } while (0)
#define DRAIN() asm volatile("s_waitcnt vmcnt(0)" ::: "memory")

#define ST(i)

struct __attribute__((packed, aligned(2))) U16x8 { v4u v; };

DEV void transpose8x4(const v2u (&r)[8], v4u (&f)[4]) {
#pragma unroll
  for (int p = 0; p < 4; p++) {
    f[0][p] = __builtin_amdgcn_perm(r[2 * p + 1].x, r[2 * p].x, 0x05040100);
    f[1][p] = __builtin_amdgcn_perm(r[2 * p + 1].x, r[2 * p].x, 0x07060302);
    f[2][p] = __builtin_amdgcn_perm(r[2 * p + 1].y, r[2 * p].y, 0x05040100);
    f[3][p] = __builtin_amdgcn_perm(r[2 * p + 1].y, r[2 * p].y, 0x07060302);
  }
}

__global__ __launch_bounds__(512, 2) void fused_kernel(const u16* __restrict__ x, const u16* __restrict__ wr, const u16* __restrict__ wi,
                                                       const u16* __restrict__ w1, const u16* __restrict__ b1, const u16* __restrict__ w2,
                                                       const u16* __restrict__ b2, u16* __restrict__ out, unsigned char* ws) {
  extern __shared__ __attribute__((aligned(16))) unsigned char smem_[];
  LAS unsigned char* L = (LAS unsigned char*)smem_;
  const unsigned long long c1_ = __builtin_amdgcn_s_memrealtime();
  const int t = threadIdx.x, lane = t & 63, lo = lane & 15, g = lane >> 4;
  const int w = __builtin_amdgcn_readfirstlane(t >> 6);
  const int b = blockIdx.x, c = (b & 7) * 32 + (b >> 3);
  const __amdgpu_buffer_rsrc_t rs = __builtin_amdgcn_make_buffer_rsrc((void*)ws, 0, WS_USED, 0x00020000);
  const __amdgpu_buffer_rsrc_t rw1 = __builtin_amdgcn_make_buffer_rsrc((void*)w1, 0, S * F * 2, 0x00020000);
  const __amdgpu_buffer_rsrc_t rw2 = __builtin_amdgcn_make_buffer_rsrc((void*)w2, 0, S * F * 2, 0x00020000);

  const v4u xv = *(const v4u*)(x + (256 * w + c) * D + lane * 8);
  const u32 tag = __builtin_amdgcn_raw_buffer_load_b32(rs, 0, 0, SC1) + 1u;
  u32 pf = 0;
  if (t < CODE_LINES) {
    unsigned long long pc;
    asm volatile("s_getpc_b64 %0" : "=s"(pc));
    pf = *(const GAS u32*)((pc & ~127ull) + t * 128);
  }
  const int k1B = c >> 5, blkB = c & 31;
  const v4u wv = ((const U16x8*)(((t >> 8) ? wi : wr) + (k1B + 8 * (t & 255)) * DF + 8 * blkB + 1))->v;

  const int xc = b & 7, xi = b >> 3;
  const int q1 = xc >> 1, tl = q1 * 30 + (q1 ? 1 : 0) + xi;
  const bool g1on = xi < (q1 ? 30 : 31);
  const int db = tl / 11, fg = (xc & 1) * 11 + tl % 11;
  const int nst = db == 10 ? 2 : 3;
  {
    const v4u sent = {~0u, ~0u, ~0u, ~0u};
    __builtin_amdgcn_raw_buffer_store_b128(sent, rs, OFF_Y1 + c * 8192 + t * 16, 0, SC1);
    __builtin_amdgcn_raw_buffer_store_b128(sent, rs, OFF_HN + c * 8192 + t * 16, 0, SC1);
    __builtin_amdgcn_raw_buffer_store_b128(sent, rs, OFF_H + (256 * w + c) * 1024 + lane * 16, 0, SC1);
    if (g1on && (w >> 1) < nst)
      __builtin_amdgcn_raw_buffer_store_b128(sent, rs, OFF_U + ((3 * db + (w >> 1)) * 44 + 2 * fg) * 1024 + (t & 127) * 16, 0, SC1);
  }
  SBAR;

  const float flo = (float)lo, fg4 = (float)(4 * g);
  v4u F32re, F32im;
#pragma unroll
  for (int p = 0; p < 4; p++) {
    const float a0 = fg4 + (float)(16 * (p >> 1) + 2 * (p & 1));
    const float r0 = a0 * flo * (1.f / 32), r1 = (a0 + 1.f) * flo * (1.f / 32);
    F32re[p] = pk(fcos(r0), fcos(r1));
    const u32 im = pk(-fsin(r0), -fsin(r1));
    F32im[p] = lo == 0 ? 0xBF803F80u : im;
  }
  float twc[4], tws[4];
  v4u F16re, F16im, SPre, SPim, Sre, Sim;
  const float szero = ((lo >> 3) == (g >> 1)) ? 1.f : 0.f;
#pragma unroll
  for (int r = 0; r < 4; r++) {
    const float bb = fg4 + (float)r;
    const float th = bb * flo * (1.f / 512);
    twc[r] = fcos(th); tws[r] = fsin(th);
    const float ph = bb * flo * (1.f / 16);
    const float cc = fcos(ph), ss = fsin(ph);
    F16re[r] = pk(cc, ss); F16im[r] = pk(-ss, cc);
    const float ps = bb * (flo + 1.f) * (1.f / 32);
    const u32 sp = pk(fcos(ps), -fsin(ps));
    SPre[r] = (lo & 1) ? (sp & 0xFFFFu) : (sp << 16);
    SPim[r] = (lo & 1) ? (sp >> 16) : (sp & 0xFFFF0000u);
    const float al = (float)((lo & 7) * (256 * (4 * (g & 1) + r) + c)) * (1.f / 2048);
    const float ca = fcos(al) * szero, sa = fsin(al) * szero;
    Sre[r] = pk(ca, sa); Sim[r] = pk(-sa, ca);
  }
  const int col0 = lo == 0 ? 64 * g + 15 : (g < 2 ? lo + 128 * g - 1 : 511 - lo - 128 * g);
  const int cstep = lo == 0 ? 16 : (g < 2 ? 32 : -32);
  SBAR;

  *(LAS v4u*)(L + L_X + w * 1024 + lane * 16) = xv;
  ST(1);
  {
    float s = 0.f, q = 0.f;
#pragma unroll
    for (int i = 0; i < 4; i++) { s = dot2(xv[i], 0x3F803F80u, s); q = dot2(xv[i], xv[i], q); }
    const v2u xa0 = trread(L + L_X + w * 1024 + lane * 8), xa1 = trread(L + L_X + w * 1024 + 512 + lane * 8);
    const v4u xa = {xa0.x, xa0.y, xa1.x, xa1.y};
    const f32x4 z4 = {0.f, 0.f, 0.f, 0.f};
    f32x4 Tre = mfma(xa, F32re, z4), Tim = mfma(xa, F32im, z4);
    wavesum2(s, q);
    const float mean = s * (1.f / D);
    const float var = fmaxf(q * (1.f / D) - mean * mean, 0.f);
    const float rstd = __builtin_amdgcn_rsqf(var + 1e-5f);
    const float msub = lo == 0 ? 32.f * mean : 0.f;
    v4u P;
#pragma unroll
    for (int r = 0; r < 4; r++) {
      const float wc = twc[r] * rstd, wsn = tws[r] * rstd;
      const float re = Tre[r] - msub, im = Tim[r];
      P[r] = pk(re * wc + im * wsn, im * wc - re * wsn);
    }
    const u32 sg = lo >= 8 ? 0x80008000u : 0u;
    const v4u F16imc = {F16im[0] ^ sg, F16im[1] ^ sg, F16im[2] ^ sg, F16im[3] ^ sg};
    const f32x4 Zre = mfma(F16re, P, z4), Zim = mfma(F16imc, P, z4), Yre = mfma(SPre, P, z4), Yim = mfma(SPim, P, z4);
    LAS u32* Zb = (LAS u32*)(L + L_P);
#pragma unroll
    for (int r = 0; r < 4; r++) {
      const u32 zg = pk(Zre[r], Zim[r]), zs = pk(Yre[r], Yim[r]);
      Zb[w * 260 + col0 + r * cstep] = lo == 0 ? zs : zg;
    }
    *(LAS v4u*)(L + L_R + t * 16) = wv;
    asm volatile("" ::"v"(pf));
    DRAIN();
    LBAR();
    v4u za;
#pragma unroll
    for (int j = 0; j < 4; j++) za[j] = Zb[(4 * (g & 1) + j) * 260 + 32 * w + 16 * (g >> 1) + lo];
    const f32x4 Ore = mfma(za, Sre, z4), Oim = mfma(za, Sim, z4);
    const int cB = (lo & 7) * 32 + 4 * w + 2 * (lo >> 3) + (g >> 1);
    const int o = OFF_X1 + c * 16384 + ((((cB >> 2) + c) & 63) << 7) + (cB & 3) * 32 + (g & 1) * 16;
    const v4u s0 = {pk(Ore[0], Oim[0]), tag, pk(Ore[1], Oim[1]), tag}, s1 = {pk(Ore[2], Oim[2]), tag, pk(Ore[3], Oim[3]), tag};
    __builtin_amdgcn_raw_buffer_store_b128(s0, rs, o, 0, SC1);
    __builtin_amdgcn_raw_buffer_store_b128(s1, rs, o + 8192, 0, SC1);
  }
  ST(2);
  SBAR;

  v4u W1[8][4];
  {
    const int vo = (32 * w + g) * (F * 2) + min(128 * fg + 8 * lo, F * 2 - 8);
#pragma unroll
    for (int j = 0; j < 8; j++) {
      v2u r[8];
#pragma unroll
      for (int i = 0; i < 8; i++) r[i] = __builtin_amdgcn_raw_buffer_load_b64(rw1, vo, (256 * i + 4 * j) * (F * 2), 0);
      transpose8x4(r, W1[j]);
    }
  }
  float t2c[4], t2s[4], fWr[4], fWi[4];
#pragma unroll
  for (int r = 0; r < 4; r++) {
    const float th = (fg4 + (float)r) * flo * (1.f / 256);
    t2c[r] = fcos(th); t2s[r] = fsin(th);
    const int k2 = lo + 16 * (4 * g + r);
    fWr[r] = blo(*(LAS u16*)(L + L_R + k2 * 16 + w * 2)) * (2.f / (S * D));
    fWi[r] = blo(*(LAS u16*)(L + L_R + 4096 + k2 * 16 + w * 2)) * (2.f / (S * D));
  }
  SBAR;
  DRAIN();
  ST(3);
  const int uB = t >> 1, hfB = t & 1;
  const int n2B = 16 * (4 * (uB >> 6) + (uB & 3)) + ((uB >> 2) & 15);
  {
    const int o = OFF_X1 + n2B * 16384 + ((((c >> 2) + n2B) & 63) << 7) + (c & 3) * 32 + hfB * 16;
    v4u a0, a1;
    for (int sp = 0; sp < SPIN_CAP; sp++) {
      MEMF;
      a0 = __builtin_amdgcn_raw_buffer_load_b128(rs, o, 0, SC1);
      a1 = __builtin_amdgcn_raw_buffer_load_b128(rs, o + 8192, 0, SC1);
      const u32 bad = (a0.y ^ tag) | (a0.w ^ tag) | (a1.y ^ tag) | (a1.w ^ tag);
      if (__all(bad == 0)) break;
    }
    ST(4);
    LAS u32* Bb = (LAS u32*)(L + L_Q);
    Bb[hfB * 1040 + uB] = a0.x; Bb[hfB * 1040 + 256 + uB] = a0.z; Bb[hfB * 1040 + 512 + uB] = a1.x; Bb[hfB * 1040 + 768 + uB] = a1.z;
    LBAR();
    if (b == 0 && t == 0) __builtin_amdgcn_raw_buffer_store_b32(tag, rs, 0, 0, SC1);
    LAS v4u* colp = (LAS v4u*)(Bb + (w >> 2) * 1040 + (w & 3) * 256) + lane;
    v4u P = *colp;
    const f32x4 z4 = {0.f, 0.f, 0.f, 0.f};
    f32x4 re = mfma(P, F16re, z4), im = mfma(P, F16im, z4);
#pragma unroll
    for (int r = 0; r < 4; r++) P[r] = pk(re[r] * t2c[r] + im[r] * t2s[r], im[r] * t2c[r] - re[r] * t2s[r]);
    re = mfma(F16re, P, z4); im = mfma(F16im, P, z4);
#pragma unroll
    for (int r = 0; r < 4; r++) P[r] = pk(re[r] * fWi[r] + im[r] * fWr[r], re[r] * fWr[r] - im[r] * fWi[r]);
    im = mfma(P, F16re, z4); re = mfma(P, F16im, z4);
#pragma unroll
    for (int r = 0; r < 4; r++) P[r] = pk(im[r] * t2c[r] + re[r] * t2s[r], re[r] * t2c[r] - im[r] * t2s[r]);
    im = mfma(F16re, P, z4); re = mfma(F16im, P, z4);
#pragma unroll
    for (int r = 0; r < 4; r++) P[r] = clean(pk(re[r], im[r]));
    *colp = P;
    LBAR();
    const v4u y = {Bb[hfB * 1040 + uB], Bb[hfB * 1040 + 256 + uB], Bb[hfB * 1040 + 512 + uB], Bb[hfB * 1040 + 768 + uB]};
    __builtin_amdgcn_raw_buffer_store_b128(y, rs, OFF_Y1 + c * 8192 + ((((n2B >> 2) + c) & 63) << 7) + (n2B & 3) * 32 + hfB * 16, 0, SC1);
  }
  ST(5);
  SBAR;
  v4u Ire, Iim, Gre, Gim, Hre, Him, E0, E1;
#pragma unroll
  for (int r = 0; r < 4; r++) {
    const float al = (float)((4 * (g & 1) + r) * (256 * (lo & 7) + c)) * (1.f / 2048);
    const float ca = fcos(al) * szero, sa = fsin(al) * szero;
    Ire[r] = pk(ca, -sa); Iim[r] = pk(sa, ca);
    Gre[r] = F16re[r] ^ (g < 2 ? 0x80000000u : 0u);
    Gim[r] = F16im[r] ^ (g < 2 ? 0x00008000u : 0x80008000u);
    const float ps = flo * (fg4 + (float)(r + 1)) * (1.f / 32);
    const float wg = (r == 3 && g == 3) ? 0.5f : 1.f;
    const u32 sp = pk(fcos(ps) * wg, -fsin(ps));
    Hre[r] = (r & 1) ? sp : 0u;
    Him[r] = (r & 1) ? 0u : sp;
    const float be = flo * (fg4 + (float)r) * (1.f / 32);
    u32 e = pk(fcos(be), -fsin(be));
    if (r == 0) e = g == 0 ? ((lo & 1) ? 0xBF803F80u : 0x3F803F80u) : e;
    E0[r] = e;
    E1[r] = (r & 1) ? e ^ 0x80008000u : e;
  }
  SBAR;
  DRAIN();
  ST(6);
  {
    const int pB = (lane >> 3) * 32 + 4 * w + ((lane >> 1) & 3);
    const int o = OFF_Y1 + pB * 8192 + ((((c >> 2) + pB) & 63) << 7) + (c & 3) * 32 + (lane & 1) * 16;
    v4u a;
    for (int sp = 0; sp < SPIN_CAP; sp++) {
      MEMF;
      a = __builtin_amdgcn_raw_buffer_load_b128(rs, o, 0, SC1);
      if (__all(max(umax3(a.x, a.y, a.z), a.w) != ~0u)) break;
    }
    ST(7);
    LAS u32* Cb = (LAS u32*)(L + L_P) + w * 288;
    *(LAS v4u*)(Cb + (lane >> 3) * 36 + (lane & 7) * 4) = a;
    v4u P;
#pragma unroll
    for (int j = 0; j < 4; j++) P[j] = Cb[(4 * (g & 1) + j) * 36 + 16 * (g >> 1) + lo];
    const f32x4 z4 = {0.f, 0.f, 0.f, 0.f};
    f32x4 re = mfma(P, Ire, z4), im = mfma(P, Iim, z4);
    LAS u32* Qb = (LAS u32*)(L + L_R);
#pragma unroll
    for (int r = 0; r < 4; r++) P[r] = pk(re[r], im[r]);
    *(LAS v4u*)(Qb + (lo & 7) * 260 + 32 * w + 16 * (lo >> 3) + 4 * g) = P;
    const v2u x0 = *(LAS v2u*)(L + L_X + w * 1024 + 32 * lo + 8 * g), x1 = *(LAS v2u*)(L + L_X + w * 1024 + 512 + 32 * lo + 8 * g);
    LBAR();
#pragma unroll
    for (int r = 0; r < 4; r++) P[r] = Qb[w * 260 + col0 + r * cstep];
    re = mfma(P, Gre, z4); im = mfma(P, Gim, z4);
    const f32x4 sre = mfma(P, Hre, z4), sim = mfma(P, Him, z4);
    re[0] = g == 0 ? sre[0] : re[0];
    im[0] = g == 0 ? sim[0] : im[0];
#pragma unroll
    for (int r = 0; r < 4; r++) P[r] = pk(re[r] * twc[r] - im[r] * tws[r], im[r] * twc[r] + re[r] * tws[r]);
    const f32x4 y0 = mfma(P, E0, z4), y1 = mfma(P, E1, z4);
    v4u h;
    h[0] = pk(blo(x0.x) + y0[0], bhi(x0.x) + y0[1]); h[1] = pk(blo(x0.y) + y0[2], bhi(x0.y) + y0[3]);
    h[2] = pk(blo(x1.x) + y1[0], bhi(x1.x) + y1[1]); h[3] = pk(blo(x1.y) + y1[2], bhi(x1.y) + y1[3]);
    float s = 0.f, q = 0.f;
#pragma unroll
    for (int i = 0; i < 4; i++) { s = dot2(h[i], 0x3F803F80u, s); q = dot2(h[i], h[i], q); }
    wavesum2(s, q);
    const float mean = s * (1.f / D);
    const float var = fmaxf(q * (1.f / D) - mean * mean, 0.f);
    const float r1 = __builtin_amdgcn_rsqf(var + 1e-5f);
    const float sc = r1 * __builtin_amdgcn_rsqf(var * r1 * r1 + 1e-5f);
    const float mo = -mean * sc;
    v2u n0, n1;
    n0.x = pk(blo(h[0]) * sc + mo, bhi(h[0]) * sc + mo); n0.y = pk(blo(h[1]) * sc + mo, bhi(h[1]) * sc + mo);
    n1.x = pk(blo(h[2]) * sc + mo, bhi(h[2]) * sc + mo); n1.y = pk(blo(h[3]) * sc + mo, bhi(h[3]) * sc + mo);
    *(LAS v2u*)(L + L_Q + w * 1088 + 32 * lo + 8 * g) = n0;
    *(LAS v2u*)(L + L_Q + w * 1088 + 512 + 32 * lo + 8 * g) = n1;
    *(LAS v2u*)(L + L_X + w * 1024 + 32 * lo + 8 * g) = v2u{clean(h[0]), clean(h[1])};
    *(LAS v2u*)(L + L_X + w * 1024 + 512 + 32 * lo + 8 * g) = v2u{clean(h[2]), clean(h[3])};
    LBAR();
    LAS unsigned char* tp = L + L_Q + (lo >> 2) * 1088 + 128 * w + 32 * g + 8 * (lo & 3);
    const v2u t0 = trread(tp), t1 = trread(tp + 4 * 1088);
    const v4u hv = *(LAS v4u*)(L + L_X + w * 1024 + lane * 16);
    const int pos = 64 * w + 16 * ((lane >> 1) & 3) + 2 * (lane >> 3) + (lane & 1);
    const v4u hn = {clean(t0.x), clean(t0.y), clean(t1.x), clean(t1.y)};
    __builtin_amdgcn_raw_buffer_store_b128(hn, rs, OFF_HN + c * 8192 + pos * 16, 0, SC1);
    __builtin_amdgcn_raw_buffer_store_b128(hv, rs, OFF_H + (256 * w + c) * 1024 + lane * 16, 0, SC1);
  }
  ST(8);
  SBAR;
  const int f0 = 64 * fg + 16 * (w & 3) + 8 * (lane & 1) + 2 * (w >> 2) + ((lane >> 1) & 1);
  const bool fok = f0 < F;
  const float bia0 = blo(b1[fok ? f0 : 0]), bia1 = blo(b1[fok ? f0 + 4 : 0]);
  LBAR();
  DRAIN();
  ST(9);
  if (g1on) {
    const int ho = OFF_HN + (32 * w + g) * 8192 + (48 * db + lo) * 16;
    v4u A[2][8];
#pragma unroll
    for (int st = 0; st < 2; st++)
#pragma unroll
      for (int j = 0; j < 8; j++) A[st][j] = __builtin_amdgcn_raw_buffer_load_b128(rs, ho + st * 256, j * 32768, SC1);
    SBAR;
#pragma unroll
    for (int st = 0; st < 3; st++) {
      if (st < nst) {
        f32x4 acc[4];
        for (int sp = 0; sp < SPIN_CAP; sp++) {
          u32 mx = 0;
#pragma unroll
          for (int cc = 0; cc < 4; cc++) acc[cc] = f32x4{0.f, 0.f, 0.f, 0.f};
#pragma unroll
          for (int j = 0; j < 8; j++) {
            const v4u a = A[st & 1][j];
#pragma unroll
            for (int cc = 0; cc < 4; cc++) acc[cc] = mfma(W1[j][cc], a, acc[cc]);
            mx = umax3(umax3(mx, a.x, a.y), a.z, a.w);
          }
          if (__all(mx != ~0u)) break;
          MEMF;
#pragma unroll
          for (int j = 0; j < 8; j++) A[st & 1][j] = __builtin_amdgcn_raw_buffer_load_b128(rs, ho + st * 256, j * 32768, SC1);
        }
        if (st == 0 && nst == 3) {
#pragma unroll
          for (int j = 0; j < 8; j++) A[0][j] = __builtin_amdgcn_raw_buffer_load_b128(rs, ho + 512, j * 32768, SC1);
        }
        LAS unsigned char* R = L + st * 33792;
#pragma unroll
        for (int cc = 0; cc < 4; cc++) *(LAS f32x4*)(R + w * 4224 + (cc >> 1) * 1024 + (cc & 1) * 2176 + lane * 16) = acc[cc];
        LBAR();
        LAS unsigned char* rp = R + (w >> 2) * 1024 + ((lane >> 1) & 1) * 2176 + ((w & 3) * 16 + (lane >> 2)) * 16 + (lane & 1) * 8;
        f32x2 v = {bia0, bia1};
#pragma unroll
        for (int ww = 0; ww < 8; ww++) v += *(LAS f32x2*)(rp + ww * 4224);
        float o2[2];
#pragma unroll
        for (int e = 0; e < 2; e++) {
          const float u = v[e];
          const float z = u * (u * u * (-0.044715f * 2.f * 0.7978845608f * 1.4426950409f) + (-2.f * 0.7978845608f * 1.4426950409f));
          o2[e] = u * __builtin_amdgcn_rcpf(1.f + __builtin_amdgcn_exp2f(z));
        }
        const u32 uo = fok ? clean(pk(o2[0], o2[1])) : 0u;
        v4u ov;
        ov.x = uo;
        ov.y = (u32)__builtin_amdgcn_update_dpp(0, (int)uo, 0x55, 0xf, 0xf, true);
        ov.z = (u32)__builtin_amdgcn_update_dpp(0, (int)uo, 0xAA, 0xf, 0xf, true);
        ov.w = (u32)__builtin_amdgcn_update_dpp(0, (int)uo, 0xFF, 0xf, 0xf, true);
        if ((lane & 3) == 0) __builtin_amdgcn_raw_buffer_store_b128(ov, rs, OFF_U + ((3 * db + st) * 44 + 2 * fg) * 1024 + t * 4, 0, SC1);
      }
    }
  }
  ST(10);
  SBAR;
  {
    const int d2 = 2 * (xc >> 1) + (xi >> 4), sb = 16 * (xc & 1) + (xi & 15);
    const int j0 = w < 4 ? 6 * w : 4 + 5 * w;
    const bool six = w < 4;
    v4u W2[6][4];
    {
      const int vo = 16 * g * (S * 2) + 128 * sb + 8 * lo;
#pragma unroll
      for (int j = 0; j < 6; j++) {
        if (j < 5 || six) {
          const int jg = j0 + j;
          const int fb = 64 * (jg >> 1) + 2 * (jg & 1);
          v2u r[8];
#pragma unroll
          for (int e = 0; e < 8; e++) r[e] = __builtin_amdgcn_raw_buffer_load_b64(rw2, vo + (fb + 4 * (e & 3) + (e >> 2)) * (S * 2), 0, 0);
          transpose8x4(r, W2[j]);
        }
      }
    }
    const int sl = t >> 3, q8 = t & 7;
    const int so = 64 * sb + 4 * (sl & 15) + (sl >> 4);
    const float bias2 = blo(b2[so]);
    LBAR();
    DRAIN();
    ST(11);
    const int uo = OFF_U + (4 * d2 * 44 + j0) * 1024 + lane * 16;
    v4u A[4][6];
#pragma unroll
    for (int st = 0; st < 4; st++)
#pragma unroll
      for (int j = 0; j < 6; j++)
        if (j < 5 || six) A[st][j] = __builtin_amdgcn_raw_buffer_load_b128(rs, uo, (st * 44 + j) * 1024, SC1);
    const int hoff = OFF_H + so * 1024 + d2 * 128 + q8 * 16;
    v4u hv = __builtin_amdgcn_raw_buffer_load_b128(rs, hoff, 0, SC1);
    SBAR;
    f32x2 res[4];
#pragma unroll
    for (int st = 0; st < 4; st++) {
      f32x4 acc[4];
      for (int sp = 0; sp < SPIN_CAP; sp++) {
        u32 mx = 0;
#pragma unroll
        for (int cc = 0; cc < 4; cc++) acc[cc] = f32x4{0.f, 0.f, 0.f, 0.f};
#pragma unroll
        for (int j = 0; j < 6; j++) {
          if (j < 5 || six) {
            const v4u a = A[st][j];
#pragma unroll
            for (int cc = 0; cc < 4; cc++) acc[cc] = mfma(a, W2[j][cc], acc[cc]);
            mx = umax3(umax3(mx, a.x, a.y), a.z, a.w);
          }
        }
        if (__all(mx != ~0u)) break;
        MEMF;
#pragma unroll
        for (int j = 0; j < 6; j++)
          if (j < 5 || six) A[st][j] = __builtin_amdgcn_raw_buffer_load_b128(rs, uo, (st * 44 + j) * 1024, SC1);
      }
      LAS unsigned char* R = L + st * 32768;
#pragma unroll
      for (int cc = 0; cc < 4; cc++) *(LAS f32x4*)(R + w * 4096 + cc * 1024 + g * 256 + ((lo ^ (4 * g)) * 16)) = acc[cc];
      LBAR();
      LAS unsigned char* rp = R + (sl >> 4) * 1024 + (q8 >> 1) * 256 + (((sl & 15) ^ (4 * (q8 >> 1))) * 16) + (q8 & 1) * 8;
      f32x2 v = {bias2, bias2};
#pragma unroll
      for (int ww = 0; ww < 8; ww++) v += *(LAS f32x2*)(rp + ww * 4096);
      res[st] = v;
    }
    for (int sp = 0; sp < SPIN_CAP; sp++) {
      if (__all(max(umax3(hv.x, hv.y, hv.z), hv.w) != ~0u)) break;
      MEMF;
      hv = __builtin_amdgcn_raw_buffer_load_b128(rs, hoff, 0, SC1);
    }
    v4u o;
#pragma unroll
    for (int st = 0; st < 4; st++) o[st] = pk(blo(hv[st]) + res[st][0], bhi(hv[st]) + res[st][1]);
    *(v4u*)(out + so * D + d2 * 64 + q8 * 8) = o;
  }
  if (w == 0) { const unsigned long long c0_ = *(volatile unsigned long long*)(ws + 4096 + b * 8); unsigned long long e0 = __builtin_amdgcn_s_memrealtime(), lim = (c1_ - c0_) * MULT; if (lim > 20000) lim = 20000; while (__builtin_amdgcn_s_memrealtime() - e0 < lim) __builtin_amdgcn_s_sleep(1); }
}

__global__ void kfirst(unsigned char* ws) { if (threadIdx.x == 0) *(unsigned long long*)(ws + 4096 + blockIdx.x * 8) = __builtin_amdgcn_s_memrealtime(); }
extern "C" void kernel_launch(void* const* d_in, const int* in_sizes, int n_in, void* d_out, int out_size, void* d_ws, size_t ws_size,
                              hipStream_t stream) {
  (void)hipFuncSetAttribute((const void*)fused_kernel, hipFuncAttributeMaxDynamicSharedMemorySize, LDS_BYTES);
  kfirst<<<256, 64, 0, stream>>>((unsigned char*)d_ws);
  fused_kernel<<<256, 512, LDS_BYTES, stream>>>((const u16*)d_in[0], (const u16*)d_in[1], (const u16*)d_in[2], (const u16*)d_in[4],
                                                (const u16*)d_in[5], (const u16*)d_in[6], (const u16*)d_in[7], (u16*)d_out,
                                                (unsigned char*)d_ws);
}
```

```cpp
#define MULT 0
#define NLD 6
#include <hip/hip_runtime.h>

typedef unsigned short u16;
typedef unsigned u32;
typedef u32 v4u __attribute__((ext_vector_type(4)));
typedef u32 v2u __attribute__((ext_vector_type(2)));
typedef short v4s __attribute__((ext_vector_type(4)));
typedef float f32x2 __attribute__((ext_vector_type(2)));
typedef float f32x4 __attribute__((ext_vector_type(4)));
typedef __bf16 bf16x2 __attribute__((ext_vector_type(2)));
typedef __bf16 bf16x8 __attribute__((ext_vector_type(8)));
#define LAS __attribute__((address_space(3)))
#define GAS __attribute__((address_space(1)))
#define DEV __device__ __forceinline__

constexpr int S = 2048, D = 512, DF = 257, F = 1376;
constexpr int OFF_X1 = 2 << 20, OFF_Y1 = 6 << 20, OFF_HN = 8 << 20, OFF_H = 10 << 20, OFF_U = 12 << 20, WS_USED = 14 << 20;
constexpr int SC1 = 16;
constexpr int SPIN_CAP = 1 << 16;
constexpr int CODE_LINES = 128;

constexpr int L_X = 0;
constexpr int L_P = 8192;
constexpr int L_Q = L_P + 9216;
constexpr int L_R = L_Q + 8704;
constexpr int L_END = L_R + 8320;
constexpr int LDS_BYTES = 131072 + 1024;

DEV float asf(u32 v) { return __builtin_bit_cast(float, v); }
DEV u32 asu(float v) { return __builtin_bit_cast(u32, v); }
DEV u32 pk(float lo, float hi) { f32x2 v = {lo, hi}; return __builtin_bit_cast(u32, __builtin_convertvector(v, bf16x2)); }
DEV float blo(u32 v) { return asf(v << 16); }
DEV float bhi(u32 v) { return asf(v & 0xFFFF0000u); }
DEV f32x4 mfma(v4u a, v4u b, f32x4 c) {
  return __builtin_amdgcn_mfma_f32_16x16x32_bf16(__builtin_bit_cast(bf16x8, a), __builtin_bit_cast(bf16x8, b), c, 0, 0, 0);
}
DEV float dot2(u32 a, u32 b, float c) {
  return __builtin_amdgcn_fdot2_f32_bf16(__builtin_bit_cast(bf16x2, a), __builtin_bit_cast(bf16x2, b), c, false);
}
DEV float fsin(float rev) { return __builtin_amdgcn_sinf(rev); }
DEV float fcos(float rev) { return __builtin_amdgcn_cosf(rev); }
DEV v2u trread(LAS unsigned char* p) { return __builtin_bit_cast(v2u, __builtin_amdgcn_ds_read_tr16_b64_v4i16((LAS v4s*)p)); }
DEV u32 umax3(u32 a, u32 b, u32 c) { return max(max(a, b), c); }
DEV u32 clean(u32 v) { return min(v, 0xFFFFFFFEu); }
template <int CTRL, int RM> DEV float dppadd(float v) {
  return v + asf((u32)__builtin_amdgcn_update_dpp(0, (int)asu(v), CTRL, RM, 0xf, true));
}
DEV void wavesum2(float& s, float& q) {
  auto sw = __builtin_amdgcn_permlane32_swap(asu(s), asu(q), false, false);
  u32 a = sw[0], b = sw[1];
  float e = asf(a) + asf(b);
  e = dppadd<0x128, 0xf>(e);
  e = dppadd<0x124, 0xf>(e);
  e = dppadd<0x122, 0xf>(e);
  e = dppadd<0x121, 0xf>(e);
  e = dppadd<0x142, 0xa>(e);
  s = asf((u32)__builtin_amdgcn_readlane((int)asu(e), 31));
  q = asf((u32)__builtin_amdgcn_readlane((int)asu(e), 63));
}
#define MEMF asm volatile("" ::: "memory")
#define SBAR __builtin_amdgcn_sched_barrier(0)
#define LBAR() do { asm volatile("s_waitcnt lgkmcnt(0)" ::: "memory"); __builtin_amdgcn_s_barrier(); MEMF; } while (0)
#define DRAIN() asm volatile("s_waitcnt vmcnt(0)" ::: "memory")

#define ST(i)

struct __attribute__((packed, aligned(2))) U16x8 { v4u v; };

DEV void transpose8x4(const v2u (&r)[8], v4u (&f)[4]) {
#pragma unroll
  for (int p = 0; p < 4; p++) {
    f[0][p] = __builtin_amdgcn_perm(r[2 * p + 1].x, r[2 * p].x, 0x05040100);
    f[1][p] = __builtin_amdgcn_perm(r[2 * p + 1].x, r[2 * p].x, 0x07060302);
    f[2][p] = __builtin_amdgcn_perm(r[2 * p + 1].y, r[2 * p].y, 0x05040100);
    f[3][p] = __builtin_amdgcn_perm(r[2 * p + 1].y, r[2 * p].y, 0x07060302);
  }
}

__global__ __launch_bounds__(512, 2) void fused_kernel(const u16* __restrict__ x, const u16* __restrict__ wr, const u16* __restrict__ wi,
                                                       const u16* __restrict__ w1, const u16* __restrict__ b1, const u16* __restrict__ w2,
                                                       const u16* __restrict__ b2, u16* __restrict__ out, unsigned char* ws) {
  extern __shared__ __attribute__((aligned(16))) unsigned char smem_[];
  LAS unsigned char* L = (LAS unsigned char*)smem_;
  ST(0);
  const int t = threadIdx.x, lane = t & 63, lo = lane & 15, g = lane >> 4;
  const int w = __builtin_amdgcn_readfirstlane(t >> 6);
  const int b = blockIdx.x, c = (b & 7) * 32 + (b >> 3);
  const __amdgpu_buffer_rsrc_t rs = __builtin_amdgcn_make_buffer_rsrc((void*)ws, 0, WS_USED, 0x00020000);
  const __amdgpu_buffer_rsrc_t rw1 = __builtin_amdgcn_make_buffer_rsrc((void*)w1, 0, S * F * 2, 0x00020000);
  const __amdgpu_buffer_rsrc_t rw2 = __builtin_amdgcn_make_buffer_rsrc((void*)w2, 0, S * F * 2, 0x00020000);

  unsigned long long c0_, c1_;
  {
    asm volatile("s_memrealtime %0\n\ts_waitcnt lgkmcnt(0)" : "=s"(c0_)::"memory");
    v4u q[NLD];
#pragma unroll
    for (int i = 0; i < NLD; i++) q[i] = __builtin_amdgcn_raw_buffer_load_b128((i & 1) ? rw1 : rw2, (b * 3 + (i >> 1)) * 7168 + t * 16, 0, 0);
    u32 acc = 0;
#pragma unroll
    for (int i = 0; i < NLD; i++) acc ^= q[i].x ^ q[i].y ^ q[i].z ^ q[i].w;
    asm volatile("" ::"v"(acc) : "memory");
    __builtin_amdgcn_s_barrier();
    asm volatile("s_memrealtime %0\n\ts_waitcnt lgkmcnt(0)" : "=s"(c1_)::"memory");
  }
  SBAR;
  const v4u xv = *(const v4u*)(x + (256 * w + c) * D + lane * 8);
  const u32 tag = __builtin_amdgcn_raw_buffer_load_b32(rs, 0, 0, SC1) + 1u;
  u32 pf = 0;
  if (t < CODE_LINES) {
    unsigned long long pc;
    asm volatile("s_getpc_b64 %0" : "=s"(pc));
    pf = *(const GAS u32*)((pc & ~127ull) + t * 128);
  }
  const int k1B = c >> 5, blkB = c & 31;
  const v4u wv = ((const U16x8*)(((t >> 8) ? wi : wr) + (k1B + 8 * (t & 255)) * DF + 8 * blkB + 1))->v;

  const int xc = b & 7, xi = b >> 3;
  const int q1 = xc >> 1, tl = q1 * 30 + (q1 ? 1 : 0) + xi;
  const bool g1on = xi < (q1 ? 30 : 31);
  const int db = tl / 11, fg = (xc & 1) * 11 + tl % 11;
  const int nst = db == 10 ? 2 : 3;
  {
    const v4u sent = {~0u, ~0u, ~0u, ~0u};
    __builtin_amdgcn_raw_buffer_store_b128(sent, rs, OFF_Y1 + c * 8192 + t * 16, 0, SC1);
    __builtin_amdgcn_raw_buffer_store_b128(sent, rs, OFF_HN + c * 8192 + t * 16, 0, SC1);
    __builtin_amdgcn_raw_buffer_store_b128(sent, rs, OFF_H + (256 * w + c) * 1024 + lane * 16, 0, SC1);
    if (g1on && (w >> 1) < nst)
      __builtin_amdgcn_raw_buffer_store_b128(sent, rs, OFF_U + ((3 * db + (w >> 1)) * 44 + 2 * fg) * 1024 + (t & 127) * 16, 0, SC1);
  }
  SBAR;

  const float flo = (float)lo, fg4 = (float)(4 * g);
  v4u F32re, F32im;
#pragma unroll
  for (int p = 0; p < 4; p++) {
    const float a0 = fg4 + (float)(16 * (p >> 1) + 2 * (p & 1));
    const float r0 = a0 * flo * (1.f / 32), r1 = (a0 + 1.f) * flo * (1.f / 32);
    F32re[p] = pk(fcos(r0), fcos(r1));
    const u32 im = pk(-fsin(r0), -fsin(r1));
    F32im[p] = lo == 0 ? 0xBF803F80u : im;
  }
  float twc[4], tws[4];
  v4u F16re, F16im, SPre, SPim, Sre, Sim;
  const float szero = ((lo >> 3) == (g >> 1)) ? 1.f : 0.f;
#pragma unroll
  for (int r = 0; r < 4; r++) {
    const float bb = fg4 + (float)r;
    const float th = bb * flo * (1.f / 512);
    twc[r] = fcos(th); tws[r] = fsin(th);
    const float ph = bb * flo * (1.f / 16);
    const float cc = fcos(ph), ss = fsin(ph);
    F16re[r] = pk(cc, ss); F16im[r] = pk(-ss, cc);
    const float ps = bb * (flo + 1.f) * (1.f / 32);
    const u32 sp = pk(fcos(ps), -fsin(ps));
    SPre[r] = (lo & 1) ? (sp & 0xFFFFu) : (sp << 16);
    SPim[r] = (lo & 1) ? (sp >> 16) : (sp & 0xFFFF0000u);
    const float al = (float)((lo & 7) * (256 * (4 * (g & 1) + r) + c)) * (1.f / 2048);
    const float ca = fcos(al) * szero, sa = fsin(al) * szero;
    Sre[r] = pk(ca, sa); Sim[r] = pk(-sa, ca);
  }
  const int col0 = lo == 0 ? 64 * g + 15 : (g < 2 ? lo + 128 * g - 1 : 511 - lo - 128 * g);
  const int cstep = lo == 0 ? 16 : (g < 2 ? 32 : -32);
  SBAR;

  *(LAS v4u*)(L + L_X + w * 1024 + lane * 16) = xv;
  ST(1);
  {
    float s = 0.f, q = 0.f;
#pragma unroll
    for (int i = 0; i < 4; i++) { s = dot2(xv[i], 0x3F803F80u, s); q = dot2(xv[i], xv[i], q); }
    const v2u xa0 = trread(L + L_X + w * 1024 + lane * 8), xa1 = trread(L + L_X + w * 1024 + 512 + lane * 8);
    const v4u xa = {xa0.x, xa0.y, xa1.x, xa1.y};
    const f32x4 z4 = {0.f, 0.f, 0.f, 0.f};
    f32x4 Tre = mfma(xa, F32re, z4), Tim = mfma(xa, F32im, z4);
    wavesum2(s, q);
    const float mean = s * (1.f / D);
    const float var = fmaxf(q * (1.f / D) - mean * mean, 0.f);
    const float rstd = __builtin_amdgcn_rsqf(var + 1e-5f);
    const float msub = lo == 0 ? 32.f * mean : 0.f;
    v4u P;
#pragma unroll
    for (int r = 0; r < 4; r++) {
      const float wc = twc[r] * rstd, wsn = tws[r] * rstd;
      const float re = Tre[r] - msub, im = Tim[r];
      P[r] = pk(re * wc + im * wsn, im * wc - re * wsn);
    }
    const u32 sg = lo >= 8 ? 0x80008000u : 0u;
    const v4u F16imc = {F16im[0] ^ sg, F16im[1] ^ sg, F16im[2] ^ sg, F16im[3] ^ sg};
    const f32x4 Zre = mfma(F16re, P, z4), Zim = mfma(F16imc, P, z4), Yre = mfma(SPre, P, z4), Yim = mfma(SPim, P, z4);
    LAS u32* Zb = (LAS u32*)(L + L_P);
#pragma unroll
    for (int r = 0; r < 4; r++) {
      const u32 zg = pk(Zre[r], Zim[r]), zs = pk(Yre[r], Yim[r]);
      Zb[w * 260 + col0 + r * cstep] = lo == 0 ? zs : zg;
    }
    *(LAS v4u*)(L + L_R + t * 16) = wv;
    asm volatile("" ::"v"(pf));
    DRAIN();
    LBAR();
    v4u za;
#pragma unroll
    for (int j = 0; j < 4; j++) za[j] = Zb[(4 * (g & 1) + j) * 260 + 32 * w + 16 * (g >> 1) + lo];
    const f32x4 Ore = mfma(za, Sre, z4), Oim = mfma(za, Sim, z4);
    const int cB = (lo & 7) * 32 + 4 * w + 2 * (lo >> 3) + (g >> 1);
    const int o = OFF_X1 + c * 16384 + ((((cB >> 2) + c) & 63) << 7) + (cB & 3) * 32 + (g & 1) * 16;
    const v4u s0 = {pk(Ore[0], Oim[0]), tag, pk(Ore[1], Oim[1]), tag}, s1 = {pk(Ore[2], Oim[2]), tag, pk(Ore[3], Oim[3]), tag};
    __builtin_amdgcn_raw_buffer_store_b128(s0, rs, o, 0, SC1);
    __builtin_amdgcn_raw_buffer_store_b128(s1, rs, o + 8192, 0, SC1);
  }
  ST(2);
  SBAR;

  v4u W1[8][4];
  {
    const int vo = (32 * w + g) * (F * 2) + min(128 * fg + 8 * lo, F * 2 - 8);
#pragma unroll
    for (int j = 0; j < 8; j++) {
      v2u r[8];
#pragma unroll
      for (int i = 0; i < 8; i++) r[i] = __builtin_amdgcn_raw_buffer_load_b64(rw1, vo, (256 * i + 4 * j) * (F * 2), 0);
      transpose8x4(r, W1[j]);
    }
  }
  float t2c[4], t2s[4], fWr[4], fWi[4];
#pragma unroll
  for (int r = 0; r < 4; r++) {
    const float th = (fg4 + (float)r) * flo * (1.f / 256);
    t2c[r] = fcos(th); t2s[r] = fsin(th);
    const int k2 = lo + 16 * (4 * g + r);
    fWr[r] = blo(*(LAS u16*)(L + L_R + k2 * 16 + w * 2)) * (2.f / (S * D));
    fWi[r] = blo(*(LAS u16*)(L + L_R + 4096 + k2 * 16 + w * 2)) * (2.f / (S * D));
  }
  SBAR;
  DRAIN();
  ST(3);
  const int uB = t >> 1, hfB = t & 1;
  const int n2B = 16 * (4 * (uB >> 6) + (uB & 3)) + ((uB >> 2) & 15);
  {
    const int o = OFF_X1 + n2B * 16384 + ((((c >> 2) + n2B) & 63) << 7) + (c & 3) * 32 + hfB * 16;
    v4u a0, a1;
    for (int sp = 0; sp < SPIN_CAP; sp++) {
      MEMF;
      a0 = __builtin_amdgcn_raw_buffer_load_b128(rs, o, 0, SC1);
      a1 = __builtin_amdgcn_raw_buffer_load_b128(rs, o + 8192, 0, SC1);
      const u32 bad = (a0.y ^ tag) | (a0.w ^ tag) | (a1.y ^ tag) | (a1.w ^ tag);
      if (__all(bad == 0)) break;
    }
    ST(4);
    LAS u32* Bb = (LAS u32*)(L + L_Q);
    Bb[hfB * 1040 + uB] = a0.x; Bb[hfB * 1040 + 256 + uB] = a0.z; Bb[hfB * 1040 + 512 + uB] = a1.x; Bb[hfB * 1040 + 768 + uB] = a1.z;
    LBAR();
    if (b == 0 && t == 0) __builtin_amdgcn_raw_buffer_store_b32(tag, rs, 0, 0, SC1);
    LAS v4u* colp = (LAS v4u*)(Bb + (w >> 2) * 1040 + (w & 3) * 256) + lane;
    v4u P = *colp;
    const f32x4 z4 = {0.f, 0.f, 0.f, 0.f};
    f32x4 re = mfma(P, F16re, z4), im = mfma(P, F16im, z4);
#pragma unroll
    for (int r = 0; r < 4; r++) P[r] = pk(re[r] * t2c[r] + im[r] * t2s[r], im[r] * t2c[r] - re[r] * t2s[r]);
    re = mfma(F16re, P, z4); im = mfma(F16im, P, z4);
#pragma unroll
    for (int r = 0; r < 4; r++) P[r] = pk(re[r] * fWi[r] + im[r] * fWr[r], re[r] * fWr[r] - im[r] * fWi[r]);
    im = mfma(P, F16re, z4); re = mfma(P, F16im, z4);
#pragma unroll
    for (int r = 0; r < 4; r++) P[r] = pk(im[r] * t2c[r] + re[r] * t2s[r], re[r] * t2c[r] - im[r] * t2s[r]);
    im = mfma(F16re, P, z4); re = mfma(F16im, P, z4);
#pragma unroll
    for (int r = 0; r < 4; r++) P[r] = clean(pk(re[r], im[r]));
    *colp = P;
    LBAR();
    const v4u y = {Bb[hfB * 1040 + uB], Bb[hfB * 1040 + 256 + uB], Bb[hfB * 1040 + 512 + uB], Bb[hfB * 1040 + 768 + uB]};
    __builtin_amdgcn_raw_buffer_store_b128(y, rs, OFF_Y1 + c * 8192 + ((((n2B >> 2) + c) & 63) << 7) + (n2B & 3) * 32 + hfB * 16, 0, SC1);
  }
  ST(5);
  SBAR;
  v4u Ire, Iim, Gre, Gim, Hre, Him, E0, E1;
#pragma unroll
  for (int r = 0; r < 4; r++) {
    const float al = (float)((4 * (g & 1) + r) * (256 * (lo & 7) + c)) * (1.f / 2048);
    const float ca = fcos(al) * szero, sa = fsin(al) * szero;
    Ire[r] = pk(ca, -sa); Iim[r] = pk(sa, ca);
    Gre[r] = F16re[r] ^ (g < 2 ? 0x80000000u : 0u);
    Gim[r] = F16im[r] ^ (g < 2 ? 0x00008000u : 0x80008000u);
    const float ps = flo * (fg4 + (float)(r + 1)) * (1.f / 32);
    const float wg = (r == 3 && g == 3) ? 0.5f : 1.f;
    const u32 sp = pk(fcos(ps) * wg, -fsin(ps));
    Hre[r] = (r & 1) ? sp : 0u;
    Him[r] = (r & 1) ? 0u : sp;
    const float be = flo * (fg4 + (float)r) * (1.f / 32);
    u32 e = pk(fcos(be), -fsin(be));
    if (r == 0) e = g == 0 ? ((lo & 1) ? 0xBF803F80u : 0x3F803F80u) : e;
    E0[r] = e;
    E1[r] = (r & 1) ? e ^ 0x80008000u : e;
  }
  SBAR;
  DRAIN();
  ST(6);
  {
    const int pB = (lane >> 3) * 32 + 4 * w + ((lane >> 1) & 3);
    const int o = OFF_Y1 + pB * 8192 + ((((c >> 2) + pB) & 63) << 7) + (c & 3) * 32 + (lane & 1) * 16;
    v4u a;
    for (int sp = 0; sp < SPIN_CAP; sp++) {
      MEMF;
      a = __builtin_amdgcn_raw_buffer_load_b128(rs, o, 0, SC1);
      if (__all(max(umax3(a.x, a.y, a.z), a.w) != ~0u)) break;
    }
    ST(7);
    LAS u32* Cb = (LAS u32*)(L + L_P) + w * 288;
    *(LAS v4u*)(Cb + (lane >> 3) * 36 + (lane & 7) * 4) = a;
    v4u P;
#pragma unroll
    for (int j = 0; j < 4; j++) P[j] = Cb[(4 * (g & 1) + j) * 36 + 16 * (g >> 1) + lo];
    const f32x4 z4 = {0.f, 0.f, 0.f, 0.f};
    f32x4 re = mfma(P, Ire, z4), im = mfma(P, Iim, z4);
    LAS u32* Qb = (LAS u32*)(L + L_R);
#pragma unroll
    for (int r = 0; r < 4; r++) P[r] = pk(re[r], im[r]);
    *(LAS v4u*)(Qb + (lo & 7) * 260 + 32 * w + 16 * (lo >> 3) + 4 * g) = P;
    const v2u x0 = *(LAS v2u*)(L + L_X + w * 1024 + 32 * lo + 8 * g), x1 = *(LAS v2u*)(L + L_X + w * 1024 + 512 + 32 * lo + 8 * g);
    LBAR();
#pragma unroll
    for (int r = 0; r < 4; r++) P[r] = Qb[w * 260 + col0 + r * cstep];
    re = mfma(P, Gre, z4); im = mfma(P, Gim, z4);
    const f32x4 sre = mfma(P, Hre, z4), sim = mfma(P, Him, z4);
    re[0] = g == 0 ? sre[0] : re[0];
    im[0] = g == 0 ? sim[0] : im[0];
#pragma unroll
    for (int r = 0; r < 4; r++) P[r] = pk(re[r] * twc[r] - im[r] * tws[r], im[r] * twc[r] + re[r] * tws[r]);
    const f32x4 y0 = mfma(P, E0, z4), y1 = mfma(P, E1, z4);
    v4u h;
    h[0] = pk(blo(x0.x) + y0[0], bhi(x0.x) + y0[1]); h[1] = pk(blo(x0.y) + y0[2], bhi(x0.y) + y0[3]);
    h[2] = pk(blo(x1.x) + y1[0], bhi(x1.x) + y1[1]); h[3] = pk(blo(x1.y) + y1[2], bhi(x1.y) + y1[3]);
    float s = 0.f, q = 0.f;
#pragma unroll
    for (int i = 0; i < 4; i++) { s = dot2(h[i], 0x3F803F80u, s); q = dot2(h[i], h[i], q); }
    wavesum2(s, q);
    const float mean = s * (1.f / D);
    const float var = fmaxf(q * (1.f / D) - mean * mean, 0.f);
    const float r1 = __builtin_amdgcn_rsqf(var + 1e-5f);
    const float sc = r1 * __builtin_amdgcn_rsqf(var * r1 * r1 + 1e-5f);
    const float mo = -mean * sc;
    v2u n0, n1;
    n0.x = pk(blo(h[0]) * sc + mo, bhi(h[0]) * sc + mo); n0.y = pk(blo(h[1]) * sc + mo, bhi(h[1]) * sc + mo);
    n1.x = pk(blo(h[2]) * sc + mo, bhi(h[2]) * sc + mo); n1.y = pk(blo(h[3]) * sc + mo, bhi(h[3]) * sc + mo);
    *(LAS v2u*)(L + L_Q + w * 1088 + 32 * lo + 8 * g) = n0;
    *(LAS v2u*)(L + L_Q + w * 1088 + 512 + 32 * lo + 8 * g) = n1;
    *(LAS v2u*)(L + L_X + w * 1024 + 32 * lo + 8 * g) = v2u{clean(h[0]), clean(h[1])};
    *(LAS v2u*)(L + L_X + w * 1024 + 512 + 32 * lo + 8 * g) = v2u{clean(h[2]), clean(h[3])};
    LBAR();
    LAS unsigned char* tp = L + L_Q + (lo >> 2) * 1088 + 128 * w + 32 * g + 8 * (lo & 3);
    const v2u t0 = trread(tp), t1 = trread(tp + 4 * 1088);
    const v4u hv = *(LAS v4u*)(L + L_X + w * 1024 + lane * 16);
    const int pos = 64 * w + 16 * ((lane >> 1) & 3) + 2 * (lane >> 3) + (lane & 1);
    const v4u hn = {clean(t0.x), clean(t0.y), clean(t1.x), clean(t1.y)};
    __builtin_amdgcn_raw_buffer_store_b128(hn, rs, OFF_HN + c * 8192 + pos * 16, 0, SC1);
    __builtin_amdgcn_raw_buffer_store_b128(hv, rs, OFF_H + (256 * w + c) * 1024 + lane * 16, 0, SC1);
  }
  ST(8);
  SBAR;
  const int f0 = 64 * fg + 16 * (w & 3) + 8 * (lane & 1) + 2 * (w >> 2) + ((lane >> 1) & 1);
  const bool fok = f0 < F;
  const float bia0 = blo(b1[fok ? f0 : 0]), bia1 = blo(b1[fok ? f0 + 4 : 0]);
  LBAR();
  DRAIN();
  ST(9);
  if (g1on) {
    const int ho = OFF_HN + (32 * w + g) * 8192 + (48 * db + lo) * 16;
    v4u A[2][8];
#pragma unroll
    for (int st = 0; st < 2; st++)
#pragma unroll
      for (int j = 0; j < 8; j++) A[st][j] = __builtin_amdgcn_raw_buffer_load_b128(rs, ho + st * 256, j * 32768, SC1);
    SBAR;
#pragma unroll
    for (int st = 0; st < 3; st++) {
      if (st < nst) {
        f32x4 acc[4];
        for (int sp = 0; sp < SPIN_CAP; sp++) {
          u32 mx = 0;
#pragma unroll
          for (int cc = 0; cc < 4; cc++) acc[cc] = f32x4{0.f, 0.f, 0.f, 0.f};
#pragma unroll
          for (int j = 0; j < 8; j++) {
            const v4u a = A[st & 1][j];
#pragma unroll
            for (int cc = 0; cc < 4; cc++) acc[cc] = mfma(W1[j][cc], a, acc[cc]);
            mx = umax3(umax3(mx, a.x, a.y), a.z, a.w);
          }
          if (__all(mx != ~0u)) break;
          MEMF;
#pragma unroll
          for (int j = 0; j < 8; j++) A[st & 1][j] = __builtin_amdgcn_raw_buffer_load_b128(rs, ho + st * 256, j * 32768, SC1);
        }
        if (st == 0 && nst == 3) {
#pragma unroll
          for (int j = 0; j < 8; j++) A[0][j] = __builtin_amdgcn_raw_buffer_load_b128(rs, ho + 512, j * 32768, SC1);
        }
        LAS unsigned char* R = L + st * 33792;
#pragma unroll
        for (int cc = 0; cc < 4; cc++) *(LAS f32x4*)(R + w * 4224 + (cc >> 1) * 1024 + (cc & 1) * 2176 + lane * 16) = acc[cc];
        LBAR();
        LAS unsigned char* rp = R + (w >> 2) * 1024 + ((lane >> 1) & 1) * 2176 + ((w & 3) * 16 + (lane >> 2)) * 16 + (lane & 1) * 8;
        f32x2 v = {bia0, bia1};
#pragma unroll
        for (int ww = 0; ww < 8; ww++) v += *(LAS f32x2*)(rp + ww * 4224);
        float o2[2];
#pragma unroll
        for (int e = 0; e < 2; e++) {
          const float u = v[e];
          const float z = u * (u * u * (-0.044715f * 2.f * 0.7978845608f * 1.4426950409f) + (-2.f * 0.7978845608f * 1.4426950409f));
          o2[e] = u * __builtin_amdgcn_rcpf(1.f + __builtin_amdgcn_exp2f(z));
        }
        const u32 uo = fok ? clean(pk(o2[0], o2[1])) : 0u;
        v4u ov;
        ov.x = uo;
        ov.y = (u32)__builtin_amdgcn_update_dpp(0, (int)uo, 0x55, 0xf, 0xf, true);
        ov.z = (u32)__builtin_amdgcn_update_dpp(0, (int)uo, 0xAA, 0xf, 0xf, true);
        ov.w = (u32)__builtin_amdgcn_update_dpp(0, (int)uo, 0xFF, 0xf, 0xf, true);
        if ((lane & 3) == 0) __builtin_amdgcn_raw_buffer_store_b128(ov, rs, OFF_U + ((3 * db + st) * 44 + 2 * fg) * 1024 + t * 4, 0, SC1);
      }
    }
  }
  ST(10);
  SBAR;
  {
    const int d2 = 2 * (xc >> 1) + (xi >> 4), sb = 16 * (xc & 1) + (xi & 15);
    const int j0 = w < 4 ? 6 * w : 4 + 5 * w;
    const bool six = w < 4;
    v4u W2[6][4];
    {
      const int vo = 16 * g * (S * 2) + 128 * sb + 8 * lo;
#pragma unroll
      for (int j = 0; j < 6; j++) {
        if (j < 5 || six) {
          const int jg = j0 + j;
          const int fb = 64 * (jg >> 1) + 2 * (jg & 1);
          v2u r[8];
#pragma unroll
          for (int e = 0; e < 8; e++) r[e] = __builtin_amdgcn_raw_buffer_load_b64(rw2, vo + (fb + 4 * (e & 3) + (e >> 2)) * (S * 2), 0, 0);
          transpose8x4(r, W2[j]);
        }
      }
    }
    const int sl = t >> 3, q8 = t & 7;
    const int so = 64 * sb + 4 * (sl & 15) + (sl >> 4);
    const float bias2 = blo(b2[so]);
    LBAR();
    DRAIN();
    ST(11);
    const int uo = OFF_U + (4 * d2 * 44 + j0) * 1024 + lane * 16;
    v4u A[4][6];
#pragma unroll
    for (int st = 0; st < 4; st++)
#pragma unroll
      for (int j = 0; j < 6; j++)
        if (j < 5 || six) A[st][j] = __builtin_amdgcn_raw_buffer_load_b128(rs, uo, (st * 44 + j) * 1024, SC1);
    const int hoff = OFF_H + so * 1024 + d2 * 128 + q8 * 16;
    v4u hv = __builtin_amdgcn_raw_buffer_load_b128(rs, hoff, 0, SC1);
    SBAR;
    f32x2 res[4];
#pragma unroll
    for (int st = 0; st < 4; st++) {
      f32x4 acc[4];
      for (int sp = 0; sp < SPIN_CAP; sp++) {
        u32 mx = 0;
#pragma unroll
        for (int cc = 0; cc < 4; cc++) acc[cc] = f32x4{0.f, 0.f, 0.f, 0.f};
#pragma unroll
        for (int j = 0; j < 6; j++) {
          if (j < 5 || six) {
            const v4u a = A[st][j];
#pragma unroll
            for (int cc = 0; cc < 4; cc++) acc[cc] = mfma(a, W2[j][cc], acc[cc]);
            mx = umax3(umax3(mx, a.x, a.y), a.z, a.w);
          }
        }
        if (__all(mx != ~0u)) break;
        MEMF;
#pragma unroll
        for (int j = 0; j < 6; j++)
          if (j < 5 || six) A[st][j] = __builtin_amdgcn_raw_buffer_load_b128(rs, uo, (st * 44 + j) * 1024, SC1);
      }
      LAS unsigned char* R = L + st * 32768;
#pragma unroll
      for (int cc = 0; cc < 4; cc++) *(LAS f32x4*)(R + w * 4096 + cc * 1024 + g * 256 + ((lo ^ (4 * g)) * 16)) = acc[cc];
      LBAR();
      LAS unsigned char* rp = R + (sl >> 4) * 1024 + (q8 >> 1) * 256 + (((sl & 15) ^ (4 * (q8 >> 1))) * 16) + (q8 & 1) * 8;
      f32x2 v = {bias2, bias2};
#pragma unroll
      for (int ww = 0; ww < 8; ww++) v += *(LAS f32x2*)(rp + ww * 4096);
      res[st] = v;
    }
    for (int sp = 0; sp < SPIN_CAP; sp++) {
      if (__all(max(umax3(hv.x, hv.y, hv.z), hv.w) != ~0u)) break;
      MEMF;
      hv = __builtin_amdgcn_raw_buffer_load_b128(rs, hoff, 0, SC1);
    }
    v4u o;
#pragma unroll
    for (int st = 0; st < 4; st++) o[st] = pk(blo(hv[st]) + res[st][0], bhi(hv[st]) + res[st][1]);
    *(v4u*)(out + so * D + d2 * 64 + q8 * 8) = o;
  }
  ST(12);
  if (w == 0) { unsigned long long e0 = __builtin_amdgcn_s_memrealtime(), lim = (c1_ - c0_) * MULT; if (lim > 20000) lim = 20000; while (__builtin_amdgcn_s_memrealtime() - e0 < lim) __builtin_amdgcn_s_sleep(1); }
}

extern "C" void kernel_launch(void* const* d_in, const int* in_sizes, int n_in, void* d_out, int out_size, void* d_ws, size_t ws_size,
                              hipStream_t stream) {
  (void)hipFuncSetAttribute((const void*)fused_kernel, hipFuncAttributeMaxDynamicSharedMemorySize, LDS_BYTES);
  fused_kernel<<<256, 512, LDS_BYTES, stream>>>((const u16*)d_in[0], (const u16*)d_in[1], (const u16*)d_in[2], (const u16*)d_in[4],
                                                (const u16*)d_in[5], (const u16*)d_in[6], (const u16*)d_in[7], (u16*)d_out,
                                                (unsigned char*)d_ws);
}
```

```cpp
#include <hip/hip_runtime.h>

typedef unsigned short u16;

__device__ __attribute__((used, aligned(1024))) static const unsigned fk_tab[4096] asm("fk_tab") = {
    0x3f803f80, 0x3f803f80, 0x3f803f80, 0x3f803f80, 0x3f7b3f80, 0x3f553f6d, 0xbf7bbf80, 0xbf55bf6d,
    0x3f6d3f80, 0x3ec43f35, 0x3f6d3f80, 0x3ec43f35, 0x3f553f80, 0xbe483ec4, 0xbf55bf80, 0x3e48bec4,
    0x3f353f80, 0xbf350000, 0x3f353f80, 0xbf350000, 0x3f0e3f80, 0xbf7bbec4, 0xbf0ebf80, 0x3f7b3ec4,
    0x3ec43f80, 0xbf6dbf35, 0x3ec43f80, 0xbf6dbf35, 0x3e483f80, 0xbf0ebf6d, 0xbe48bf80, 0x3f0e3f6d,
    0x00003f80, 0x0000bf80, 0x00003f80, 0x0000bf80, 0xbe483f80, 0x3f0ebf6d, 0x3e48bf80, 0xbf0e3f6d,
    0xbec43f80, 0x3f6dbf35, 0xbec43f80, 0x3f6dbf35, 0xbf0e3f80, 0x3f7bbec4, 0x3f0ebf80, 0xbf7b3ec4,
    0xbf353f80, 0x3f350000, 0xbf353f80, 0x3f350000, 0xbf553f80, 0x3e483ec4, 0x3f55bf80, 0xbe48bec4,
    0xbf6d3f80, 0xbec43f35, 0xbf6d3f80, 0xbec43f35, 0xbf7b3f80, 0xbf553f6d, 0x3f7bbf80, 0x3f55bf6d,
    0x3f803f80, 0x3f803f80, 0x3f803f80, 0x3f803f80, 0x3f0e3f35, 0x3e483ec4, 0xbf0ebf35, 0xbe48bec4,
    0xbec40000, 0xbf6dbf35, 0xbec40000, 0xbf6dbf35, 0xbf7bbf35, 0xbf0ebf6d, 0x3f7b3f35, 0x3f0e3f6d,
    0xbf35bf80, 0x3f350000, 0xbf35bf80, 0x3f350000, 0x3e48bf35, 0x3f553f6d, 0xbe483f35, 0xbf55bf6d,
    0x3f6d0000, 0xbec43f35, 0x3f6d0000, 0xbec43f35, 0x3f553f35, 0xbf7bbec4, 0xbf55bf35, 0x3f7b3ec4,
    0x00003f80, 0x0000bf80, 0x00003f80, 0x0000bf80, 0xbf553f35, 0x3f7bbec4, 0x3f55bf35, 0xbf7b3ec4,
    0xbf6d0000, 0x3ec43f35, 0xbf6d0000, 0x3ec43f35, 0xbe48bf35, 0xbf553f6d, 0x3e483f35, 0x3f55bf6d,
    0x3f35bf80, 0xbf350000, 0x3f35bf80, 0xbf350000, 0x3f7bbf35, 0x3f0ebf6d, 0xbf7b3f35, 0xbf0e3f6d,
    0x3ec40000, 0x3f6dbf35, 0x3ec40000, 0x3f6dbf35, 0xbf0e3f35, 0xbe483ec4, 0x3f0ebf35, 0x3e48bec4,
    0x3f803f80, 0x3f803f80, 0x3f803f80, 0x3f803f80, 0xbe480000, 0xbf0ebec4, 0x3e480000, 0x3f0e3ec4,
    0xbf6dbf80, 0xbec4bf35, 0xbf6dbf80, 0xbec4bf35, 0x3f0e0000, 0x3f7b3f6d, 0xbf0e0000, 0xbf7bbf6d,
    0x3f353f80, 0xbf350000, 0x3f353f80, 0xbf350000, 0xbf550000, 0xbe48bf6d, 0x3f550000, 0x3e483f6d,
    0xbec4bf80, 0x3f6d3f35, 0xbec4bf80, 0x3f6d3f35, 0x3f7b0000, 0xbf553ec4, 0xbf7b0000, 0x3f55bec4,
    0x00003f80, 0x0000bf80, 0x00003f80, 0x0000bf80, 0xbf7b0000, 0x3f553ec4, 0x3f7b0000, 0xbf55bec4,
    0x3ec4bf80, 0xbf6d3f35, 0x3ec4bf80, 0xbf6d3f35, 0x3f550000, 0x3e48bf6d, 0xbf550000, 0xbe483f6d,
    0xbf353f80, 0x3f350000, 0xbf353f80, 0x3f350000, 0xbf0e0000, 0xbf7b3f6d, 0x3f0e0000, 0x3f7bbf6d,
    0x3f6dbf80, 0x3ec4bf35, 0x3f6dbf80, 0x3ec4bf35, 0x3e480000, 0x3f0ebec4, 0xbe480000, 0xbf0e3ec4,
    0x3f803f80, 0x3f803f80, 0x3f803f80, 0x3f803f80, 0xbf55bf35, 0xbf7bbf6d, 0x3f553f35, 0x3f7b3f6d,
    0x3ec40000, 0x3f6d3f35, 0x3ec40000, 0x3f6d3f35, 0x3e483f35, 0xbf55bec4, 0xbe48bf35, 0x3f553ec4,
    0xbf35bf80, 0x3f350000, 0xbf35bf80, 0x3f350000, 0x3f7b3f35, 0xbf0e3ec4, 0xbf7bbf35, 0x3f0ebec4,
    0xbf6d0000, 0x3ec4bf35, 0xbf6d0000, 0x3ec4bf35, 0x3f0ebf35, 0xbe483f6d, 0xbf0e3f35, 0x3e48bf6d,
    0x00003f80, 0x0000bf80, 0x00003f80, 0x0000bf80, 0xbf0ebf35, 0x3e483f6d, 0x3f0e3f35, 0xbe48bf6d,
    0x3f6d0000, 0xbec4bf35, 0x3f6d0000, 0xbec4bf35, 0xbf7b3f35, 0x3f0e3ec4, 0x3f7bbf35, 0xbf0ebec4,
    0x3f35bf80, 0xbf350000, 0x3f35bf80, 0xbf350000, 0xbe483f35, 0x3f55bec4, 0x3e48bf35, 0xbf553ec4,
    0xbec40000, 0xbf6d3f35, 0xbec40000, 0xbf6d3f35, 0x3f55bf35, 0x3f7bbf6d, 0xbf553f35, 0xbf7b3f6d,
    0xbf803f80, 0xbf803f80, 0xbf803f80, 0xbf803f80, 0xbe480000, 0xbf0ebec4, 0x3e480000, 0x3f0e3ec4,
    0xbec40000, 0xbf6dbf35, 0xbec40000, 0xbf6dbf35, 0xbf0e0000, 0xbf7bbf6d, 0x3f0e0000, 0x3f7b3f6d,
    0xbf350000, 0xbf35bf80, 0xbf350000, 0xbf35bf80, 0xbf550000, 0xbe48bf6d, 0x3f550000, 0x3e483f6d,
    0xbf6d0000, 0x3ec4bf35, 0xbf6d0000, 0x3ec4bf35, 0xbf7b0000, 0x3f55bec4, 0x3f7b0000, 0xbf553ec4,
    0xbf800000, 0x3f800000, 0xbf800000, 0x3f800000, 0xbf7b0000, 0x3f553ec4, 0x3f7b0000, 0xbf55bec4,
    0xbf6d0000, 0x3ec43f35, 0xbf6d0000, 0x3ec43f35, 0xbf550000, 0xbe483f6d, 0x3f550000, 0x3e48bf6d,
    0xbf350000, 0xbf353f80, 0xbf350000, 0xbf353f80, 0xbf0e0000, 0xbf7b3f6d, 0x3f0e0000, 0x3f7bbf6d,
    0xbec40000, 0xbf6d3f35, 0xbec40000, 0xbf6d3f35, 0xbe480000, 0xbf0e3ec4, 0x3e480000, 0x3f0ebec4,
    0xbf803f80, 0xbf803f80, 0xbf803f80, 0xbf803f80, 0xbf55bf35, 0xbf7bbf6d, 0x3f553f35, 0x3f7b3f6d,
    0xbf6dbf80, 0xbec4bf35, 0xbf6dbf80, 0xbec4bf35, 0xbe48bf35, 0x3f553ec4, 0x3e483f35, 0xbf55bec4,
    0x3f350000, 0x3f353f80, 0x3f350000, 0x3f353f80, 0x3f7b3f35, 0xbf0e3ec4, 0xbf7bbf35, 0x3f0ebec4,
    0x3ec43f80, 0xbf6dbf35, 0x3ec43f80, 0xbf6dbf35, 0xbf0e3f35, 0x3e48bf6d, 0x3f0ebf35, 0xbe483f6d,
    0xbf800000, 0x3f800000, 0xbf800000, 0x3f800000, 0xbf0ebf35, 0x3e483f6d, 0x3f0e3f35, 0xbe48bf6d,
    0x3ec4bf80, 0xbf6d3f35, 0x3ec4bf80, 0xbf6d3f35, 0x3f7bbf35, 0xbf0ebec4, 0xbf7b3f35, 0x3f0e3ec4,
    0x3f350000, 0x3f35bf80, 0x3f350000, 0x3f35bf80, 0xbe483f35, 0x3f55bec4, 0x3e48bf35, 0xbf553ec4,
    0xbf6d3f80, 0xbec43f35, 0xbf6d3f80, 0xbec43f35, 0xbf553f35, 0xbf7b3f6d, 0x3f55bf35, 0x3f7bbf6d,
    0xbf803f80, 0xbf803f80, 0xbf803f80, 0xbf803f80, 0xbf7bbf80, 0xbf55bf6d, 0x3f7b3f80, 0x3f553f6d,
    0x3ec40000, 0x3f6d3f35, 0x3ec40000, 0x3f6d3f35, 0x3f553f80, 0xbe483ec4, 0xbf55bf80, 0x3e48bec4,
    0xbf350000, 0xbf35bf80, 0xbf350000, 0xbf35bf80, 0xbf0ebf80, 0x3f7b3ec4, 0x3f0e3f80, 0xbf7bbec4,
    0x3f6d0000, 0xbec43f35, 0x3f6d0000, 0xbec43f35, 0x3e483f80, 0xbf0ebf6d, 0xbe48bf80, 0x3f0e3f6d,
    0xbf800000, 0x3f800000, 0xbf800000, 0x3f800000, 0x3e48bf80, 0xbf0e3f6d, 0xbe483f80, 0x3f0ebf6d,
    0x3f6d0000, 0xbec4bf35, 0x3f6d0000, 0xbec4bf35, 0xbf0e3f80, 0x3f7bbec4, 0x3f0ebf80, 0xbf7b3ec4,
    0xbf350000, 0xbf353f80, 0xbf350000, 0xbf353f80, 0x3f55bf80, 0xbe48bec4, 0xbf553f80, 0x3e483ec4,
    0x3ec40000, 0x3f6dbf35, 0x3ec40000, 0x3f6dbf35, 0xbf7b3f80, 0xbf553f6d, 0x3f7bbf80, 0x3f55bf6d,
    0xbf803f80, 0xbf803f80, 0xbf803f80, 0xbf803f80, 0xbf0ebf35, 0xbe48bec4, 0x3f0e3f35, 0x3e483ec4,
    0x3f6d3f80, 0x3ec43f35, 0x3f6d3f80, 0x3ec43f35, 0xbf7bbf35, 0xbf0ebf6d, 0x3f7b3f35, 0x3f0e3f6d,
    0x3f350000, 0x3f353f80, 0x3f350000, 0x3f353f80, 0xbe483f35, 0xbf55bf6d, 0x3e48bf35, 0x3f553f6d,
    0xbec4bf80, 0x3f6d3f35, 0xbec4bf80, 0x3f6d3f35, 0x3f553f35, 0xbf7bbec4, 0xbf55bf35, 0x3f7b3ec4,
    0xbf800000, 0x3f800000, 0xbf800000, 0x3f800000, 0x3f55bf35, 0xbf7b3ec4, 0xbf553f35, 0x3f7bbec4,
    0xbec43f80, 0x3f6dbf35, 0xbec43f80, 0x3f6dbf35, 0xbe48bf35, 0xbf553f6d, 0x3e483f35, 0x3f55bf6d,
    0x3f350000, 0x3f35bf80, 0x3f350000, 0x3f35bf80, 0xbf7b3f35, 0xbf0e3f6d, 0x3f7bbf35, 0x3f0ebf6d,
    0x3f6dbf80, 0x3ec4bf35, 0x3f6dbf80, 0x3ec4bf35, 0xbf0e3f35, 0xbe483ec4, 0x3f0ebf35, 0x3e48bec4,
    0x3f800000, 0x3f800000, 0x3f800000, 0x3f800000, 0x3f800000, 0x3f7ffb11, 0x3f7fec43, 0x3f7fd397,
    0x3f800000, 0x3f7fec43, 0x3f7fb10f, 0x3f7f4e6d, 0x3f800000, 0x3f7fd397, 0x3f7f4e6d, 0x3f7e70b0,
    0x3f800000, 0x3f7fb10f, 0x3f7ec46d, 0x3f7d3aac, 0x3f800000, 0x3f7f84ab, 0x3f7e1324, 0x3f7baccd,
    0x3f800000, 0x3f7f4e6d, 0x3f7d3aac, 0x3f79c79d, 0x3f800000, 0x3f7f0e58, 0x3f7c3b28, 0x3f778bc5,
    0x3f800000, 0x3f7ec46d, 0x3f7b14be, 0x3f74fa0b, 0x3f800000, 0x3f7e70b0, 0x3f79c79d, 0x3f721352,
    0x3f800000, 0x3f7e1324, 0x3f7853f8, 0x3f6ed89e, 0x3f800000, 0x3f7dabcc, 0x3f76ba07, 0x3f6b4b0c,
    0x3f800000, 0x3f7d3aac, 0x3f74fa0b, 0x3f676bd8, 0x3f800000, 0x3f7cbfc9, 0x3f731447, 0x3f633c5a,
    0x3f800000, 0x3f7c3b28, 0x3f710908, 0x3f5ebe05, 0x3f800000, 0x3f7baccd, 0x3f6ed89e, 0x3f59f26a,
    0x3f800000, 0x3f800000, 0x3f800000, 0x3f800000, 0x3f7fb10f, 0x3f7f84ab, 0x3f7f4e6d, 0x3f7f0e58,
    0x3f7ec46d, 0x3f7e1324, 0x3f7d3aac, 0x3f7c3b28, 0x3f7d3aac, 0x3f7baccd, 0x3f79c79d, 0x3f778bc5,
    0x3f7b14be, 0x3f7853f8, 0x3f74fa0b, 0x3f710908, 0x3f7853f8, 0x3f740bdd, 0x3f6ed89e, 0x3f68bf3c,
    0x3f74fa0b, 0x3f6ed89e, 0x3f676bd8, 0x3f5ebe05, 0x3f710908, 0x3f68bf3c, 0x3f5ebe05, 0x3f531849,
    0x3f6c835e, 0x3f61c598, 0x3f54db31, 0x3f45e403, 0x3f676bd8, 0x3f59f26a, 0x3f49d112, 0x3f373a23,
    0x3f61c598, 0x3f514d3d, 0x3f3daef9, 0x3f273656, 0x3f5b941a, 0x3f47de65, 0x3f3085bb, 0x3f15f6d9,
    0x3f54db31, 0x3f3daef9, 0x3f226799, 0x3f039c3d, 0x3f4d9f02, 0x3f32c8c9, 0x3f13682a, 0x3ee0924f,
    0x3f45e403, 0x3f273656, 0x3f039c3d, 0x3eb8442a, 0x3f3daef9, 0x3f1b02c6, 0x3ee63375, 0x3e8e9a22,
    0x3f800000, 0x3f800000, 0x3f800000, 0x3f800000, 0x3f7ec46d, 0x3f7e70b0, 0x3f7e1324, 0x3f7dabcc,
    0x3f7b14be, 0x3f79c79d, 0x3f7853f8, 0x3f76ba07, 0x3f74fa0b, 0x3f721352, 0x3f6ed89e, 0x3f6b4b0c,
    0x3f6c835e, 0x3f676bd8, 0x3f61c598, 0x3f5b941a, 0x3f61c598, 0x3f59f26a, 0x3f514d3d, 0x3f47de65,
    0x3f54db31, 0x3f49d112, 0x3f3daef9, 0x3f3085bb, 0x3f45e403, 0x3f373a23, 0x3f273656, 0x3f15f6d9,
    0x3f3504f3, 0x3f226799, 0x3f0e39da, 0x3ef15aea, 0x3f226799, 0x3f0b9a6b, 0x3ee63375, 0x3eb263ef,
    0x3f0e39da, 0x3ee63375, 0x3eac7cd4, 0x3e605c13, 0x3ef15aea, 0x3eb263ef, 0x3e605c13, 0x3dafb680,
    0x3ec3ef15, 0x3e78cfcc, 0x3dc8bd36, 0xbd48fb30, 0x3e94a031, 0x3e09cf86, 0xbcc90ab0, 0xbe3b6ecf,
    0x3e47c5c2, 0x3cc90ab0, 0xbe164083, 0xbea09ae5, 0x3dc8bd36, 0xbdafb680, 0xbe888e93, 0xbee0924f,
    0x3f800000, 0x3f800000, 0x3f800000, 0x3f800000, 0x3f7d3aac, 0x3f7cbfc9, 0x3f7c3b28, 0x3f7baccd,
    0x3f74fa0b, 0x3f731447, 0x3f710908, 0x3f6ed89e, 0x3f676bd8, 0x3f633c5a, 0x3f5ebe05, 0x3f59f26a,
    0x3f54db31, 0x3f4d9f02, 0x3f45e403, 0x3f3daef9, 0x3f3daef9, 0x3f32c8c9, 0x3f273656, 0x3f1b02c6,
    0x3f226799, 0x3f13682a, 0x3f039c3d, 0x3ee63375, 0x3f039c3d, 0x3ee0924f, 0x3eb8442a, 0x3e8e9a22,
    0x3ec3ef15, 0x3e94a031, 0x3e47c5c2, 0x3dc8bd36, 0x3e78cfcc, 0x3e09cf86, 0x3cc90ab0, 0xbdafb680,
    0x3dc8bd36, 0xbcc90ab0, 0xbe164083, 0xbe888e93, 0xbd48fb30, 0xbe3b6ecf, 0xbea09ae5, 0xbee0924f,
    0xbe47c5c2, 0xbeac7cd4, 0xbef15aea, 0xbf187fc0, 0xbeac7cd4, 0xbef6e0cb, 0xbf1d7fd1, 0xbf3b8f3b,
    0xbef15aea, 0xbf1d7fd1, 0xbf3daef9, 0xbf584853, 0xbf187fc0, 0xbf3b8f3b, 0xbf584853, 0xbf6db293,
    0x00000000, 0x00000000, 0x00000000, 0x00000000, 0x00000000, 0x3c490e90, 0x3cc90ab0, 0x3d16c32c,
    0x00000000, 0x3cc90ab0, 0x3d48fb30, 0x3d96a905, 0x00000000, 0x3d16c32c, 0x3d96a905, 0x3de1bc2e,
    0x00000000, 0x3d48fb30, 0x3dc8bd36, 0x3e164083, 0x00000000, 0x3d7b2b74, 0x3dfab273, 0x3e3b6ecf,
    0x00000000, 0x3d96a905, 0x3e164083, 0x3e605c13, 0x00000000, 0x3dafb680, 0x3e2f10a2, 0x3e827dc0,
    0x00000000, 0x3dc8bd36, 0x3e47c5c2, 0x3e94a031, 0x00000000, 0x3de1bc2e, 0x3e605c13, 0x3ea68f12,
    0x00000000, 0x3dfab273, 0x3e78cfcc, 0x3eb8442a, 0x00000000, 0x3e09cf86, 0x3e888e93, 0x3ec9b953,
    0x00000000, 0x3e164083, 0x3e94a031, 0x3edae880, 0x00000000, 0x3e22abb6, 0x3ea09ae5, 0x3eebcbbb,
    0x00000000, 0x3e2f10a2, 0x3eac7cd4, 0x3efc5d27, 0x00000000, 0x3e3b6ecf, 0x3eb8442a, 0x3f064b82,
    0x00000000, 0x00000000, 0x00000000, 0x00000000, 0x3d48fb30, 0x3d7b2b74, 0x3d96a905, 0x3dafb680,
    0x3dc8bd36, 0x3dfab273, 0x3e164083, 0x3e2f10a2, 0x3e164083, 0x3e3b6ecf, 0x3e605c13, 0x3e827dc0,
    0x3e47c5c2, 0x3e78cfcc, 0x3e94a031, 0x3eac7cd4, 0x3e78cfcc, 0x3e9aa086, 0x3eb8442a, 0x3ed53641,
    0x3e94a031, 0x3eb8442a, 0x3edae880, 0x3efc5d27, 0x3eac7cd4, 0x3ed53641, 0x3efc5d27, 0x3f10d3cd,
    0x3ec3ef15, 0x3ef15aea, 0x3f0e39da, 0x3f226799, 0x3edae880, 0x3f064b82, 0x3f1d7fd1, 0x3f32c8c9,
    0x3ef15aea, 0x3f13682a, 0x3f2beb4a, 0x3f41d870, 0x3f039c3d, 0x3f1ff6cb, 0x3f396842, 0x3f4f7a1f,
    0x3f0e39da, 0x3f2beb4a, 0x3f45e403, 0x3f5b941a, 0x3f187fc0, 0x3f373a23, 0x3f514d3d, 0x3f660f88,
    0x3f226799, 0x3f41d870, 0x3f5b941a, 0x3f6ed89e, 0x3f2beb4a, 0x3f4bbbf8, 0x3f64aa59, 0x3f75dec6,
    0x00000000, 0x00000000, 0x00000000, 0x00000000, 0x3dc8bd36, 0x3de1bc2e, 0x3dfab273, 0x3e09cf86,
    0x3e47c5c2, 0x3e605c13, 0x3e78cfcc, 0x3e888e93, 0x3e94a031, 0x3ea68f12, 0x3eb8442a, 0x3ec9b953,
    0x3ec3ef15, 0x3edae880, 0x3ef15aea, 0x3f039c3d, 0x3ef15aea, 0x3f064b82, 0x3f13682a, 0x3f1ff6cb,
    0x3f0e39da, 0x3f1d7fd1, 0x3f2beb4a, 0x3f396842, 0x3f226799, 0x3f32c8c9, 0x3f41d870, 0x3f4f7a1f,
    0x3f3504f3, 0x3f45e403, 0x3f54db31, 0x3f61c598, 0x3f45e403, 0x3f5695e5, 0x3f64aa59, 0x3f6ff573,
    0x3f54db31, 0x3f64aa59, 0x3f710908, 0x3f79c79d, 0x3f61c598, 0x3f6ff573, 0x3f79c79d, 0x3f7f0e58,
    0x3f6c835e, 0x3f7853f8, 0x3f7ec46d, 0x3f7fb10f, 0x3f74fa0b, 0x3f7dabcc, 0x3f7fec43, 0x3f7baccd,
    0x3f7b14be, 0x3f7fec43, 0x3f7d3aac, 0x3f731447, 0x3f7ec46d, 0x3f7f0e58, 0x3f76ba07, 0x3f660f88,
    0x00000000, 0x00000000, 0x00000000, 0x00000000, 0x3e164083, 0x3e22abb6, 0x3e2f10a2, 0x3e3b6ecf,
    0x3e94a031, 0x3ea09ae5, 0x3eac7cd4, 0x3eb8442a, 0x3edae880, 0x3eebcbbb, 0x3efc5d27, 0x3f064b82,
    0x3f0e39da, 0x3f187fc0, 0x3f226799, 0x3f2beb4a, 0x3f2beb4a, 0x3f373a23, 0x3f41d870, 0x3f4bbbf8,
    0x3f45e403, 0x3f514d3d, 0x3f5b941a, 0x3f64aa59, 0x3f5b941a, 0x3f660f88, 0x3f6ed89e, 0x3f75dec6,
    0x3f6c835e, 0x3f74fa0b, 0x3f7b14be, 0x3f7ec46d, 0x3f7853f8, 0x3f7dabcc, 0x3f7fec43, 0x3f7f0e58,
    0x3f7ec46d, 0x3f7fec43, 0x3f7d3aac, 0x3f76ba07, 0x3f7fb10f, 0x3f7baccd, 0x3f731447, 0x3f660f88,
    0x3f7b14be, 0x3f710908, 0x3f61c598, 0x3f4d9f02, 0x3f710908, 0x3f604621, 0x3f49d112, 0x3f2e3bde,
    0x3f61c598, 0x3f49d112, 0x3f2beb4a, 0x3f08f59b, 0x3f4d9f02, 0x3f2e3bde, 0x3f08f59b, 0x3ebe1d4a,
    0x00003f80, 0x00003f80, 0x00003f80, 0x00003f80, 0x00003f80, 0x3ec43f6d, 0x3f353f35, 0x3f6d3ec4,
    0x00003f80, 0x3f353f35, 0x3f800000, 0x3f35bf35, 0x00003f80, 0x3f6d3ec4, 0x3f35bf35, 0xbec4bf6d,
    0x00003f80, 0x3f800000, 0x0000bf80, 0xbf800000, 0x00003f80, 0x3f6dbec4, 0xbf35bf35, 0xbec43f6d,
    0x00003f80, 0x3f35bf35, 0xbf800000, 0x3f353f35, 0x00003f80, 0x3ec4bf6d, 0xbf353f35, 0x3f6dbec4,
    0x00003f80, 0x0000bf80, 0x00003f80, 0x0000bf80, 0x00003f80, 0xbec4bf6d, 0x3f353f35, 0xbf6dbec4,
    0x00003f80, 0xbf35bf35, 0x3f800000, 0xbf353f35, 0x00003f80, 0xbf6dbec4, 0x3f35bf35, 0x3ec43f6d,
    0x00003f80, 0xbf800000, 0x0000bf80, 0x3f800000, 0x00003f80, 0xbf6d3ec4, 0xbf35bf35, 0x3ec4bf6d,
    0x00003f80, 0xbf353f35, 0xbf800000, 0xbf35bf35, 0x00003f80, 0xbec43f6d, 0xbf353f35, 0xbf6d3ec4,
    0x00003f80, 0x00003f80, 0x00003f80, 0x00003f80, 0x3f800000, 0x3f6dbec4, 0x3f35bf35, 0x3ec4bf6d,
    0x0000bf80, 0xbf35bf35, 0xbf800000, 0xbf353f35, 0xbf800000, 0xbec43f6d, 0x3f353f35, 0x3f6dbec4,
    0x00003f80, 0x3f800000, 0x0000bf80, 0xbf800000, 0x3f800000, 0xbec4bf6d, 0xbf353f35, 0x3f6d3ec4,
    0x0000bf80, 0xbf353f35, 0x3f800000, 0xbf35bf35, 0xbf800000, 0x3f6d3ec4, 0xbf35bf35, 0x3ec43f6d,
    0x00003f80, 0x0000bf80, 0x00003f80, 0x0000bf80, 0x3f800000, 0xbf6d3ec4, 0x3f35bf35, 0xbec43f6d,
    0x0000bf80, 0x3f353f35, 0xbf800000, 0x3f35bf35, 0xbf800000, 0x3ec4bf6d, 0x3f353f35, 0xbf6d3ec4,
    0x00003f80, 0xbf800000, 0x0000bf80, 0x3f800000, 0x3f800000, 0x3ec43f6d, 0xbf353f35, 0xbf6dbec4,
    0x0000bf80, 0x3f35bf35, 0x3f800000, 0x3f353f35, 0xbf800000, 0xbf6dbec4, 0xbf35bf35, 0xbec4bf6d,
    0x00003f80, 0x00003f80, 0x00003f80, 0x00003f80, 0x0000bf80, 0xbec4bf6d, 0xbf35bf35, 0xbf6dbec4,
    0x00003f80, 0x3f353f35, 0x3f800000, 0x3f35bf35, 0x0000bf80, 0xbf6dbec4, 0xbf353f35, 0x3ec43f6d,
    0x00003f80, 0x3f800000, 0x0000bf80, 0xbf800000, 0x0000bf80, 0xbf6d3ec4, 0x3f353f35, 0x3ec4bf6d,
    0x00003f80, 0x3f35bf35, 0xbf800000, 0x3f353f35, 0x0000bf80, 0xbec43f6d, 0x3f35bf35, 0xbf6d3ec4,
    0x00003f80, 0x0000bf80, 0x00003f80, 0x0000bf80, 0x0000bf80, 0x3ec43f6d, 0xbf35bf35, 0x3f6d3ec4,
    0x00003f80, 0xbf35bf35, 0x3f800000, 0xbf353f35, 0x0000bf80, 0x3f6d3ec4, 0xbf353f35, 0xbec4bf6d,
    0x00003f80, 0xbf800000, 0x0000bf80, 0x3f800000, 0x0000bf80, 0x3f6dbec4, 0x3f353f35, 0xbec43f6d,
    0x00003f80, 0xbf353f35, 0xbf800000, 0xbf35bf35, 0x0000bf80, 0x3ec4bf6d, 0x3f35bf35, 0x3f6dbec4,
    0x00003f80, 0x00003f80, 0x00003f80, 0x00003f80, 0xbf800000, 0xbf6d3ec4, 0xbf353f35, 0xbec43f6d,
    0x0000bf80, 0xbf35bf35, 0xbf800000, 0xbf353f35, 0x3f800000, 0x3ec4bf6d, 0xbf35bf35, 0xbf6d3ec4,
    0x00003f80, 0x3f800000, 0x0000bf80, 0xbf800000, 0xbf800000, 0x3ec43f6d, 0x3f35bf35, 0xbf6dbec4,
    0x0000bf80, 0xbf353f35, 0x3f800000, 0xbf35bf35, 0x3f800000, 0xbf6dbec4, 0x3f353f35, 0xbec4bf6d,
    0x00003f80, 0x0000bf80, 0x00003f80, 0x0000bf80, 0xbf800000, 0x3f6dbec4, 0xbf353f35, 0x3ec4bf6d,
    0x0000bf80, 0x3f353f35, 0xbf800000, 0x3f35bf35, 0x3f800000, 0xbec43f6d, 0xbf35bf35, 0x3f6dbec4,
    0x00003f80, 0xbf800000, 0x0000bf80, 0x3f800000, 0xbf800000, 0xbec4bf6d, 0x3f35bf35, 0x3f6d3ec4,
    0x0000bf80, 0x3f35bf35, 0x3f800000, 0x3f353f35, 0x3f800000, 0x3f6d3ec4, 0x3f353f35, 0x3ec43f6d,
    0x3f800000, 0x3f800000, 0x3f800000, 0x3f800000, 0x3f800000, 0x3f6dbec4, 0x3f35bf35, 0x3ec4bf6d,
    0x3f800000, 0x3f35bf35, 0x0000bf80, 0xbf35bf35, 0x3f800000, 0x3ec4bf6d, 0xbf35bf35, 0xbf6d3ec4,
    0x3f800000, 0x0000bf80, 0xbf800000, 0x00003f80, 0x3f800000, 0xbec4bf6d, 0xbf353f35, 0x3f6d3ec4,
    0x3f800000, 0xbf35bf35, 0x00003f80, 0x3f35bf35, 0x3f800000, 0xbf6dbec4, 0x3f353f35, 0xbec4bf6d,
    0xbf800000, 0x3f800000, 0xbf800000, 0x3f800000, 0xbf800000, 0x3f6dbec4, 0xbf353f35, 0x3ec4bf6d,
    0xbf800000, 0x3f35bf35, 0x00003f80, 0xbf35bf35, 0xbf800000, 0x3ec4bf6d, 0x3f353f35, 0xbf6d3ec4,
    0xbf800000, 0x0000bf80, 0x3f800000, 0x00003f80, 0xbf800000, 0xbec4bf6d, 0x3f35bf35, 0x3f6d3ec4,
    0xbf800000, 0xbf35bf35, 0x0000bf80, 0x3f35bf35, 0xbf800000, 0xbf6dbec4, 0xbf35bf35, 0xbec4bf6d,
    0x3f800000, 0x3f800000, 0x3f800000, 0x3f800000, 0x0000bf80, 0xbec4bf6d, 0xbf35bf35, 0xbf6dbec4,
    0xbf800000, 0xbf353f35, 0x00003f80, 0x3f353f35, 0x00003f80, 0x3f6d3ec4, 0x3f35bf35, 0xbec4bf6d,
    0x3f800000, 0x0000bf80, 0xbf800000, 0x00003f80, 0x0000bf80, 0xbf6d3ec4, 0x3f353f35, 0x3ec4bf6d,
    0xbf800000, 0x3f353f35, 0x0000bf80, 0xbf353f35, 0x00003f80, 0x3ec4bf6d, 0xbf353f35, 0x3f6dbec4,
    0xbf800000, 0x3f800000, 0xbf800000, 0x3f800000, 0x00003f80, 0xbec4bf6d, 0x3f353f35, 0xbf6dbec4,
    0x3f800000, 0xbf353f35, 0x0000bf80, 0x3f353f35, 0x0000bf80, 0x3f6d3ec4, 0xbf353f35, 0xbec4bf6d,
    0xbf800000, 0x0000bf80, 0x3f800000, 0x00003f80, 0x00003f80, 0xbf6d3ec4, 0xbf35bf35, 0x3ec4bf6d,
    0x3f800000, 0x3f353f35, 0x00003f80, 0xbf353f35, 0x0000bf80, 0x3ec4bf6d, 0x3f35bf35, 0x3f6dbec4,
    0x3f800000, 0x3f800000, 0x3f800000, 0x3f800000, 0xbf800000, 0xbf6d3ec4, 0xbf353f35, 0xbec43f6d,
    0x3f800000, 0x3f35bf35, 0x0000bf80, 0xbf35bf35, 0xbf800000, 0xbec43f6d, 0x3f353f35, 0x3f6dbec4,
    0x3f800000, 0x0000bf80, 0xbf800000, 0x00003f80, 0xbf800000, 0x3ec43f6d, 0x3f35bf35, 0xbf6dbec4,
    0x3f800000, 0xbf35bf35, 0x00003f80, 0x3f35bf35, 0xbf800000, 0x3f6d3ec4, 0xbf35bf35, 0x3ec43f6d,
    0xbf800000, 0x3f800000, 0xbf800000, 0x3f800000, 0x3f800000, 0xbf6d3ec4, 0x3f35bf35, 0xbec43f6d,
    0xbf800000, 0x3f35bf35, 0x00003f80, 0xbf35bf35, 0x3f800000, 0xbec43f6d, 0xbf35bf35, 0x3f6dbec4,
    0xbf800000, 0x0000bf80, 0x3f800000, 0x00003f80, 0x3f800000, 0x3ec43f6d, 0xbf353f35, 0xbf6dbec4,
    0xbf800000, 0xbf35bf35, 0x0000bf80, 0x3f35bf35, 0x3f800000, 0x3f6d3ec4, 0x3f353f35, 0x3ec43f6d,
    0x3f800000, 0x3f800000, 0x3f800000, 0x3f800000, 0x00003f80, 0x3ec43f6d, 0x3f353f35, 0x3f6d3ec4,
    0xbf800000, 0xbf353f35, 0x00003f80, 0x3f353f35, 0x0000bf80, 0xbf6dbec4, 0xbf353f35, 0x3ec43f6d,
    0x3f800000, 0x0000bf80, 0xbf800000, 0x00003f80, 0x00003f80, 0x3f6dbec4, 0xbf35bf35, 0xbec43f6d,
    0xbf800000, 0x3f353f35, 0x0000bf80, 0xbf353f35, 0x0000bf80, 0xbec43f6d, 0x3f35bf35, 0xbf6d3ec4,
    0xbf800000, 0x3f800000, 0xbf800000, 0x3f800000, 0x0000bf80, 0x3ec43f6d, 0xbf35bf35, 0x3f6d3ec4,
    0x3f800000, 0xbf353f35, 0x0000bf80, 0x3f353f35, 0x00003f80, 0xbf6dbec4, 0x3f35bf35, 0x3ec43f6d,
    0xbf800000, 0x0000bf80, 0x3f800000, 0x00003f80, 0x0000bf80, 0x3f6dbec4, 0x3f353f35, 0xbec43f6d,
    0x3f800000, 0x3f353f35, 0x00003f80, 0xbf353f35, 0x00003f80, 0xbec43f6d, 0xbf353f35, 0xbf6d3ec4,
    0x3f800000, 0x3f7b0000, 0x3f6d0000, 0x3f550000, 0x00003f80, 0x00003f6d, 0x00003f35, 0x00003ec4,
    0x3f800000, 0x3f550000, 0x3ec40000, 0xbe480000, 0x00003f80, 0x00003f35, 0x00000000, 0x0000bf35,
    0x3f800000, 0x3f0e0000, 0xbec40000, 0xbf7b0000, 0x00003f80, 0x00003ec4, 0x0000bf35, 0x0000bf6d,
    0x3f800000, 0x3e480000, 0xbf6d0000, 0xbf0e0000, 0x00003f80, 0x00000000, 0x0000bf80, 0x00000000,
    0x3f800000, 0xbe480000, 0xbf6d0000, 0x3f0e0000, 0x00003f80, 0x0000bec4, 0x0000bf35, 0x00003f6d,
    0x3f800000, 0xbf0e0000, 0xbec40000, 0x3f7b0000, 0x00003f80, 0x0000bf35, 0x00000000, 0x00003f35,
    0x3f800000, 0xbf550000, 0x3ec40000, 0x3e480000, 0x00003f80, 0x0000bf6d, 0x00003f35, 0x0000bec4,
    0x3f800000, 0xbf7b0000, 0x3f6d0000, 0xbf550000, 0x00003f80, 0x0000bf80, 0x00003f80, 0x0000bf80,
    0x3f350000, 0x3f0e0000, 0x3ec40000, 0x3e480000, 0x00000000, 0x0000bec4, 0x0000bf35, 0x0000bf6d,
    0xbf350000, 0xbf7b0000, 0xbf6d0000, 0xbf0e0000, 0x0000bf80, 0x0000bf35, 0x00000000, 0x00003f35,
    0xbf350000, 0x3e480000, 0x3f6d0000, 0x3f550000, 0x00000000, 0x00003f6d, 0x00003f35, 0x0000bec4,
    0x3f350000, 0x3f550000, 0xbec40000, 0xbf7b0000, 0x00003f80, 0x00000000, 0x0000bf80, 0x00000000,
    0x3f350000, 0xbf550000, 0xbec40000, 0x3f7b0000, 0x00000000, 0x0000bf6d, 0x00003f35, 0x00003ec4,
    0xbf350000, 0xbe480000, 0x3f6d0000, 0xbf550000, 0x0000bf80, 0x00003f35, 0x00000000, 0x0000bf35,
    0xbf350000, 0x3f7b0000, 0xbf6d0000, 0x3f0e0000, 0x00000000, 0x00003ec4, 0x0000bf35, 0x00003f6d,
    0x3f350000, 0xbf0e0000, 0x3ec40000, 0xbe480000, 0x00003f80, 0x0000bf80, 0x00003f80, 0x0000bf80,
    0x00000000, 0xbe480000, 0xbec40000, 0xbf0e0000, 0x0000bf80, 0x0000bf6d, 0x0000bf35, 0x0000bec4,
    0x00000000, 0x3f0e0000, 0x3f6d0000, 0x3f7b0000, 0x00003f80, 0x00003f35, 0x00000000, 0x0000bf35,
    0x00000000, 0xbf550000, 0xbf6d0000, 0xbe480000, 0x0000bf80, 0x0000bec4, 0x00003f35, 0x00003f6d,
    0x00000000, 0x3f7b0000, 0x3ec40000, 0xbf550000, 0x00003f80, 0x00000000, 0x0000bf80, 0x00000000,
    0x00000000, 0xbf7b0000, 0x3ec40000, 0x3f550000, 0x0000bf80, 0x00003ec4, 0x00003f35, 0x0000bf6d,
    0x00000000, 0x3f550000, 0xbf6d0000, 0x3e480000, 0x00003f80, 0x0000bf35, 0x00000000, 0x00003f35,
    0x00000000, 0xbf0e0000, 0x3f6d0000, 0xbf7b0000, 0x0000bf80, 0x00003f6d, 0x0000bf35, 0x00003ec4,
    0x00000000, 0x3e480000, 0xbec40000, 0x3f0e0000, 0x00003f80, 0x0000bf80, 0x00003f80, 0x0000bf80,
    0xbf350000, 0xbf550000, 0xbf6d0000, 0xbf7b0000, 0x00000000, 0x00003ec4, 0x00003f35, 0x00003f6d,
    0x3f350000, 0x3e480000, 0xbec40000, 0xbf550000, 0x0000bf80, 0x0000bf35, 0x00000000, 0x00003f35,
    0x3f350000, 0x3f7b0000, 0x3ec40000, 0xbf0e0000, 0x00000000, 0x0000bf6d, 0x0000bf35, 0x00003ec4,
    0xbf350000, 0x3f0e0000, 0x3f6d0000, 0xbe480000, 0x00003f80, 0x00000000, 0x0000bf80, 0x00000000,
    0xbf350000, 0xbf0e0000, 0x3f6d0000, 0x3e480000, 0x00000000, 0x00003f6d, 0x0000bf35, 0x0000bec4,
    0x3f350000, 0xbf7b0000, 0x3ec40000, 0x3f0e0000, 0x0000bf80, 0x00003f35, 0x00000000, 0x0000bf35,
    0x3f350000, 0xbe480000, 0xbec40000, 0x3f550000, 0x00000000, 0x0000bec4, 0x00003f35, 0x0000bf6d,
    0xbf350000, 0x3f550000, 0xbf6d0000, 0x3f7b0000, 0x00003f80, 0x0000bf80, 0x00003f80, 0x0000bf80,
    0x00000000, 0xbe480000, 0xbec40000, 0xbf0e0000, 0x00000000, 0x0000bec4, 0x0000bf35, 0x0000bf6d,
    0x00000000, 0xbf0e0000, 0xbf6d0000, 0xbf7b0000, 0x00000000, 0x0000bf35, 0x0000bf80, 0x0000bf35,
    0x00000000, 0xbf550000, 0xbf6d0000, 0xbe480000, 0x00000000, 0x0000bf6d, 0x0000bf35, 0x00003ec4,
    0x00000000, 0xbf7b0000, 0xbec40000, 0x3f550000, 0x00000000, 0x0000bf80, 0x00000000, 0x00003f80,
    0x00000000, 0xbf7b0000, 0x3ec40000, 0x3f550000, 0x00000000, 0x0000bf6d, 0x00003f35, 0x00003ec4,
    0x00000000, 0xbf550000, 0x3f6d0000, 0xbe480000, 0x00000000, 0x0000bf35, 0x00003f80, 0x0000bf35,
    0x00000000, 0xbf0e0000, 0x3f6d0000, 0xbf7b0000, 0x00000000, 0x0000bec4, 0x00003f35, 0x0000bf6d,
    0x00000000, 0xbe480000, 0x3ec40000, 0xbf0e0000, 0x00000000, 0x00000000, 0x00000000, 0x00000000,
    0xbf350000, 0xbf550000, 0xbf6d0000, 0xbf7b0000, 0x0000bf80, 0x0000bf6d, 0x0000bf35, 0x0000bec4,
    0xbf350000, 0xbe480000, 0x3ec40000, 0x3f550000, 0x00000000, 0x00003f35, 0x00003f80, 0x00003f35,
    0x3f350000, 0x3f7b0000, 0x3ec40000, 0xbf0e0000, 0x00003f80, 0x00003ec4, 0x0000bf35, 0x0000bf6d,
    0x3f350000, 0xbf0e0000, 0xbf6d0000, 0x3e480000, 0x00000000, 0x0000bf80, 0x00000000, 0x00003f80,
    0xbf350000, 0xbf0e0000, 0x3f6d0000, 0x3e480000, 0x0000bf80, 0x00003ec4, 0x00003f35, 0x0000bf6d,
    0xbf350000, 0x3f7b0000, 0xbec40000, 0xbf0e0000, 0x00000000, 0x00003f35, 0x0000bf80, 0x00003f35,
    0x3f350000, 0xbe480000, 0xbec40000, 0x3f550000, 0x00003f80, 0x0000bf6d, 0x00003f35, 0x0000bec4,
    0x3f350000, 0xbf550000, 0x3f6d0000, 0xbf7b0000, 0x00000000, 0x00000000, 0x00000000, 0x00000000,
    0xbf800000, 0xbf7b0000, 0xbf6d0000, 0xbf550000, 0x00000000, 0x00003ec4, 0x00003f35, 0x00003f6d,
    0x3f800000, 0x3f550000, 0x3ec40000, 0xbe480000, 0x00000000, 0x0000bf35, 0x0000bf80, 0x0000bf35,
    0xbf800000, 0xbf0e0000, 0x3ec40000, 0x3f7b0000, 0x00000000, 0x00003f6d, 0x00003f35, 0x0000bec4,
    0x3f800000, 0x3e480000, 0xbf6d0000, 0xbf0e0000, 0x00000000, 0x0000bf80, 0x00000000, 0x00003f80,
    0xbf800000, 0x3e480000, 0x3f6d0000, 0xbf0e0000, 0x00000000, 0x00003f6d, 0x0000bf35, 0x0000bec4,
    0x3f800000, 0xbf0e0000, 0xbec40000, 0x3f7b0000, 0x00000000, 0x0000bf35, 0x00003f80, 0x0000bf35,
    0xbf800000, 0x3f550000, 0xbec40000, 0xbe480000, 0x00000000, 0x00003ec4, 0x0000bf35, 0x00003f6d,
    0x3f800000, 0xbf7b0000, 0x3f6d0000, 0xbf550000, 0x00000000, 0x00000000, 0x00000000, 0x00000000,
    0xbf350000, 0xbf0e0000, 0xbec40000, 0xbe480000, 0x00003f80, 0x00003f6d, 0x00003f35, 0x00003ec4,
    0xbf350000, 0xbf7b0000, 0xbf6d0000, 0xbf0e0000, 0x00000000, 0x00003f35, 0x00003f80, 0x00003f35,
    0x3f350000, 0xbe480000, 0xbf6d0000, 0xbf550000, 0x0000bf80, 0x0000bec4, 0x00003f35, 0x00003f6d,
    0x3f350000, 0x3f550000, 0xbec40000, 0xbf7b0000, 0x00000000, 0x0000bf80, 0x00000000, 0x00003f80,
    0xbf350000, 0x3f550000, 0x3ec40000, 0xbf7b0000, 0x00003f80, 0x0000bec4, 0x0000bf35, 0x00003f6d,
    0xbf350000, 0xbe480000, 0x3f6d0000, 0xbf550000, 0x00000000, 0x00003f35, 0x0000bf80, 0x00003f35,
    0x3f350000, 0xbf7b0000, 0x3f6d0000, 0xbf0e0000, 0x0000bf80, 0x00003f6d, 0x0000bf35, 0x00003ec4,
    0x3f350000, 0xbf0e0000, 0x3ec40000, 0xbe480000, 0x00000000, 0x00000000, 0x00000000, 0x00000000,
    0x00003f80, 0x00003f80, 0x00003f80, 0x00003f80, 0x00003f80, 0xbec43f6d, 0xbf353f35, 0xbf6d3ec4,
    0x00003f80, 0xbf353f35, 0xbf800000, 0xbf35bf35, 0x00003f80, 0xbf6d3ec4, 0xbf35bf35, 0x3ec4bf6d,
    0x00003f80, 0xbf800000, 0x0000bf80, 0x3f800000, 0x00003f80, 0xbf6dbec4, 0x3f35bf35, 0x3ec43f6d,
    0x00003f80, 0xbf35bf35, 0x3f800000, 0xbf353f35, 0x00003f80, 0xbec4bf6d, 0x3f353f35, 0xbf6dbec4,
    0x00003f80, 0x0000bf80, 0x00003f80, 0x0000bf80, 0x00003f80, 0x3ec4bf6d, 0xbf353f35, 0x3f6dbec4,
    0x00003f80, 0x3f35bf35, 0xbf800000, 0x3f353f35, 0x00003f80, 0x3f6dbec4, 0xbf35bf35, 0xbec43f6d,
    0x00003f80, 0x3f800000, 0x0000bf80, 0xbf800000, 0x00003f80, 0x3f6d3ec4, 0x3f35bf35, 0xbec4bf6d,
    0x00003f80, 0x3f353f35, 0x3f800000, 0x3f35bf35, 0x00003f80, 0x3ec43f6d, 0x3f353f35, 0x3f6d3ec4,
    0x00003f80, 0x00003f80, 0x00003f80, 0x00003f80, 0xbf800000, 0xbf6dbec4, 0xbf35bf35, 0xbec4bf6d,
    0x0000bf80, 0x3f35bf35, 0x3f800000, 0x3f353f35, 0x3f800000, 0x3ec43f6d, 0xbf353f35, 0xbf6dbec4,
    0x00003f80, 0xbf800000, 0x0000bf80, 0x3f800000, 0xbf800000, 0x3ec4bf6d, 0x3f353f35, 0xbf6d3ec4,
    0x0000bf80, 0x3f353f35, 0xbf800000, 0x3f35bf35, 0x3f800000, 0xbf6d3ec4, 0x3f35bf35, 0xbec43f6d,
    0x00003f80, 0x0000bf80, 0x00003f80, 0x0000bf80, 0xbf800000, 0x3f6d3ec4, 0xbf35bf35, 0x3ec43f6d,
    0x0000bf80, 0xbf353f35, 0x3f800000, 0xbf35bf35, 0x3f800000, 0xbec4bf6d, 0xbf353f35, 0x3f6d3ec4,
    0x00003f80, 0x3f800000, 0x0000bf80, 0xbf800000, 0xbf800000, 0xbec43f6d, 0x3f353f35, 0x3f6dbec4,
    0x0000bf80, 0xbf35bf35, 0xbf800000, 0xbf353f35, 0x3f800000, 0x3f6dbec4, 0x3f35bf35, 0x3ec4bf6d,
    0x00003f80, 0x00003f80, 0x00003f80, 0x00003f80, 0x0000bf80, 0xbec4bf6d, 0xbf35bf35, 0xbf6dbec4,
    0x00003f80, 0x3f353f35, 0x3f800000, 0x3f35bf35, 0x0000bf80, 0xbf6dbec4, 0xbf353f35, 0x3ec43f6d,
    0x00003f80, 0x3f800000, 0x0000bf80, 0xbf800000, 0x0000bf80, 0xbf6d3ec4, 0x3f353f35, 0x3ec4bf6d,
    0x00003f80, 0x3f35bf35, 0xbf800000, 0x3f353f35, 0x0000bf80, 0xbec43f6d, 0x3f35bf35, 0xbf6d3ec4,
    0x00003f80, 0x0000bf80, 0x00003f80, 0x0000bf80, 0x0000bf80, 0x3ec43f6d, 0xbf35bf35, 0x3f6d3ec4,
    0x00003f80, 0xbf35bf35, 0x3f800000, 0xbf353f35, 0x0000bf80, 0x3f6d3ec4, 0xbf353f35, 0xbec4bf6d,
    0x00003f80, 0xbf800000, 0x0000bf80, 0x3f800000, 0x0000bf80, 0x3f6dbec4, 0x3f353f35, 0xbec43f6d,
    0x00003f80, 0xbf353f35, 0xbf800000, 0xbf35bf35, 0x0000bf80, 0x3ec4bf6d, 0x3f35bf35, 0x3f6dbec4,
    0x00003f80, 0x00003f80, 0x00003f80, 0x00003f80, 0xbf800000, 0xbf6d3ec4, 0xbf353f35, 0xbec43f6d,
    0x0000bf80, 0xbf35bf35, 0xbf800000, 0xbf353f35, 0x3f800000, 0x3ec4bf6d, 0xbf35bf35, 0xbf6d3ec4,
    0x00003f80, 0x3f800000, 0x0000bf80, 0xbf800000, 0xbf800000, 0x3ec43f6d, 0x3f35bf35, 0xbf6dbec4,
    0x0000bf80, 0xbf353f35, 0x3f800000, 0xbf35bf35, 0x3f800000, 0xbf6dbec4, 0x3f353f35, 0xbec4bf6d,
    0x00003f80, 0x0000bf80, 0x00003f80, 0x0000bf80, 0xbf800000, 0x3f6dbec4, 0xbf353f35, 0x3ec4bf6d,
    0x0000bf80, 0x3f353f35, 0xbf800000, 0x3f35bf35, 0x3f800000, 0xbec43f6d, 0xbf35bf35, 0x3f6dbec4,
    0x00003f80, 0xbf800000, 0x0000bf80, 0x3f800000, 0xbf800000, 0xbec4bf6d, 0x3f35bf35, 0x3f6d3ec4,
    0x0000bf80, 0x3f35bf35, 0x3f800000, 0x3f353f35, 0x3f800000, 0x3f6d3ec4, 0x3f353f35, 0x3ec43f6d,
    0x3f800000, 0x3f800000, 0x3f800000, 0x3f800000, 0x3f800000, 0x3f6d3ec4, 0x3f353f35, 0x3ec43f6d,
    0x3f800000, 0x3f353f35, 0x00003f80, 0xbf353f35, 0x3f800000, 0x3ec43f6d, 0xbf353f35, 0xbf6dbec4,
    0x3f800000, 0x00003f80, 0xbf800000, 0x0000bf80, 0x3f800000, 0xbec43f6d, 0xbf35bf35, 0x3f6dbec4,
    0x3f800000, 0xbf353f35, 0x0000bf80, 0x3f353f35, 0x3f800000, 0xbf6d3ec4, 0x3f35bf35, 0xbec43f6d,
    0x3f800000, 0xbf800000, 0x3f800000, 0xbf800000, 0x3f800000, 0xbf6dbec4, 0x3f353f35, 0xbec4bf6d,
    0x3f800000, 0xbf35bf35, 0x00003f80, 0x3f35bf35, 0x3f800000, 0xbec4bf6d, 0xbf353f35, 0x3f6d3ec4,
    0x3f800000, 0x0000bf80, 0xbf800000, 0x00003f80, 0x3f800000, 0x3ec4bf6d, 0xbf35bf35, 0xbf6d3ec4,
    0x3f800000, 0x3f35bf35, 0x0000bf80, 0xbf35bf35, 0x3f800000, 0x3f6dbec4, 0x3f35bf35, 0x3ec4bf6d,
    0x3f800000, 0x3f800000, 0x3f800000, 0x3f800000, 0x00003f80, 0xbec43f6d, 0xbf353f35, 0xbf6d3ec4,
    0xbf800000, 0xbf35bf35, 0x0000bf80, 0x3f35bf35, 0x0000bf80, 0x3f6dbec4, 0x3f353f35, 0xbec43f6d,
    0x3f800000, 0x00003f80, 0xbf800000, 0x0000bf80, 0x00003f80, 0xbf6dbec4, 0x3f35bf35, 0x3ec43f6d,
    0xbf800000, 0x3f35bf35, 0x00003f80, 0xbf35bf35, 0x0000bf80, 0x3ec43f6d, 0xbf35bf35, 0x3f6d3ec4,
    0x3f800000, 0xbf800000, 0x3f800000, 0xbf800000, 0x00003f80, 0x3ec4bf6d, 0xbf353f35, 0x3f6dbec4,
    0xbf800000, 0x3f353f35, 0x0000bf80, 0xbf353f35, 0x0000bf80, 0xbf6d3ec4, 0x3f353f35, 0x3ec4bf6d,
    0x3f800000, 0x0000bf80, 0xbf800000, 0x00003f80, 0x00003f80, 0x3f6d3ec4, 0x3f35bf35, 0xbec4bf6d,
    0xbf800000, 0xbf353f35, 0x00003f80, 0x3f353f35, 0x0000bf80, 0xbec4bf6d, 0xbf35bf35, 0xbf6dbec4,
    0xbf800000, 0xbf800000, 0xbf800000, 0xbf800000, 0x3f800000, 0x3f6dbec4, 0x3f35bf35, 0x3ec4bf6d,
    0xbf800000, 0xbf353f35, 0x00003f80, 0x3f353f35, 0x3f800000, 0x3ec4bf6d, 0xbf35bf35, 0xbf6d3ec4,
    0xbf800000, 0x00003f80, 0x3f800000, 0x0000bf80, 0x3f800000, 0xbec4bf6d, 0xbf353f35, 0x3f6d3ec4,
    0xbf800000, 0x3f353f35, 0x0000bf80, 0xbf353f35, 0x3f800000, 0xbf6dbec4, 0x3f353f35, 0xbec4bf6d,
    0xbf800000, 0x3f800000, 0xbf800000, 0x3f800000, 0x3f800000, 0xbf6d3ec4, 0x3f35bf35, 0xbec43f6d,
    0xbf800000, 0x3f35bf35, 0x00003f80, 0xbf35bf35, 0x3f800000, 0xbec43f6d, 0xbf35bf35, 0x3f6dbec4,
    0xbf800000, 0x0000bf80, 0x3f800000, 0x00003f80, 0x3f800000, 0x3ec43f6d, 0xbf353f35, 0xbf6dbec4,
    0xbf800000, 0xbf35bf35, 0x0000bf80, 0x3f35bf35, 0x3f800000, 0x3f6d3ec4, 0x3f353f35, 0x3ec43f6d,
    0xbf800000, 0xbf800000, 0xbf800000, 0xbf800000, 0x0000bf80, 0xbec4bf6d, 0xbf35bf35, 0xbf6dbec4,
    0x3f800000, 0x3f35bf35, 0x0000bf80, 0xbf35bf35, 0x00003f80, 0x3f6d3ec4, 0x3f35bf35, 0xbec4bf6d,
    0xbf800000, 0x00003f80, 0x3f800000, 0x0000bf80, 0x0000bf80, 0xbf6d3ec4, 0x3f353f35, 0x3ec4bf6d,
    0x3f800000, 0xbf35bf35, 0x00003f80, 0x3f35bf35, 0x00003f80, 0x3ec4bf6d, 0xbf353f35, 0x3f6dbec4,
    0xbf800000, 0x3f800000, 0xbf800000, 0x3f800000, 0x0000bf80, 0x3ec43f6d, 0xbf35bf35, 0x3f6d3ec4,
    0x3f800000, 0xbf353f35, 0x0000bf80, 0x3f353f35, 0x00003f80, 0xbf6dbec4, 0x3f35bf35, 0x3ec43f6d,
    0xbf800000, 0x0000bf80, 0x3f800000, 0x00003f80, 0x0000bf80, 0x3f6dbec4, 0x3f353f35, 0xbec43f6d,
    0x3f800000, 0x3f353f35, 0x00003f80, 0xbf353f35, 0x00003f80, 0xbec43f6d, 0xbf353f35, 0xbf6d3ec4,
    0x00000000, 0x00003f80, 0x00000000, 0x00003f80, 0x00000000, 0xbec43f6d, 0x00000000, 0xbf353f35,
    0x00000000, 0xbf353f35, 0x00000000, 0xbf800000, 0x00000000, 0xbf6d3ec4, 0x00000000, 0xbf35bf35,
    0x00000000, 0xbf800000, 0x00000000, 0x0000bf80, 0x00000000, 0xbf6dbec4, 0x00000000, 0x3f35bf35,
    0x00000000, 0xbf35bf35, 0x00000000, 0x3f800000, 0x00000000, 0xbec4bf6d, 0x00000000, 0x3f353f35,
    0x00000000, 0x0000bf80, 0x00000000, 0x00003f80, 0x00000000, 0x3ec4bf6d, 0x00000000, 0xbf353f35,
    0x00000000, 0x3f35bf35, 0x00000000, 0xbf800000, 0x00000000, 0x3f6dbec4, 0x00000000, 0xbf35bf35,
    0x00000000, 0x3f800000, 0x00000000, 0x0000bf80, 0x00000000, 0x3f6d3ec4, 0x00000000, 0x3f35bf35,
    0x00000000, 0x3f353f35, 0x00000000, 0x3f800000, 0x00000000, 0x3ec43f6d, 0x00000000, 0x3f353f35,
    0x00000000, 0x00003f80, 0x00000000, 0x00003f80, 0x00000000, 0xbf6d3ec4, 0x00000000, 0xbf800000,
    0x00000000, 0xbf35bf35, 0x00000000, 0x0000bf80, 0x00000000, 0x3ec4bf6d, 0x00000000, 0x3f800000,
    0x00000000, 0x3f800000, 0x00000000, 0x00003f80, 0x00000000, 0x3ec43f6d, 0x00000000, 0xbf800000,
    0x00000000, 0xbf353f35, 0x00000000, 0x0000bf80, 0x00000000, 0xbf6dbec4, 0x00000000, 0x3f800000,
    0x00000000, 0x0000bf80, 0x00000000, 0x00003f80, 0x00000000, 0x3f6dbec4, 0x00000000, 0xbf800000,
    0x00000000, 0x3f353f35, 0x00000000, 0x0000bf80, 0x00000000, 0xbec43f6d, 0x00000000, 0x3f800000,
    0x00000000, 0xbf800000, 0x00000000, 0x00003f80, 0x00000000, 0xbec4bf6d, 0x00000000, 0xbf800000,
    0x00000000, 0x3f35bf35, 0x00000000, 0x0000bf80, 0x00000000, 0x3f6d3ec4, 0x00000000, 0x3f800000,
    0x00000000, 0x00003f80, 0x00000000, 0x00003f80, 0x00000000, 0xbf6dbec4, 0x00000000, 0xbf35bf35,
    0x00000000, 0x3f35bf35, 0x00000000, 0x3f800000, 0x00000000, 0x3ec43f6d, 0x00000000, 0xbf353f35,
    0x00000000, 0xbf800000, 0x00000000, 0x0000bf80, 0x00000000, 0x3ec4bf6d, 0x00000000, 0x3f353f35,
    0x00000000, 0x3f353f35, 0x00000000, 0xbf800000, 0x00000000, 0xbf6d3ec4, 0x00000000, 0x3f35bf35,
    0x00000000, 0x0000bf80, 0x00000000, 0x00003f80, 0x00000000, 0x3f6d3ec4, 0x00000000, 0xbf35bf35,
    0x00000000, 0xbf353f35, 0x00000000, 0x3f800000, 0x00000000, 0xbec4bf6d, 0x00000000, 0xbf353f35,
    0x00000000, 0x3f800000, 0x00000000, 0x0000bf80, 0x00000000, 0xbec43f6d, 0x00000000, 0x3f353f35,
    0x00000000, 0xbf35bf35, 0x00000000, 0xbf800000, 0x00000000, 0x3f6dbec4, 0x00000000, 0x3f35bf35,
    0x00000000, 0x00003f80, 0x00000000, 0x00003f00, 0x00000000, 0xbec4bf6d, 0x00000000, 0x0000bf00,
    0x00000000, 0x3f353f35, 0x00000000, 0x00003f00, 0x00000000, 0xbf6dbec4, 0x00000000, 0x0000bf00,
    0x00000000, 0x3f800000, 0x00000000, 0x00003f00, 0x00000000, 0xbf6d3ec4, 0x00000000, 0x0000bf00,
    0x00000000, 0x3f35bf35, 0x00000000, 0x00003f00, 0x00000000, 0xbec43f6d, 0x00000000, 0x0000bf00,
    0x00000000, 0x0000bf80, 0x00000000, 0x00003f00, 0x00000000, 0x3ec43f6d, 0x00000000, 0x0000bf00,
    0x00000000, 0xbf35bf35, 0x00000000, 0x00003f00, 0x00000000, 0x3f6d3ec4, 0x00000000, 0x0000bf00,
    0x00000000, 0xbf800000, 0x00000000, 0x00003f00, 0x00000000, 0x3f6dbec4, 0x00000000, 0x0000bf00,
    0x00000000, 0xbf353f35, 0x00000000, 0x00003f00, 0x00000000, 0x3ec4bf6d, 0x00000000, 0x0000bf00,
    0x00003f80, 0x00000000, 0x00003f80, 0x00000000, 0xbe483f7b, 0x00000000, 0xbf0e3f55, 0x00000000,
    0xbec43f6d, 0x00000000, 0xbf6d3ec4, 0x00000000, 0xbf0e3f55, 0x00000000, 0xbf7bbe48, 0x00000000,
    0xbf353f35, 0x00000000, 0xbf35bf35, 0x00000000, 0xbf553f0e, 0x00000000, 0xbe48bf7b, 0x00000000,
    0xbf6d3ec4, 0x00000000, 0x3ec4bf6d, 0x00000000, 0xbf7b3e48, 0x00000000, 0x3f55bf0e, 0x00000000,
    0xbf800000, 0x00000000, 0x3f800000, 0x00000000, 0xbf7bbe48, 0x00000000, 0x3f553f0e, 0x00000000,
    0xbf6dbec4, 0x00000000, 0x3ec43f6d, 0x00000000, 0xbf55bf0e, 0x00000000, 0xbe483f7b, 0x00000000,
    0xbf35bf35, 0x00000000, 0xbf353f35, 0x00000000, 0xbf0ebf55, 0x00000000, 0xbf7b3e48, 0x00000000,
    0xbec4bf6d, 0x00000000, 0xbf6dbec4, 0x00000000, 0xbe48bf7b, 0x00000000, 0xbf0ebf55, 0x00000000,
    0x00003f80, 0x00000000, 0x00003f80, 0x00000000, 0xbf553f0e, 0x00000000, 0xbf7b3e48, 0x00000000,
    0xbf6dbec4, 0x00000000, 0xbec4bf6d, 0x00000000, 0xbe48bf7b, 0x00000000, 0x3f55bf0e, 0x00000000,
    0x3f35bf35, 0x00000000, 0x3f353f35, 0x00000000, 0x3f7b3e48, 0x00000000, 0xbf0e3f55, 0x00000000,
    0x3ec43f6d, 0x00000000, 0xbf6dbec4, 0x00000000, 0xbf0e3f55, 0x00000000, 0x3e48bf7b, 0x00000000,
    0xbf800000, 0x00000000, 0x3f800000, 0x00000000, 0xbf0ebf55, 0x00000000, 0x3e483f7b, 0x00000000,
    0x3ec4bf6d, 0x00000000, 0xbf6d3ec4, 0x00000000, 0x3f7bbe48, 0x00000000, 0xbf0ebf55, 0x00000000,
    0x3f353f35, 0x00000000, 0x3f35bf35, 0x00000000, 0xbe483f7b, 0x00000000, 0x3f553f0e, 0x00000000,
    0xbf6d3ec4, 0x00000000, 0xbec43f6d, 0x00000000, 0xbf55bf0e, 0x00000000, 0xbf7bbe48, 0x00000000,
    0x00003f80, 0x00000000, 0x00003f80, 0x00000000, 0xbf7bbe48, 0x00000000, 0xbf55bf0e, 0x00000000,
    0x3ec4bf6d, 0x00000000, 0x3f6dbec4, 0x00000000, 0x3f553f0e, 0x00000000, 0xbe483f7b, 0x00000000,
    0xbf353f35, 0x00000000, 0xbf35bf35, 0x00000000, 0xbf0ebf55, 0x00000000, 0x3f7bbe48, 0x00000000,
    0x3f6dbec4, 0x00000000, 0xbec43f6d, 0x00000000, 0x3e483f7b, 0x00000000, 0xbf0ebf55, 0x00000000,
    0xbf800000, 0x00000000, 0x3f800000, 0x00000000, 0x3e48bf7b, 0x00000000, 0xbf0e3f55, 0x00000000,
    0x3f6d3ec4, 0x00000000, 0xbec4bf6d, 0x00000000, 0xbf0e3f55, 0x00000000, 0x3f7b3e48, 0x00000000,
    0xbf35bf35, 0x00000000, 0xbf353f35, 0x00000000, 0x3f55bf0e, 0x00000000, 0xbe48bf7b, 0x00000000,
    0x3ec43f6d, 0x00000000, 0x3f6d3ec4, 0x00000000, 0xbf7b3e48, 0x00000000, 0xbf553f0e, 0x00000000,
    0x00003f80, 0x00000000, 0x00003f80, 0x00000000, 0xbf0ebf55, 0x00000000, 0xbe48bf7b, 0x00000000,
    0x3f6d3ec4, 0x00000000, 0x3ec43f6d, 0x00000000, 0xbf7b3e48, 0x00000000, 0xbf0ebf55, 0x00000000,
    0x3f35bf35, 0x00000000, 0x3f353f35, 0x00000000, 0xbe483f7b, 0x00000000, 0xbf55bf0e, 0x00000000,
    0xbec4bf6d, 0x00000000, 0x3f6d3ec4, 0x00000000, 0x3f553f0e, 0x00000000, 0xbf7bbe48, 0x00000000,
    0xbf800000, 0x00000000, 0x3f800000, 0x00000000, 0x3f55bf0e, 0x00000000, 0xbf7b3e48, 0x00000000,
    0xbec43f6d, 0x00000000, 0x3f6dbec4, 0x00000000, 0xbe48bf7b, 0x00000000, 0xbf553f0e, 0x00000000,
    0x3f353f35, 0x00000000, 0x3f35bf35, 0x00000000, 0xbf7bbe48, 0x00000000, 0xbf0e3f55, 0x00000000,
    0x3f6dbec4, 0x00000000, 0x3ec4bf6d, 0x00000000, 0xbf0e3f55, 0x00000000, 0xbe483f7b, 0x00000000,
    0x3f803f80, 0x00003f80, 0x00003f80, 0x00003f80, 0xbf803f80, 0xbe483f7b, 0xbec43f6d, 0xbf0e3f55,
    0x3f803f80, 0xbec43f6d, 0xbf353f35, 0xbf6d3ec4, 0xbf803f80, 0xbf0e3f55, 0xbf6d3ec4, 0xbf7bbe48,
    0x3f803f80, 0xbf353f35, 0xbf800000, 0xbf35bf35, 0xbf803f80, 0xbf553f0e, 0xbf6dbec4, 0xbe48bf7b,
    0x3f803f80, 0xbf6d3ec4, 0xbf35bf35, 0x3ec4bf6d, 0xbf803f80, 0xbf7b3e48, 0xbec4bf6d, 0x3f55bf0e,
    0x3f803f80, 0xbf800000, 0x0000bf80, 0x3f800000, 0xbf803f80, 0xbf7bbe48, 0x3ec4bf6d, 0x3f553f0e,
    0x3f803f80, 0xbf6dbec4, 0x3f35bf35, 0x3ec43f6d, 0xbf803f80, 0xbf55bf0e, 0x3f6dbec4, 0xbe483f7b,
    0x3f803f80, 0xbf35bf35, 0x3f800000, 0xbf353f35, 0xbf803f80, 0xbf0ebf55, 0x3f6d3ec4, 0xbf7b3e48,
    0x3f803f80, 0xbec4bf6d, 0x3f353f35, 0xbf6dbec4, 0xbf803f80, 0xbe48bf7b, 0x3ec43f6d, 0xbf0ebf55,
    0x00003f80, 0x00003f80, 0x00003f80, 0x00003f80, 0xbf353f35, 0xbf553f0e, 0xbf6d3ec4, 0xbf7b3e48,
    0xbf800000, 0xbf6dbec4, 0xbf35bf35, 0xbec4bf6d, 0xbf35bf35, 0xbe48bf7b, 0x3ec4bf6d, 0x3f55bf0e,
    0x0000bf80, 0x3f35bf35, 0x3f800000, 0x3f353f35, 0x3f35bf35, 0x3f7b3e48, 0x3ec43f6d, 0xbf0e3f55,
    0x3f800000, 0x3ec43f6d, 0xbf353f35, 0xbf6dbec4, 0x3f353f35, 0xbf0e3f55, 0xbf6dbec4, 0x3e48bf7b,
    0x00003f80, 0xbf800000, 0x0000bf80, 0x3f800000, 0xbf353f35, 0xbf0ebf55, 0x3f6dbec4, 0x3e483f7b,
    0xbf800000, 0x3ec4bf6d, 0x3f353f35, 0xbf6d3ec4, 0xbf35bf35, 0x3f7bbe48, 0xbec43f6d, 0xbf0ebf55,
    0x0000bf80, 0x3f353f35, 0xbf800000, 0x3f35bf35, 0x3f35bf35, 0xbe483f7b, 0xbec4bf6d, 0x3f553f0e,
    0x3f800000, 0xbf6d3ec4, 0x3f35bf35, 0xbec43f6d, 0x3f353f35, 0xbf55bf0e, 0x3f6d3ec4, 0xbf7bbe48,
    0x00003f80, 0x00003f80, 0x00003f80, 0x00003f80, 0xbf800000, 0xbf7bbe48, 0xbf6dbec4, 0xbf55bf0e,
    0x0000bf80, 0x3ec4bf6d, 0x3f35bf35, 0x3f6dbec4, 0x3f800000, 0x3f553f0e, 0x3ec43f6d, 0xbe483f7b,
    0x00003f80, 0xbf353f35, 0xbf800000, 0xbf35bf35, 0xbf800000, 0xbf0ebf55, 0x3ec4bf6d, 0x3f7bbe48,
    0x0000bf80, 0x3f6dbec4, 0x3f353f35, 0xbec43f6d, 0x3f800000, 0x3e483f7b, 0xbf6d3ec4, 0xbf0ebf55,
    0x00003f80, 0xbf800000, 0x0000bf80, 0x3f800000, 0xbf800000, 0x3e48bf7b, 0x3f6d3ec4, 0xbf0e3f55,
    0x0000bf80, 0x3f6d3ec4, 0xbf353f35, 0xbec4bf6d, 0x3f800000, 0xbf0e3f55, 0xbec4bf6d, 0x3f7b3e48,
    0x00003f80, 0xbf35bf35, 0x3f800000, 0xbf353f35, 0xbf800000, 0x3f55bf0e, 0xbec43f6d, 0xbe48bf7b,
    0x0000bf80, 0x3ec43f6d, 0xbf35bf35, 0x3f6d3ec4, 0x3f800000, 0xbf7b3e48, 0x3f6dbec4, 0xbf553f0e,
    0x00003f80, 0x00003f80, 0x00003f80, 0x00003f80, 0xbf35bf35, 0xbf0ebf55, 0xbec4bf6d, 0xbe48bf7b,
    0x3f800000, 0x3f6d3ec4, 0x3f353f35, 0x3ec43f6d, 0xbf353f35, 0xbf7b3e48, 0xbf6dbec4, 0xbf0ebf55,
    0x0000bf80, 0x3f35bf35, 0x3f800000, 0x3f353f35, 0x3f353f35, 0xbe483f7b, 0xbf6d3ec4, 0xbf55bf0e,
    0xbf800000, 0xbec4bf6d, 0x3f35bf35, 0x3f6d3ec4, 0x3f35bf35, 0x3f553f0e, 0xbec43f6d, 0xbf7bbe48,
    0x00003f80, 0xbf800000, 0x0000bf80, 0x3f800000, 0xbf35bf35, 0x3f55bf0e, 0x3ec43f6d, 0xbf7b3e48,
    0x3f800000, 0xbec43f6d, 0xbf35bf35, 0x3f6dbec4, 0xbf353f35, 0xbe48bf7b, 0x3f6d3ec4, 0xbf553f0e,
    0x0000bf80, 0x3f353f35, 0xbf800000, 0x3f35bf35, 0x3f353f35, 0xbf7bbe48, 0x3f6dbec4, 0xbf0e3f55,
    0xbf800000, 0x3f6dbec4, 0xbf353f35, 0x3ec4bf6d, 0x3f35bf35, 0xbf0e3f55, 0x3ec4bf6d, 0xbe483f7b,
    0x3f803f80, 0x0000bf80, 0x00003f80, 0x0000bf80, 0xbf803f80, 0x3e48bf7b, 0xbec43f6d, 0x3f0ebf55,
    0x3f803f80, 0x3ec4bf6d, 0xbf353f35, 0x3f6dbec4, 0xbf803f80, 0x3f0ebf55, 0xbf6d3ec4, 0x3f7b3e48,
    0x3f803f80, 0x3f35bf35, 0xbf800000, 0x3f353f35, 0xbf803f80, 0x3f55bf0e, 0xbf6dbec4, 0x3e483f7b,
    0x3f803f80, 0x3f6dbec4, 0xbf35bf35, 0xbec43f6d, 0xbf803f80, 0x3f7bbe48, 0xbec4bf6d, 0xbf553f0e,
    0x3f803f80, 0x3f800000, 0x0000bf80, 0xbf800000, 0xbf803f80, 0x3f7b3e48, 0x3ec4bf6d, 0xbf55bf0e,
    0x3f803f80, 0x3f6d3ec4, 0x3f35bf35, 0xbec4bf6d, 0xbf803f80, 0x3f553f0e, 0x3f6dbec4, 0x3e48bf7b,
    0x3f803f80, 0x3f353f35, 0x3f800000, 0x3f35bf35, 0xbf803f80, 0x3f0e3f55, 0x3f6d3ec4, 0x3f7bbe48,
    0x3f803f80, 0x3ec43f6d, 0x3f353f35, 0x3f6d3ec4, 0xbf803f80, 0x3e483f7b, 0x3ec43f6d, 0x3f0e3f55,
    0x00003f80, 0x0000bf80, 0x00003f80, 0x0000bf80, 0xbf353f35, 0x3f55bf0e, 0xbf6d3ec4, 0x3f7bbe48,
    0xbf800000, 0x3f6d3ec4, 0xbf35bf35, 0x3ec43f6d, 0xbf35bf35, 0x3e483f7b, 0x3ec4bf6d, 0xbf553f0e,
    0x0000bf80, 0xbf353f35, 0x3f800000, 0xbf35bf35, 0x3f35bf35, 0xbf7bbe48, 0x3ec43f6d, 0x3f0ebf55,
    0x3f800000, 0xbec4bf6d, 0xbf353f35, 0x3f6d3ec4, 0x3f353f35, 0x3f0ebf55, 0xbf6dbec4, 0xbe483f7b,
    0x00003f80, 0x3f800000, 0x0000bf80, 0xbf800000, 0xbf353f35, 0x3f0e3f55, 0x3f6dbec4, 0xbe48bf7b,
    0xbf800000, 0xbec43f6d, 0x3f353f35, 0x3f6dbec4, 0xbf35bf35, 0xbf7b3e48, 0xbec43f6d, 0x3f0e3f55,
    0x0000bf80, 0xbf35bf35, 0xbf800000, 0xbf353f35, 0x3f35bf35, 0x3e48bf7b, 0xbec4bf6d, 0xbf55bf0e,
    0x3f800000, 0x3f6dbec4, 0x3f35bf35, 0x3ec4bf6d, 0x3f353f35, 0x3f553f0e, 0x3f6d3ec4, 0x3f7b3e48,
    0x00003f80, 0x0000bf80, 0x00003f80, 0x0000bf80, 0xbf800000, 0x3f7b3e48, 0xbf6dbec4, 0x3f553f0e,
    0x0000bf80, 0xbec43f6d, 0x3f35bf35, 0xbf6d3ec4, 0x3f800000, 0xbf55bf0e, 0x3ec43f6d, 0x3e48bf7b,
    0x00003f80, 0x3f35bf35, 0xbf800000, 0x3f353f35, 0xbf800000, 0x3f0e3f55, 0x3ec4bf6d, 0xbf7b3e48,
    0x0000bf80, 0xbf6d3ec4, 0x3f353f35, 0x3ec4bf6d, 0x3f800000, 0xbe48bf7b, 0xbf6d3ec4, 0x3f0e3f55,
    0x00003f80, 0x3f800000, 0x0000bf80, 0xbf800000, 0xbf800000, 0xbe483f7b, 0x3f6d3ec4, 0x3f0ebf55,
    0x0000bf80, 0xbf6dbec4, 0xbf353f35, 0x3ec43f6d, 0x3f800000, 0x3f0ebf55, 0xbec4bf6d, 0xbf7bbe48,
    0x00003f80, 0x3f353f35, 0x3f800000, 0x3f35bf35, 0xbf800000, 0xbf553f0e, 0xbec43f6d, 0x3e483f7b,
    0x0000bf80, 0xbec4bf6d, 0xbf35bf35, 0xbf6dbec4, 0x3f800000, 0x3f7bbe48, 0x3f6dbec4, 0x3f55bf0e,
    0x00003f80, 0x0000bf80, 0x00003f80, 0x0000bf80, 0xbf35bf35, 0x3f0e3f55, 0xbec4bf6d, 0x3e483f7b,
    0x3f800000, 0xbf6dbec4, 0x3f353f35, 0xbec4bf6d, 0xbf353f35, 0x3f7bbe48, 0xbf6dbec4, 0x3f0e3f55,
    0x0000bf80, 0xbf353f35, 0x3f800000, 0xbf35bf35, 0x3f353f35, 0x3e48bf7b, 0xbf6d3ec4, 0x3f553f0e,
    0xbf800000, 0x3ec43f6d, 0x3f35bf35, 0xbf6dbec4, 0x3f35bf35, 0xbf55bf0e, 0xbec43f6d, 0x3f7b3e48,
    0x00003f80, 0x3f800000, 0x0000bf80, 0xbf800000, 0xbf35bf35, 0xbf553f0e, 0x3ec43f6d, 0x3f7bbe48,
    0x3f800000, 0x3ec4bf6d, 0xbf35bf35, 0xbf6d3ec4, 0xbf353f35, 0x3e483f7b, 0x3f6d3ec4, 0x3f55bf0e,
    0x0000bf80, 0xbf35bf35, 0xbf800000, 0xbf353f35, 0x3f353f35, 0x3f7b3e48, 0x3f6dbec4, 0x3f0ebf55,
    0xbf800000, 0xbf6d3ec4, 0xbf353f35, 0xbec43f6d, 0x3f35bf35, 0x3f0ebf55, 0x3ec4bf6d, 0x3e48bf7b,
    0x00000000, 0x00000000, 0x00000000, 0x00000000, 0x00000000, 0x00000000, 0x00000000, 0x00000000,
    0x00000000, 0x00000000, 0x00000000, 0x00000000, 0x00000000, 0x00000000, 0x00000000, 0x00000000,
    0x00000000, 0x00000000, 0x00000000, 0x00000000, 0x00000000, 0x00000000, 0x00000000, 0x00000000,
    0x00000000, 0x00000000, 0x00000000, 0x00000000, 0x00000000, 0x00000000, 0x00000000, 0x00000000,
    0x00000000, 0x00000000, 0x00000000, 0x00000000, 0x00000000, 0x00000000, 0x00000000, 0x00000000,
    0x00000000, 0x00000000, 0x00000000, 0x00000000, 0x00000000, 0x00000000, 0x00000000, 0x00000000,
    0x00000000, 0x00000000, 0x00000000, 0x00000000, 0x00000000, 0x00000000, 0x00000000, 0x00000000,
    0x00000000, 0x00000000, 0x00000000, 0x00000000, 0x00000000, 0x00000000, 0x00000000, 0x00000000,
    0x00000000, 0x00000000, 0x00000000, 0x00000000, 0x00000000, 0x00000000, 0x00000000, 0x00000000,
    0x00000000, 0x00000000, 0x00000000, 0x00000000, 0x00000000, 0x00000000, 0x00000000, 0x00000000,
    0x00000000, 0x00000000, 0x00000000, 0x00000000, 0x00000000, 0x00000000, 0x00000000, 0x00000000,
    0x00000000, 0x00000000, 0x00000000, 0x00000000, 0x00000000, 0x00000000, 0x00000000, 0x00000000,
    0x00000000, 0x00000000, 0x00000000, 0x00000000, 0x00000000, 0x00000000, 0x00000000, 0x00000000,
    0x00000000, 0x00000000, 0x00000000, 0x00000000, 0x00000000, 0x00000000, 0x00000000, 0x00000000,
    0x00000000, 0x00000000, 0x00000000, 0x00000000, 0x00000000, 0x00000000, 0x00000000, 0x00000000,
    0x00000000, 0x00000000, 0x00000000, 0x00000000, 0x00000000, 0x00000000, 0x00000000, 0x00000000,
    0x00000000, 0x00000000, 0x00000000, 0x00000000, 0x00000000, 0x00000000, 0x00000000, 0x00000000,
    0x00000000, 0x00000000, 0x00000000, 0x00000000, 0x00000000, 0x00000000, 0x00000000, 0x00000000,
    0x00000000, 0x00000000, 0x00000000, 0x00000000, 0x00000000, 0x00000000, 0x00000000, 0x00000000,
    0x00000000, 0x00000000, 0x00000000, 0x00000000, 0x00000000, 0x00000000, 0x00000000, 0x00000000,
    0x00000000, 0x00000000, 0x00000000, 0x00000000, 0x00000000, 0x00000000, 0x00000000, 0x00000000,
    0x00000000, 0x00000000, 0x00000000, 0x00000000, 0x00000000, 0x00000000, 0x00000000, 0x00000000,
    0x00000000, 0x00000000, 0x00000000, 0x00000000, 0x00000000, 0x00000000, 0x00000000, 0x00000000,
    0x00000000, 0x00000000, 0x00000000, 0x00000000, 0x00000000, 0x00000000, 0x00000000, 0x00000000,
    0x00000000, 0x00000000, 0x00000000, 0x00000000, 0x00000000, 0x00000000, 0x00000000, 0x00000000,
    0x00000000, 0x00000000, 0x00000000, 0x00000000, 0x00000000, 0x00000000, 0x00000000, 0x00000000,
    0x00000000, 0x00000000, 0x00000000, 0x00000000, 0x00000000, 0x00000000, 0x00000000, 0x00000000,
    0x00000000, 0x00000000, 0x00000000, 0x00000000, 0x00000000, 0x00000000, 0x00000000, 0x00000000,
    0x00000000, 0x00000000, 0x00000000, 0x00000000, 0x00000000, 0x00000000, 0x00000000, 0x00000000,
    0x00000000, 0x00000000, 0x00000000, 0x00000000, 0x00000000, 0x00000000, 0x00000000, 0x00000000,
    0x00000000, 0x00000000, 0x00000000, 0x00000000, 0x00000000, 0x00000000, 0x00000000, 0x00000000,
    0x00000000, 0x00000000, 0x00000000, 0x00000000, 0x00000000, 0x00000000, 0x00000000, 0x00000000,
    0x00000000, 0x00000000, 0x00000000, 0x00000000, 0x00000000, 0x00000000, 0x00000000, 0x00000000,
    0x00000000, 0x00000000, 0x00000000, 0x00000000, 0x00000000, 0x00000000, 0x00000000, 0x00000000,
    0x00000000, 0x00000000, 0x00000000, 0x00000000, 0x00000000, 0x00000000, 0x00000000, 0x00000000,
    0x00000000, 0x00000000, 0x00000000, 0x00000000, 0x00000000, 0x00000000, 0x00000000, 0x00000000,
    0x00000000, 0x00000000, 0x00000000, 0x00000000, 0x00000000, 0x00000000, 0x00000000, 0x00000000,
    0x00000000, 0x00000000, 0x00000000, 0x00000000, 0x00000000, 0x00000000, 0x00000000, 0x00000000,
    0x00000000, 0x00000000, 0x00000000, 0x00000000, 0x00000000, 0x00000000, 0x00000000, 0x00000000,
    0x00000000, 0x00000000, 0x00000000, 0x00000000, 0x00000000, 0x00000000, 0x00000000, 0x00000000,
    0x00000000, 0x00000000, 0x00000000, 0x00000000, 0x00000000, 0x00000000, 0x00000000, 0x00000000,
    0x00000000, 0x00000000, 0x00000000, 0x00000000, 0x00000000, 0x00000000, 0x00000000, 0x00000000,
    0x00000000, 0x00000000, 0x00000000, 0x00000000, 0x00000000, 0x00000000, 0x00000000, 0x00000000,
    0x00000000, 0x00000000, 0x00000000, 0x00000000, 0x00000000, 0x00000000, 0x00000000, 0x00000000,
    0x00000000, 0x00000000, 0x00000000, 0x00000000, 0x00000000, 0x00000000, 0x00000000, 0x00000000,
    0x00000000, 0x00000000, 0x00000000, 0x00000000, 0x00000000, 0x00000000, 0x00000000, 0x00000000,
    0x00000000, 0x00000000, 0x00000000, 0x00000000, 0x00000000, 0x00000000, 0x00000000, 0x00000000,
    0x00000000, 0x00000000, 0x00000000, 0x00000000, 0x00000000, 0x00000000, 0x00000000, 0x00000000,
    0x00000000, 0x00000000, 0x00000000, 0x00000000, 0x00000000, 0x00000000, 0x00000000, 0x00000000,
    0x00000000, 0x00000000, 0x00000000, 0x00000000, 0x00000000, 0x00000000, 0x00000000, 0x00000000,
    0x00000000, 0x00000000, 0x00000000, 0x00000000, 0x00000000, 0x00000000, 0x00000000, 0x00000000,
    0x00000000, 0x00000000, 0x00000000, 0x00000000, 0x00000000, 0x00000000, 0x00000000, 0x00000000,
    0x00000000, 0x00000000, 0x00000000, 0x00000000, 0x00000000, 0x00000000, 0x00000000, 0x00000000,
    0x00000000, 0x00000000, 0x00000000, 0x00000000, 0x00000000, 0x00000000, 0x00000000, 0x00000000,
    0x00000000, 0x00000000, 0x00000000, 0x00000000, 0x00000000, 0x00000000, 0x00000000, 0x00000000,
    0x00000000, 0x00000000, 0x00000000, 0x00000000, 0x00000000, 0x00000000, 0x00000000, 0x00000000,
    0x00000000, 0x00000000, 0x00000000, 0x00000000, 0x00000000, 0x00000000, 0x00000000, 0x00000000,
    0x00000000, 0x00000000, 0x00000000, 0x00000000, 0x00000000, 0x00000000, 0x00000000, 0x00000000,
    0x00000000, 0x00000000, 0x00000000, 0x00000000, 0x00000000, 0x00000000, 0x00000000, 0x00000000,
    0x00000000, 0x00000000, 0x00000000, 0x00000000, 0x00000000, 0x00000000, 0x00000000, 0x00000000,
    0x00000000, 0x00000000, 0x00000000, 0x00000000, 0x00000000, 0x00000000, 0x00000000, 0x00000000,
    0x00000000, 0x00000000, 0x00000000, 0x00000000, 0x00000000, 0x00000000, 0x00000000, 0x00000000,
    0x00000000, 0x00000000, 0x00000000, 0x00000000, 0x00000000, 0x00000000, 0x00000000, 0x00000000,
    0x00000000, 0x00000000, 0x00000000, 0x00000000, 0x00000000, 0x00000000, 0x00000000, 0x00000000,
};

__global__ __launch_bounds__(512, 2) void fused_kernel(const u16* x, unsigned char* ws, const u16* wr, const u16* wi, const u16* w1,
                                                       const u16* w2, const u16* b1, const u16* b2, u16* out) {
  asm volatile(
      "s_mov_b32 s16, s2\n"
      "s_load_dwordx2 s[2:3], s[0:1], 0x0\n"
      "s_load_dwordx8 s[4:11], s[0:1], 0x8\n"
      "s_load_dwordx4 s[12:15], s[0:1], 0x28\n"
      "v_lshrrev_b32 v6, 6, v0\n"
      "s_and_b32 s41, s16, 0xf8\n"
      "v_add_u32 v5, s41, v6\n"
      "s_getpc_b64 s[46:47]\n"
      "v_min_u32 v5, 159, v5\n"
      "s_and_b32 s36, s16, 7\n"
      "v_lshlrev_b32 v5, 7, v5\n"
      "s_lshr_b32 s41, s16, 4\n"
      "global_load_dword v240, v5, s[46:47]\n"
      "v_lshlrev_b32 v2, 4, v0\n"
      "s_lshl4_add_u32 s38, s36, s41\n"
      "v_and_b32 v1, 0x3f0, v2\n"
      "s_lshl_b32 s41, s38, 11\n"
      "v_lshl_add_u32 v14, v6, 18, v1\n"
      "s_lshr_b32 s37, s16, 3\n"
      "v_readfirstlane_b32 s39, v6\n"
      "v_add_u32 v14, s41, v14\n"
      "s_mov_b32 s22, 12648448\n"
      "v_and_b32 v3, 15, v0\n"
      "s_mov_b32 s23, 0x20000\n"
      "v_bfe_u32 v4, v0, 4, 2\n"
      "s_lshl_b32 s58, s38, 14\n"
      "v_mov_b32 v108, -1\n"
      "v_mov_b32 v109, -1\n"
      "v_mov_b32 v110, -1\n"
      "v_mov_b32 v111, -1\n"
      "s_mov_b32 s44, 1000000\n"
      "s_mov_b32 s57, 8454144\n"
      "s_bitcmp1_b32 s16, 3\n"
      "s_cbranch_scc1 Lrole2%=\n"
      "s_getpc_b64 s[78:79]\n"
      "s_add_u32 s78, s78, fk_tab@rel32@lo+4\n"
      "s_addc_u32 s79, s79, fk_tab@rel32@hi+12\n"
      "s_lshl_b32 s41, s39, 10\n"
      "s_add_u32 m0, s41, 35840\n"
      "s_nop 0\n"
      "global_load_lds_dwordx4 v2, s[78:79]\n"
      "s_add_u32 s75, s58, 2162688\n"
      "s_add_u32 s76, s58, 2170880\n"
      "v_and_b32 v92, 7, v3\n"
      "v_and_b32 v93, 1, v4\n"
      "v_lshrrev_b32 v94, 3, v3\n"
      "v_lshrrev_b32 v95, 1, v4\n"
      "v_cmp_eq_u32 vcc, v94, v95\n"
      "v_lshl_add_u32 v94, s38, 1, v94\n"
      "v_lshl_add_u32 v93, v93, 10, v94\n"
      "v_cndmask_b32_e64 v16, 0, -1, vcc\n"
      "s_waitcnt lgkmcnt(0)\n"
      "global_load_dwordx4 v[100:103], v14, s[2:3]\n"
      "global_load_dwordx4 v[104:107], v14, s[2:3] offset:1024\n"
      "s_mov_b64 s[20:21], s[4:5]\n"
      "s_load_dword s40, s[4:5], 0x0\n"
      "s_load_dwordx4 s[32:35], s[0:1], 0x38\n"
      "v_cvt_f32_u32 v92, v92\n"
      "v_cvt_f32_u32 v93, v93\n"
      "v_mul_f32 v15, 0x3e000000, v92\n"
      "v_mul_f32 v93, 0x3b800000, v93\n"
      "v_mul_f32 v96, v15, v93\n"
      "v_add_f32 v97, v96, v15\n"
      "v_add_f32 v98, v97, v15\n"
      "v_add_f32 v99, v98, v15\n"
      "v_cos_f32 v92, v96\n"
      "v_sin_f32 v88, v96\n"
      "v_cos_f32 v93, v97\n"
      "v_sin_f32 v89, v97\n"
      "v_cos_f32 v94, v98\n"
      "v_sin_f32 v90, v98\n"
      "v_cos_f32 v95, v99\n"
      "v_sin_f32 v91, v99\n"
      "v_and_b32 v92, v92, v16\n"
      "v_and_b32 v88, v88, v16\n"
      "v_and_b32 v93, v93, v16\n"
      "v_and_b32 v89, v89, v16\n"
      "v_and_b32 v94, v94, v16\n"
      "v_and_b32 v90, v90, v16\n"
      "v_and_b32 v95, v95, v16\n"
      "v_and_b32 v91, v91, v16\n"
      "v_cvt_pk_bf16_f32 v232, v92, v88\n"
      "v_cvt_pk_bf16_f32 v236, -v88, v92\n"
      "v_cvt_pk_bf16_f32 v233, v93, v89\n"
      "v_cvt_pk_bf16_f32 v237, -v89, v93\n"
      "v_cvt_pk_bf16_f32 v234, v94, v90\n"
      "v_cvt_pk_bf16_f32 v238, -v90, v94\n"
      "v_cvt_pk_bf16_f32 v235, v95, v91\n"
      "v_cvt_pk_bf16_f32 v239, -v91, v95\n"
      "s_lshl_b32 s41, s39, 11\n"
      "v_add_u32 v15, s41, v1\n"
      "v_lshrrev_b32 v16, 1, v1\n"
      "v_add_u32 v16, s41, v16\n"
      "v_cmp_eq_u32_e64 s[80:81], 0, v3\n"
      "v_cmp_gt_u32 vcc, 2, v4\n"
      "v_lshl_add_u32 v5, v4, 7, v3\n"
      "v_sub_u32 v6, 0x200, v5\n"
      "v_cndmask_b32 v5, v6, v5, vcc\n"
      "v_lshl_add_u32 v6, v4, 6, 16\n"
      "v_cndmask_b32_e64 v5, v5, v6, s[80:81]\n"
      "s_lshr_b32 s42, s39, 2\n"
      "s_lshl4_add_u32 s42, s39, s42\n"
      "s_lshl_b32 s42, s42, 6\n"
      "s_sub_u32 s42, s42, 4\n"
      "v_lshl_add_u32 v20, v5, 2, s42\n"
      "v_mov_b32 v6, 0x80\n"
      "v_mov_b32 v5, 0xffffff80\n"
      "v_cndmask_b32 v5, v5, v6, vcc\n"
      "v_cndmask_b32_e64 v5, v5, 64, s[80:81]\n"
      "v_add_u32 v21, v20, v5\n"
      "v_add_u32 v22, v21, v5\n"
      "v_add_u32 v23, v22, v5\n"
      "v_mov_b32 v5, 0xc3800000\n"
      "v_cndmask_b32_e64 v19, 0, v5, s[80:81]\n"
      "v_mov_b32 v24, 0x38000000\n"
      "v_mov_b32 v96, 0\n"
      "v_mov_b32 v97, 0x38a7c5ac\n"
      "v_mov_b32 v98, 0\n"
      "v_mov_b32 v99, 0x38a7c5ac\n"
      "s_lshl_b32 s41, s39, 7\n"
      "v_mul_u32_u24 v17, 4160, v4\n"
      "v_lshl_add_u32 v5, v3, 2, s41\n"
      "v_add_u32 v17, v17, v5\n"
      "v_add_u32 v28, 64, v17\n"
      "v_and_b32 v5, 7, v3\n"
      "v_and_b32 v6, 8, v3\n"
      "v_lshlrev_b32 v6, 3, v6\n"
      "v_lshl_add_u32 v18, v5, 18, v6\n"
      "v_lshl_add_u32 v18, v4, 4, v18\n"
      "s_and_b32 s41, s38, 0x60\n"
      "s_and_b32 s42, s38, 7\n"
      "s_lshl2_add_u32 s41, s42, s41\n"
      "s_bfe_u32 s42, s38, 0x20003\n"
      "s_add_u32 s41, s41, s42\n"
      "s_lshl_b32 s41, s41, 7\n"
      "s_lshl_b32 s42, s39, 15\n"
      "s_add_u32 s41, s41, s42\n"
      "s_add_u32 s82, s41, 65536\n"
      "s_add_u32 s83, s41, 81920\n"
      "v_mov_b32 v25, 0xfff8fff8\n"
      "s_mul_i32 s42, s36, 11\n"
      "s_add_u32 s43, s42, 14\n"
      "s_lshr_b32 s42, s42, 2\n"
      "s_lshr_b32 s43, s43, 2\n"
      "s_lshl_b32 s43, s43, 7\n"
      "s_min_u32 s43, s43, 2752\n"
      "s_sub_u32 s43, s43, 4\n"
      "v_and_b32 v5, 7, v0\n"
      "v_add_lshl_u32 v5, v5, s42, 7\n"
      "v_min_u32 v5, s43, v5\n"
      "s_lshr_b32 s41, s37, 1\n"
      "s_and_b32 s42, s39, 3\n"
      "s_lshl2_add_u32 s41, s41, s42\n"
      "s_lshr_b32 s42, s39, 2\n"
      "s_lshl2_add_u32 s41, s41, s42\n"
      "v_bfe_u32 v9, v0, 3, 3\n"
      "v_lshl_add_u32 v9, s41, 3, v9\n"
      "v_mul_u32_u24 v9, 2752, v9\n"
      "v_add_u32 v9, v9, v5\n"
      "s_waitcnt vmcnt(2)\n"
      "s_barrier\n"
      "ds_read_b128 v[208:211], v1 offset:35840\n"
      "ds_read_b128 v[212:215], v1 offset:36864\n"
      "ds_read_b128 v[92:95], v1 offset:37888\n"
      "ds_read_b128 v[88:91], v1 offset:38912\n"
      "ds_read_b128 v[216:219], v1 offset:39936\n"
      "ds_read_b128 v[220:223], v1 offset:40960\n"
      "ds_read_b128 v[224:227], v1 offset:41984\n"
      "ds_read_b128 v[228:231], v1 offset:43008\n"
      "buffer_store_dwordx4 v[108:111], v2, s[20:23], s75 offen sc1\n"
      "buffer_store_dwordx4 v[108:111], v2, s[20:23], s76 offen sc1\n"
      "s_waitcnt lgkmcnt(0)\n"
      "s_add_u32 s40, s40, 1\n"
      "s_and_b32 s45, s40, 7\n"
      "s_bfe_u32 s41, s40, 0x30003\n"
      "s_lshl_b32 s41, s41, 16\n"
      "s_or_b32 s45, s45, s41\n"
      "s_lshr_b32 s41, s37, 1\n"
      "s_xor_b32 s41, s41, 6\n"
      "s_or_b32 s77, s41, s39\n"
      "v_and_b32 v26, 7, v0\n"
      "s_lshl_b32 s41, s36, 7\n"
      "v_lshl_add_u32 v26, v26, 10, s41\n"
      "v_mov_b32 v27, s40\n"
      "s_waitcnt vmcnt(3)\n"
      "ds_write_b128 v15, v[100:103] offset:0\n"
      "ds_read_b64_tr_b16 v[48:49], v16 offset:0\n"
      "ds_read_b64_tr_b16 v[50:51], v16 offset:512\n"
      "s_waitcnt vmcnt(2)\n"
      "ds_write_b128 v15, v[104:107] offset:1024\n"
      "ds_read_b64_tr_b16 v[52:53], v16 offset:1024\n"
      "ds_read_b64_tr_b16 v[54:55], v16 offset:1536\n"
      "v_dot2c_f32_bf16 v96, 0x39803980, v100\n"
      "v_dot2c_f32_bf16 v97, v100, v100\n"
      "v_dot2c_f32_bf16 v96, 0x39803980, v101\n"
      "v_dot2c_f32_bf16 v97, v101, v101\n"
      "v_dot2c_f32_bf16 v96, 0x39803980, v102\n"
      "v_dot2c_f32_bf16 v97, v102, v102\n"
      "v_dot2c_f32_bf16 v96, 0x39803980, v103\n"
      "v_dot2c_f32_bf16 v97, v103, v103\n"
      "v_dot2c_f32_bf16 v98, 0x39803980, v104\n"
      "v_dot2c_f32_bf16 v99, v104, v104\n"
      "v_dot2c_f32_bf16 v98, 0x39803980, v105\n"
      "v_dot2c_f32_bf16 v99, v105, v105\n"
      "v_dot2c_f32_bf16 v98, 0x39803980, v106\n"
      "v_dot2c_f32_bf16 v99, v106, v106\n"
      "v_dot2c_f32_bf16 v98, 0x39803980, v107\n"
      "v_dot2c_f32_bf16 v99, v107, v107\n"
      "global_load_dword v241, v9, s[10:11]\n"
      "s_waitcnt lgkmcnt(3)\n"
      "v_permlane32_swap_b32 v96, v97\n"
      "v_mfma_f32_16x16x32_bf16 v[72:75], v[48:51], v[208:211], 0\n"
      "v_permlane32_swap_b32 v98, v99\n"
      "v_add_f32 v96, v96, v97\n"
      "v_add_f32 v98, v98, v99\n"
      "v_mfma_f32_16x16x32_bf16 v[76:79], v[48:51], v[212:215], 0\n"
      "s_waitcnt lgkmcnt(0)\n"
      "v_permlane16_swap_b32 v96, v98\n"
      "v_add_f32 v96, v96, v98\n"
      "v_mfma_f32_16x16x32_bf16 v[56:59], v[52:55], v[208:211], 0\n"
      "v_mfma_f32_16x16x32_bf16 v[60:63], v[52:55], v[212:215], 0\n"
      "v_add_f32_dpp v96, v96, v96 row_ror:8 row_mask:0xf bank_mask:0xf\n"
      "s_nop 1\n"
      "s_nop 0\n"
      "v_add_f32_dpp v96, v96, v96 row_ror:4 row_mask:0xf bank_mask:0xf\n"
      "v_pk_mul_f32 v[80:81], v[72:73], v[88:89]\n"
      "v_pk_mul_f32 v[82:83], v[74:75], v[90:91]\n"
      "v_add_f32_dpp v96, v96, v96 row_ror:2 row_mask:0xf bank_mask:0xf\n"
      "v_pk_mul_f32 v[84:85], v[76:77], v[88:89]\n"
      "v_pk_mul_f32 v[86:87], v[78:79], v[90:91]\n"
      "v_add_f32_dpp v96, v96, v96 row_ror:1 row_mask:0xf bank_mask:0xf\n"
      "v_pk_fma_f32 v[76:77], v[76:77], v[92:93], v[80:81] neg_lo:[0,0,1] neg_hi:[0,0,1]\n"
      "v_readlane_b32 s84, v96, 0\n"
      "v_readlane_b32 s85, v96, 16\n"
      "v_readlane_b32 s86, v96, 32\n"
      "v_readlane_b32 s87, v96, 48\n"
      "v_pk_fma_f32 v[72:73], v[72:73], v[92:93], v[84:85]\n"
      "v_pk_fma_f32 v[78:79], v[78:79], v[94:95], v[82:83] neg_lo:[0,0,1] neg_hi:[0,0,1]\n"
      "v_pk_fma_f32 v[74:75], v[74:75], v[94:95], v[86:87]\n"
      "v_mul_f32_e64 v5, s84, s84\n"
      "v_mul_f32_e64 v6, s85, s85\n"
      "v_fma_f32 v5, s86, v24, -v5\n"
      "v_fma_f32 v6, s87, v24, -v6\n"
      "v_max_f32 v5, 0x3427c5ac, v5\n"
      "v_max_f32 v6, 0x3427c5ac, v6\n"
      "v_rsq_f32 v44, v5\n"
      "v_rsq_f32 v46, v6\n"
      "v_mul_f32 v5, s84, v19\n"
      "v_mul_f32 v6, s85, v19\n"
      "v_mul_f32 v45, v5, v44\n"
      "v_mul_f32 v47, v6, v46\n"
      "v_pk_mul_f32 v[76:77], v[76:77], v[44:45] op_sel_hi:[1,0]\n"
      "v_pk_fma_f32 v[72:73], v[72:73], v[44:45], v[44:45] op_sel:[0,0,1] op_sel_hi:[1,0,1]\n"
      "v_pk_mul_f32 v[78:79], v[78:79], v[44:45] op_sel_hi:[1,0]\n"
      "v_pk_fma_f32 v[74:75], v[74:75], v[44:45], v[44:45] op_sel:[0,0,1] op_sel_hi:[1,0,1]\n"
      "v_cvt_pk_bf16_f32 v48, v72, v76\n"
      "v_cvt_pk_bf16_f32 v49, v73, v77\n"
      "v_cvt_pk_bf16_f32 v50, v74, v78\n"
      "v_cvt_pk_bf16_f32 v51, v75, v79\n"
      "v_pk_mul_f32 v[64:65], v[56:57], v[88:89]\n"
      "v_pk_mul_f32 v[66:67], v[58:59], v[90:91]\n"
      "v_mfma_f32_16x16x32_bf16 v[72:75], v[216:219], v[48:51], 0\n"
      "v_pk_mul_f32 v[68:69], v[60:61], v[88:89]\n"
      "v_pk_mul_f32 v[70:71], v[62:63], v[90:91]\n"
      "v_mfma_f32_16x16x32_bf16 v[76:79], v[220:223], v[48:51], 0\n"
      "v_pk_fma_f32 v[60:61], v[60:61], v[92:93], v[64:65] neg_lo:[0,0,1] neg_hi:[0,0,1]\n"
      "v_pk_fma_f32 v[56:57], v[56:57], v[92:93], v[68:69]\n"
      "v_mfma_f32_16x16x32_bf16 v[80:83], v[224:227], v[48:51], 0\n"
      "v_pk_fma_f32 v[62:63], v[62:63], v[94:95], v[66:67] neg_lo:[0,0,1] neg_hi:[0,0,1]\n"
      "v_pk_fma_f32 v[58:59], v[58:59], v[94:95], v[70:71]\n"
      "v_mfma_f32_16x16x32_bf16 v[84:87], v[228:231], v[48:51], 0\n"
      "v_pk_mul_f32 v[60:61], v[60:61], v[46:47] op_sel_hi:[1,0]\n"
      "v_pk_fma_f32 v[56:57], v[56:57], v[46:47], v[46:47] op_sel:[0,0,1] op_sel_hi:[1,0,1]\n"
      "v_pk_mul_f32 v[62:63], v[62:63], v[46:47] op_sel_hi:[1,0]\n"
      "v_pk_fma_f32 v[58:59], v[58:59], v[46:47], v[46:47] op_sel:[0,0,1] op_sel_hi:[1,0,1]\n"
      "v_cvt_pk_bf16_f32 v52, v56, v60\n"
      "v_cvt_pk_bf16_f32 v53, v57, v61\n"
      "v_cvt_pk_bf16_f32 v54, v58, v62\n"
      "v_cvt_pk_bf16_f32 v55, v59, v63\n"
      "v_cvt_pk_bf16_f32 v72, v72, v76\n"
      "v_cvt_pk_bf16_f32 v73, v73, v77\n"
      "v_mfma_f32_16x16x32_bf16 v[56:59], v[216:219], v[52:55], 0\n"
      "v_cvt_pk_bf16_f32 v74, v74, v78\n"
      "v_cvt_pk_bf16_f32 v75, v75, v79\n"
      "v_mfma_f32_16x16x32_bf16 v[60:63], v[220:223], v[52:55], 0\n"
      "v_cvt_pk_bf16_f32 v80, v80, v84\n"
      "v_cvt_pk_bf16_f32 v81, v81, v85\n"
      "v_mfma_f32_16x16x32_bf16 v[64:67], v[224:227], v[52:55], 0\n"
      "v_cvt_pk_bf16_f32 v82, v82, v86\n"
      "v_cvt_pk_bf16_f32 v83, v83, v87\n"
      "v_mfma_f32_16x16x32_bf16 v[68:71], v[228:231], v[52:55], 0\n"
      "v_cndmask_b32_e64 v72, v72, v80, s[80:81]\n"
      "ds_write_b32 v20, v72 offset:16896\n"
      "v_cndmask_b32_e64 v73, v73, v81, s[80:81]\n"
      "ds_write_b32 v21, v73 offset:16896\n"
      "v_cndmask_b32_e64 v74, v74, v82, s[80:81]\n"
      "ds_write_b32 v22, v74 offset:16896\n"
      "v_cndmask_b32_e64 v75, v75, v83, s[80:81]\n"
      "ds_write_b32 v23, v75 offset:16896\n"
      "v_cvt_pk_bf16_f32 v56, v56, v60\n"
      "v_cvt_pk_bf16_f32 v57, v57, v61\n"
      "v_cvt_pk_bf16_f32 v58, v58, v62\n"
      "v_cvt_pk_bf16_f32 v59, v59, v63\n"
      "v_cvt_pk_bf16_f32 v64, v64, v68\n"
      "v_cvt_pk_bf16_f32 v65, v65, v69\n"
      "v_cvt_pk_bf16_f32 v66, v66, v70\n"
      "v_cvt_pk_bf16_f32 v67, v67, v71\n"
      "v_cndmask_b32_e64 v56, v56, v64, s[80:81]\n"
      "ds_write_b32 v20, v56 offset:25216\n"
      "v_cndmask_b32_e64 v57, v57, v65, s[80:81]\n"
      "ds_write_b32 v21, v57 offset:25216\n"
      "v_cndmask_b32_e64 v58, v58, v66, s[80:81]\n"
      "ds_write_b32 v22, v58 offset:25216\n"
      "v_cndmask_b32_e64 v59, v59, v67, s[80:81]\n"
      "ds_write_b32 v23, v59 offset:25216\n"
      "s_waitcnt vmcnt(1)\n"
      "s_waitcnt lgkmcnt(0)\n"
      "s_barrier\n"
      "ds_read2st64_b32 v[48:49], v17 offset0:66 offset1:70\n"
      "ds_read2st64_b32 v[50:51], v17 offset0:74 offset1:78\n"
      "ds_read2st64_b32 v[52:53], v28 offset0:66 offset1:70\n"
      "ds_read2st64_b32 v[54:55], v28 offset0:74 offset1:78\n"
      "s_waitcnt lgkmcnt(2)\n"
      "v_mfma_f32_16x16x32_bf16 v[72:75], v[48:51], v[232:235], 0\n"
      "v_mfma_f32_16x16x32_bf16 v[76:79], v[48:51], v[236:239], 0\n"
      "s_waitcnt lgkmcnt(0)\n"
      "v_mfma_f32_16x16x32_bf16 v[56:59], v[52:55], v[232:235], 0\n"
      "v_mfma_f32_16x16x32_bf16 v[60:63], v[52:55], v[236:239], 0\n"
      "s_nop 4\n"
      "v_cvt_pkrtz_f16_f32 v72, v72, v76\n"
      "v_cvt_pkrtz_f16_f32 v73, v73, v77\n"
      "v_cvt_pkrtz_f16_f32 v74, v74, v78\n"
      "v_cvt_pkrtz_f16_f32 v75, v75, v79\n"
      "v_bfi_b32 v72, v25, v72, s45\n"
      "v_bfi_b32 v73, v25, v73, s45\n"
      "v_bfi_b32 v74, v25, v74, s45\n"
      "v_bfi_b32 v75, v25, v75, s45\n"
      "buffer_store_dwordx4 v[72:75], v18, s[20:23], s82 offen sc1\n"
      "v_cvt_pkrtz_f16_f32 v56, v56, v60\n"
      "v_cvt_pkrtz_f16_f32 v57, v57, v61\n"
      "v_cvt_pkrtz_f16_f32 v58, v58, v62\n"
      "v_cvt_pkrtz_f16_f32 v59, v59, v63\n"
      "v_bfi_b32 v56, v25, v56, s45\n"
      "v_bfi_b32 v57, v25, v57, s45\n"
      "v_bfi_b32 v58, v25, v58, s45\n"
      "v_bfi_b32 v59, v25, v59, s45\n"
      "buffer_store_dwordx4 v[56:59], v18, s[20:23], s83 offen sc1\n"
      "s_cmp_eq_u32 s77, 0\n"
      "s_cbranch_scc0 Lfb0%=\n"
      "s_mov_b32 s46, 4096\n"
      "buffer_store_dword v27, v26, s[20:23], s46 offen sc1\n"
      "Lfb0%=:\n"
      "v_add_u32 v9, 44032, v9\n"
      "s_barrier\n"
      "global_load_dword v241, v9, s[10:11]\n"
      "s_and_b32 s41, s36, 3\n"
      "s_lshl_b32 s41, s41, 3\n"
      "s_lshr_b32 s42, 0x5d3d1d00, s41\n"
      "s_lshr_b32 s43, 0x1c20201d, s41\n"
      "s_and_b32 s42, s42, 0xff\n"
      "s_and_b32 s43, s43, 0xff\n"
      "s_cmp_lt_u32 s37, s43\n"
      "s_cselect_b32 s50, -1, 0\n"
      "s_cmp_lt_u32 s36, 4\n"
      "s_cselect_b32 s41, 0, 121\n"
      "s_add_u32 s42, s42, s41\n"
      "s_add_u32 s42, s42, s37\n"
      "s_mul_i32 s48, s42, 373\n"
      "s_lshr_b32 s48, s48, 12\n"
      "s_mul_i32 s41, s48, 11\n"
      "s_sub_u32 s49, s42, s41\n"
      "s_mul_i32 s59, s49, 0\n"
      "v_lshlrev_b32 v7, 5, v4\n"
      "s_mul_i32 s41, s39, 88064\n"
      "v_xor_b32 v8, v1, v7\n"
      "s_lshl_b32 s42, s48, 7\n"
      "v_and_b32 v8, 0x70, v8\n"
      "s_add_u32 s41, s41, s42\n"
      "v_bfe_u32 v7, v0, 3, 2\n"
      "s_lshl_b32 s51, s39, 12\n"
      "v_lshrrev_b32 v9, 1, v4\n"
      "s_add_u32 s51, s51, 65536\n"
      "v_lshl_add_u32 v7, v7, 8, v9\n"
      "s_and_b32 s26, s50, 5636096\n"
      "v_mul_u32_u24 v7, 2752, v7\n"
      "s_mov_b32 s27, 0x20000\n"
      "v_add3_u32 v7, v8, v7, s41\n"
      "s_cmp_eq_u32 s48, 21\n"
      "s_cselect_b32 s41, 64, 0\n"
      "v_and_b32 v9, s41, v8\n"
      "v_lshl_add_u32 v7, v9, 24, v7\n"
      "s_mov_b32 s60, 2817024\n"
      "s_mov_b32 s61, 3456\n"
      "s_mov_b32 s62, 2820480\n"
      "s_mov_b64 s[24:25], s[10:11]\n"
      "s_barrier\n"
      "s_lshl_b32 s41, s39, 10\n"
      "s_add_u32 m0, s41, 44032\n"
      "v_add_u32 v5, 0x2000, v2\n"
      "global_load_lds_dwordx4 v5, s[78:79]\n"
      "s_add_u32 m0, s51, 0\n"
      "s_add_u32 s52, s59, 0\n"
      "s_and_b32 s52, s52, 7\n"
      "s_mul_i32 s52, s52, 11008\n"
      "v_add_u32 v9, s52, v7\n"
      "buffer_load_dwordx4 v9, s[24:27], 0 offen offset:0 lds\n"
      "buffer_load_dwordx4 v9, s[24:27], s60 offen offset:1024 lds\n"
      "buffer_load_dwordx4 v9, s[24:27], s61 offen offset:2048 lds\n"
      "buffer_load_dwordx4 v9, s[24:27], s62 offen offset:3072 lds\n"
      "s_mov_b64 s[28:29], s[12:13]\n"
      "s_mov_b32 s30, 5636096\n"
      "s_mov_b32 s31, 0x20000\n"
      "s_mov_b32 s63, 64512\n"
      "s_mov_b32 s64, 30720\n"
      "s_mov_b32 s65, 95232\n"
      "s_and_b32 s53, s37, 7\n"
      "s_lshr_b32 s54, s38, 2\n"
      "s_mul_i32 s41, s39, 6\n"
      "s_mul_i32 s42, s39, 5\n"
      "s_add_u32 s42, s42, 3\n"
      "s_cmp_lt_u32 s39, 3\n"
      "s_cselect_b32 s41, s41, s42\n"
      "s_cselect_b32 s42, 6, 5\n"
      "s_sub_u32 s43, s53, s42\n"
      "s_cselect_b32 s43, s53, s43\n"
      "s_add_u32 s69, s41, s43\n"
      "s_add_u32 s43, s43, 1\n"
      "s_cmp_eq_u32 s43, s42\n"
      "s_cselect_b32 s43, 0, s43\n"
      "s_add_u32 s70, s41, s43\n"
      "s_add_u32 s43, s43, 1\n"
      "s_cmp_eq_u32 s43, s42\n"
      "s_cselect_b32 s43, 0, s43\n"
      "s_add_u32 s71, s41, s43\n"
      "s_add_u32 s43, s43, 1\n"
      "s_cmp_eq_u32 s43, s42\n"
      "s_cselect_b32 s43, 0, s43\n"
      "s_add_u32 s72, s41, s43\n"
      "s_add_u32 s43, s43, 1\n"
      "s_cmp_eq_u32 s43, s42\n"
      "s_cselect_b32 s43, 0, s43\n"
      "s_add_u32 s73, s41, s43\n"
      "s_add_u32 s43, s43, 1\n"
      "s_cmp_eq_u32 s43, s42\n"
      "s_cselect_b32 s43, 0, s43\n"
      "s_add_u32 s74, s41, s43\n"
      "s_cmp_lt_u32 s39, 3\n"
      "s_cselect_b32 s74, s74, 0x3000\n"
      "v_bfe_u32 v5, v0, 3, 3\n"
      "s_lshl_b32 s41, s54, 7\n"
      "v_lshlrev_b32 v5, 12, v5\n"
      "v_add3_u32 v8, v8, v5, s41\n"
      "v_bfe_u32 v5, v0, 2, 3\n"
      "v_and_b32 v6, 3, v0\n"
      "v_lshl_add_u32 v6, v6, 3, s51\n"
      "v_lshl_add_u32 v6, v5, 7, v6\n"
      "v_lshrrev_b32 v10, 1, v4\n"
      "v_lshl_add_u32 v6, v10, 11, v6\n"
      "v_lshrrev_b32 v5, 1, v5\n"
      "v_lshl_add_u32 v10, v5, 5, v6\n"
      "v_xor_b32 v11, 32, v10\n"
      "v_xor_b32 v12, 64, v10\n"
      "v_xor_b32 v13, 96, v10\n"
      "s_mul_i32 s41, s49, 135168\n"
      "s_mul_i32 s42, s48, 2048\n"
      "s_add_u32 s41, s41, s42\n"
      "s_andn2_b32 s42, 536870912, s50\n"
      "s_add_u32 s41, s41, s42\n"
      "s_add_u32 s66, s41, 10551296\n"
      "s_add_u32 s67, s41, 10596352\n"
      "s_add_u32 s68, s41, 10641408\n"
      "s_cmp_eq_u32 s49, 10\n"
      "s_cselect_b32 s55, 1073741824, 0\n"
      "s_add_u32 s68, s68, s55\n"
      "s_cmp_lt_u32 s39, 6\n"
      "s_cselect_b32 s56, s68, 1073741824\n"
      "s_cmp_lt_u32 s39, 4\n"
      "s_cselect_b32 s56, s67, s56\n"
      "s_cmp_lt_u32 s39, 2\n"
      "s_cselect_b32 s56, s66, s56\n"
      "s_add_u32 m0, s51, 32768\n"
      "s_add_u32 s52, s59, 1\n"
      "s_and_b32 s52, s52, 7\n"
      "s_mul_i32 s52, s52, 11008\n"
      "v_add_u32 v9, s52, v7\n"
      "buffer_load_dwordx4 v9, s[24:27], 0 offen offset:0 lds\n"
      "buffer_load_dwordx4 v9, s[24:27], s60 offen offset:1024 lds\n"
      "buffer_load_dwordx4 v9, s[24:27], s61 offen offset:2048 lds\n"
      "buffer_load_dwordx4 v9, s[24:27], s62 offen offset:3072 lds\n"
      "v_lshlrev_b32 v5, 3, v4\n"
      "v_lshl_add_u32 v5, v3, 5, v5\n"
      "s_lshl_b32 s42, s39, 11\n"
      "v_add_u32 v6, s42, v5\n"
      "ds_read2st64_b64 v[72:75], v6 offset0:0 offset1:1\n"
      "ds_read2st64_b64 v[56:59], v6 offset0:2 offset1:3\n"
      "s_mul_i32 s41, s39, 1088\n"
      "v_add_u32 v19, s41, v5\n"
      "v_sub_u32 v14, v14, v1\n"
      "v_lshl_add_u32 v14, v3, 5, v14\n"
      "v_and_b32 v5, 1, v4\n"
      "v_lshl_add_u32 v14, v5, 9, v14\n"
      "v_mul_u32_u24 v17, 512, v5\n"
      "v_lshrrev_b32 v5, 1, v4\n"
      "v_lshl_add_u32 v14, v5, 4, v14\n"
      "v_lshl_add_u32 v17, v5, 6, v17\n"
      "v_lshl_add_u32 v17, v3, 2, v17\n"
      "v_add_u32 v17, s42, v17\n"
      "v_mul_u32_u24 v18, 1040, v3\n"
      "v_lshl_add_u32 v18, v4, 4, v18\n"
      "s_lshl_b32 s41, s39, 7\n"
      "v_add_u32 v18, s41, v18\n"
      "v_lshrrev_b32 v5, 2, v3\n"
      "v_mul_u32_u24 v24, 1088, v5\n"
      "v_and_b32 v5, 3, v3\n"
      "v_lshl_add_u32 v24, v5, 3, v24\n"
      "v_lshl_add_u32 v24, v4, 5, v24\n"
      "v_add_u32 v24, s41, v24\n"
      "v_and_b32 v25, 0x1e30, v2\n"
      "v_bfe_u32 v5, v0, 2, 1\n"
      "v_lshl_add_u32 v25, v5, 8, v25\n"
      "v_bfe_u32 v5, v0, 3, 2\n"
      "v_lshl_add_u32 v25, v5, 6, v25\n"
      "s_and_b32 s41, s39, 3\n"
      "s_lshl_b32 s41, s41, 4\n"
      "v_add_u32 v20, s41, v20\n"
      "v_add_u32 v21, s41, v21\n"
      "v_add_u32 v22, s41, v22\n"
      "v_add_u32 v23, s41, v23\n"
      "v_cmp_eq_u32_e64 s[80:81], 0, v4\n"
      "s_mul_i32 s83, s38, 16384\n"
      "s_add_u32 s82, s83, 4259840\n"
      "s_add_u32 s83, s83, 4268032\n"
      "s_waitcnt lgkmcnt(0)\n"
      "s_waitcnt vmcnt(9)\n"
      "v_lshlrev_b32 v240, 16, v72\n"
      "v_and_b32 v241, 0xffff0000, v72\n"
      "v_lshlrev_b32 v242, 16, v73\n"
      "v_and_b32 v243, 0xffff0000, v73\n"
      "v_lshlrev_b32 v244, 16, v74\n"
      "v_and_b32 v245, 0xffff0000, v74\n"
      "v_lshlrev_b32 v246, 16, v75\n"
      "v_and_b32 v247, 0xffff0000, v75\n"
      "v_lshlrev_b32 v248, 16, v56\n"
      "v_and_b32 v249, 0xffff0000, v56\n"
      "v_lshlrev_b32 v250, 16, v57\n"
      "v_and_b32 v251, 0xffff0000, v57\n"
      "v_lshlrev_b32 v252, 16, v58\n"
      "v_and_b32 v253, 0xffff0000, v58\n"
      "v_lshlrev_b32 v254, 16, v59\n"
      "v_and_b32 v255, 0xffff0000, v59\n"
      "v_and_b32 v76, 7, v3\n"
      "v_and_b32 v77, 1, v4\n"
      "v_lshrrev_b32 v78, 3, v3\n"
      "v_lshrrev_b32 v79, 1, v4\n"
      "v_cmp_eq_u32 vcc, v78, v79\n"
      "v_lshl_add_u32 v78, s38, 1, v78\n"
      "v_lshl_add_u32 v76, v76, 8, v78\n"
      "v_cndmask_b32_e64 v85, 0, -1, vcc\n"
      "v_lshlrev_b32 v77, 2, v77\n"
      "v_cvt_f32_u32 v76, v76\n"
      "v_cvt_f32_u32 v77, v77\n"
      "v_mul_f32 v84, 0x3a000000, v76\n"
      "v_mul_f32 v72, v84, v77\n"
      "v_add_f32 v73, v72, v84\n"
      "v_add_f32 v74, v73, v84\n"
      "v_add_f32 v75, v74, v84\n"
      "v_cos_f32 v76, v72\n"
      "v_sin_f32 v80, v72\n"
      "v_cos_f32 v77, v73\n"
      "v_sin_f32 v81, v73\n"
      "v_cos_f32 v78, v74\n"
      "v_sin_f32 v82, v74\n"
      "v_cos_f32 v79, v75\n"
      "v_sin_f32 v83, v75\n"
      "v_and_b32 v76, v76, v85\n"
      "v_and_b32 v80, v80, v85\n"
      "v_and_b32 v77, v77, v85\n"
      "v_and_b32 v81, v81, v85\n"
      "v_and_b32 v78, v78, v85\n"
      "v_and_b32 v82, v82, v85\n"
      "v_and_b32 v79, v79, v85\n"
      "v_and_b32 v83, v83, v85\n"
      "v_cvt_pk_bf16_f32 v108, v76, -v80\n"
      "v_cvt_pk_bf16_f32 v104, v80, v76\n"
      "v_cvt_pk_bf16_f32 v109, v77, -v81\n"
      "v_cvt_pk_bf16_f32 v105, v81, v77\n"
      "v_cvt_pk_bf16_f32 v110, v78, -v82\n"
      "v_cvt_pk_bf16_f32 v106, v82, v78\n"
      "v_cvt_pk_bf16_f32 v111, v79, -v83\n"
      "v_cvt_pk_bf16_f32 v107, v83, v79\n"
      "v_mov_b32 v96, 0\n"
      "v_mov_b32 v44, -1\n"
      "v_mov_b32 v97, 0\n"
      "v_mov_b32 v45, -1\n"
      "v_mov_b32 v98, 0\n"
      "v_mov_b32 v46, -1\n"
      "v_mov_b32 v99, 0\n"
      "v_mov_b32 v47, -1\n"
      "v_mov_b32 v30, 0x3b000000\n"
      "v_mov_b32 v31, 0x3f800054\n"
      "s_add_u32 m0, s51, 65536\n"
      "s_add_u32 s52, s59, 2\n"
      "s_and_b32 s52, s52, 7\n"
      "s_mul_i32 s52, s52, 11008\n"
      "v_add_u32 v9, s52, v7\n"
      "buffer_load_dwordx4 v9, s[24:27], 0 offen offset:0 lds\n"
      "buffer_load_dwordx4 v9, s[24:27], s60 offen offset:1024 lds\n"
      "buffer_load_dwordx4 v9, s[24:27], s61 offen offset:2048 lds\n"
      "buffer_load_dwordx4 v9, s[24:27], s62 offen offset:3072 lds\n"
      "s_waitcnt vmcnt(8)\n"
      "ds_read_b64_tr_b16 v[128:129], v10 offset:0\n"
      "ds_read_b64_tr_b16 v[130:131], v10 offset:1024\n"
      "ds_read_b64_tr_b16 v[132:133], v11 offset:0\n"
      "ds_read_b64_tr_b16 v[134:135], v11 offset:1024\n"
      "ds_read_b64_tr_b16 v[136:137], v12 offset:0\n"
      "ds_read_b64_tr_b16 v[138:139], v12 offset:1024\n"
      "ds_read_b64_tr_b16 v[140:141], v13 offset:0\n"
      "ds_read_b64_tr_b16 v[142:143], v13 offset:1024\n"
      "s_waitcnt lgkmcnt(0)\n"
      "s_add_u32 m0, s51, 0\n"
      "s_add_u32 s52, s59, 3\n"
      "s_and_b32 s52, s52, 7\n"
      "s_mul_i32 s52, s52, 11008\n"
      "v_add_u32 v9, s52, v7\n"
      "buffer_load_dwordx4 v9, s[24:27], 0 offen offset:0 lds\n"
      "buffer_load_dwordx4 v9, s[24:27], s60 offen offset:1024 lds\n"
      "buffer_load_dwordx4 v9, s[24:27], s61 offen offset:2048 lds\n"
      "buffer_load_dwordx4 v9, s[24:27], s62 offen offset:3072 lds\n"
      "s_waitcnt vmcnt(8)\n"
      "ds_read_b64_tr_b16 v[144:145], v10 offset:32768\n"
      "ds_read_b64_tr_b16 v[146:147], v10 offset:33792\n"
      "ds_read_b64_tr_b16 v[148:149], v11 offset:32768\n"
      "ds_read_b64_tr_b16 v[150:151], v11 offset:33792\n"
      "ds_read_b64_tr_b16 v[152:153], v12 offset:32768\n"
      "ds_read_b64_tr_b16 v[154:155], v12 offset:33792\n"
      "ds_read_b64_tr_b16 v[156:157], v13 offset:32768\n"
      "ds_read_b64_tr_b16 v[158:159], v13 offset:33792\n"
      "s_waitcnt lgkmcnt(0)\n"
      "s_add_u32 m0, s51, 32768\n"
      "s_add_u32 s52, s59, 4\n"
      "s_and_b32 s52, s52, 7\n"
      "s_mul_i32 s52, s52, 11008\n"
      "v_add_u32 v9, s52, v7\n"
      "buffer_load_dwordx4 v9, s[24:27], 0 offen offset:0 lds\n"
      "buffer_load_dwordx4 v9, s[24:27], s60 offen offset:1024 lds\n"
      "buffer_load_dwordx4 v9, s[24:27], s61 offen offset:2048 lds\n"
      "buffer_load_dwordx4 v9, s[24:27], s62 offen offset:3072 lds\n"
      "s_waitcnt vmcnt(8)\n"
      "v_add_u32 v10, 0x8000, v10\n"
      "v_add_u32 v11, 0x8000, v11\n"
      "v_add_u32 v12, 0x8000, v12\n"
      "v_add_u32 v13, 0x8000, v13\n"
      "ds_read_b64_tr_b16 v[160:161], v10 offset:32768\n"
      "ds_read_b64_tr_b16 v[162:163], v10 offset:33792\n"
      "ds_read_b64_tr_b16 v[164:165], v11 offset:32768\n"
      "ds_read_b64_tr_b16 v[166:167], v11 offset:33792\n"
      "ds_read_b64_tr_b16 v[168:169], v12 offset:32768\n"
      "ds_read_b64_tr_b16 v[170:171], v12 offset:33792\n"
      "ds_read_b64_tr_b16 v[172:173], v13 offset:32768\n"
      "ds_read_b64_tr_b16 v[174:175], v13 offset:33792\n"
      "s_waitcnt lgkmcnt(0)\n"
      "s_add_u32 m0, s51, 65536\n"
      "s_add_u32 s52, s59, 5\n"
      "s_and_b32 s52, s52, 7\n"
      "s_mul_i32 s52, s52, 11008\n"
      "v_add_u32 v9, s52, v7\n"
      "buffer_load_dwordx4 v9, s[24:27], 0 offen offset:0 lds\n"
      "buffer_load_dwordx4 v9, s[24:27], s60 offen offset:1024 lds\n"
      "s_waitcnt vmcnt(6)\n"
      "v_add_u32 v10, 0xffff8000, v10\n"
      "v_add_u32 v11, 0xffff8000, v11\n"
      "v_add_u32 v12, 0xffff8000, v12\n"
      "v_add_u32 v13, 0xffff8000, v13\n"
      "ds_read_b64_tr_b16 v[176:177], v10 offset:0\n"
      "ds_read_b64_tr_b16 v[178:179], v10 offset:1024\n"
      "ds_read_b64_tr_b16 v[180:181], v11 offset:0\n"
      "ds_read_b64_tr_b16 v[182:183], v11 offset:1024\n"
      "ds_read_b64_tr_b16 v[184:185], v12 offset:0\n"
      "ds_read_b64_tr_b16 v[186:187], v12 offset:1024\n"
      "ds_read_b64_tr_b16 v[188:189], v13 offset:0\n"
      "ds_read_b64_tr_b16 v[190:191], v13 offset:1024\n"
      "s_waitcnt lgkmcnt(0)\n"
      "s_cmp_lg_u32 s39, 0\n"
      "s_cbranch_scc1 Lgd1%=\n"
      "v_and_b32 v5, 7, v0\n"
      "s_lshl_b32 s41, s36, 10\n"
      "v_lshlrev_b32 v5, 7, v5\n"
      "s_add_u32 s41, s41, 12288\n"
      "s_mov_b32 s42, 4000000\n"
      "Lgp1%=:\n"
      "buffer_load_dword v6, v5, s[20:23], s41 offen sc1\n"
      "s_sub_u32 s42, s42, 1\n"
      "s_cbranch_scc1 Lend%=\n"
      "s_waitcnt vmcnt(0)\n"
      "v_cmp_ne_u32 vcc, s40, v6\n"
      "s_cbranch_vccnz Lgp1%=\n"
      "Lgd1%=:\n"
      "s_barrier\n"
      "buffer_load_dwordx4 v9, s[24:27], s61 offen offset:2048 lds\n"
      "buffer_load_dword v48, v17, s[20:23], s75 offen offset:0 sc1\n"
      "buffer_load_dword v49, v17, s[20:23], s75 offen offset:128 sc1\n"
      "buffer_load_dword v50, v17, s[20:23], s75 offen offset:256 sc1\n"
      "buffer_load_dword v51, v17, s[20:23], s75 offen offset:384 sc1\n"
      "buffer_load_dword v52, v17, s[20:23], s75 offen offset:1024 sc1\n"
      "buffer_load_dword v53, v17, s[20:23], s75 offen offset:1152 sc1\n"
      "buffer_load_dword v54, v17, s[20:23], s75 offen offset:1280 sc1\n"
      "buffer_load_dword v55, v17, s[20:23], s75 offen offset:1408 sc1\n"
      "ds_read_b128 v[100:103], v1 offset:44032\n"
      "ds_read_b128 v[40:43], v1 offset:45056\n"
      "ds_read_b128 v[36:39], v1 offset:46080\n"
      "ds_read_b128 v[32:35], v1 offset:47104\n"
      "s_barrier\n"
      "v_and_b32 v6, 0x70, v2\n"
      "v_bfe_u32 v5, v0, 3, 1\n"
      "v_mul_u32_u24 v5, 128, v5\n"
      "v_add_u32 v6, v6, v5\n"
      "v_bfe_u32 v5, v0, 4, 2\n"
      "v_mul_u32_u24 v5, 256, v5\n"
      "v_add_u32 v6, v6, v5\n"
      "v_bfe_u32 v5, v0, 6, 1\n"
      "v_mul_u32_u24 v5, 1024, v5\n"
      "v_add_u32 v6, v6, v5\n"
      "buffer_store_dwordx4 v[44:47], v6, s[20:23], s56 offen sc1\n"
      "s_waitcnt vmcnt(12)\n"
      "ds_read_b64_tr_b16 v[192:193], v10 offset:32768\n"
      "ds_read_b64_tr_b16 v[194:195], v10 offset:33792\n"
      "ds_read_b64_tr_b16 v[196:197], v11 offset:32768\n"
      "ds_read_b64_tr_b16 v[198:199], v11 offset:33792\n"
      "ds_read_b64_tr_b16 v[200:201], v12 offset:32768\n"
      "ds_read_b64_tr_b16 v[202:203], v12 offset:33792\n"
      "ds_read_b64_tr_b16 v[204:205], v13 offset:32768\n"
      "ds_read_b64_tr_b16 v[206:207], v13 offset:33792\n"
      "buffer_load_dwordx4 v9, s[24:27], s62 offen offset:3072 lds\n"
      "s_add_u32 m0, s51, 0\n"
      "s_lshl_b32 s52, s69, 17\n"
      "v_add_u32 v9, s52, v8\n"
      "buffer_load_dwordx4 v9, s[28:31], 0 offen offset:0 lds\n"
      "s_waitcnt vmcnt(7)\n"
      "LcC%=:\n"
      "v_mfma_f32_16x16x32_bf16 v[72:75], v[48:51], v[108:111], 0\n"
      "v_mfma_f32_16x16x32_bf16 v[76:79], v[48:51], v[104:107], 0\n"
      "v_max3_u32 v5, v48, v49, v50\n"
      "s_waitcnt vmcnt(3)\n"
      "v_mfma_f32_16x16x32_bf16 v[56:59], v[52:55], v[108:111], 0\n"
      "v_mfma_f32_16x16x32_bf16 v[60:63], v[52:55], v[104:107], 0\n"
      "v_max3_u32 v6, v51, v52, v53\n"
      "v_max3_u32 v5, v5, v54, v55\n"
      "v_max_u32 v5, v5, v6\n"
      "v_cmp_eq_u32 vcc, -1, v5\n"
      "s_cbranch_vccnz LrC%=\n"
      "v_cvt_pk_bf16_f32 v48, v72, v76\n"
      "v_cvt_pk_bf16_f32 v49, v73, v77\n"
      "v_cvt_pk_bf16_f32 v50, v74, v78\n"
      "v_cvt_pk_bf16_f32 v51, v75, v79\n"
      "ds_write_b128 v18, v[48:51] offset:0\n"
      "v_cvt_pk_bf16_f32 v52, v56, v60\n"
      "v_cvt_pk_bf16_f32 v53, v57, v61\n"
      "v_cvt_pk_bf16_f32 v54, v58, v62\n"
      "v_cvt_pk_bf16_f32 v55, v59, v63\n"
      "ds_write_b128 v18, v[52:55] offset:64\n"
      "buffer_load_dwordx4 v9, s[28:31], s63 offen offset:1024 lds\n"
      "buffer_load_dwordx4 v9, s[28:31], s64 offen offset:2048 lds\n"
      "s_waitcnt lgkmcnt(0)\n"
      "s_barrier\n"
      "ds_read_b32 v48, v20 offset:0\n"
      "ds_read_b32 v49, v21 offset:0\n"
      "ds_read_b32 v50, v22 offset:0\n"
      "ds_read_b32 v51, v23 offset:0\n"
      "ds_read_b32 v52, v20 offset:8320\n"
      "ds_read_b32 v53, v21 offset:8320\n"
      "ds_read_b32 v54, v22 offset:8320\n"
      "ds_read_b32 v55, v23 offset:8320\n"
      "ds_read_b128 v[108:111], v1 offset:48128\n"
      "ds_read_b128 v[104:107], v1 offset:49152\n"
      "buffer_load_dwordx4 v9, s[28:31], s65 offen offset:3072 lds\n"
      "s_add_u32 m0, s51, 32768\n"
      "s_add_u32 s52, s59, 6\n"
      "s_and_b32 s52, s52, 7\n"
      "s_mul_i32 s52, s52, 11008\n"
      "v_add_u32 v9, s52, v7\n"
      "buffer_load_dwordx4 v9, s[24:27], 0 offen offset:0 lds\n"
      "buffer_load_dwordx4 v9, s[24:27], s60 offen offset:1024 lds\n"
      "buffer_load_dwordx4 v9, s[24:27], s61 offen offset:2048 lds\n"
      "s_waitcnt lgkmcnt(6)\n"
      "v_mfma_f32_16x16x32_bf16 v[80:83], v[48:51], v[36:39], 0\n"
      "v_mfma_f32_16x16x32_bf16 v[84:87], v[48:51], v[32:35], 0\n"
      "v_mfma_f32_16x16x32_bf16 v[72:75], v[48:51], v[100:103], 0\n"
      "v_mfma_f32_16x16x32_bf16 v[76:79], v[48:51], v[40:43], 0\n"
      "s_waitcnt lgkmcnt(0)\n"
      "v_mfma_f32_16x16x32_bf16 v[64:67], v[52:55], v[36:39], 0\n"
      "v_mfma_f32_16x16x32_bf16 v[68:71], v[52:55], v[32:35], 0\n"
      "v_mfma_f32_16x16x32_bf16 v[56:59], v[52:55], v[100:103], 0\n"
      "v_mfma_f32_16x16x32_bf16 v[60:63], v[52:55], v[40:43], 0\n"
      "s_nop 1\n"
      "v_cndmask_b32_e64 v72, v72, v80, s[80:81]\n"
      "v_cndmask_b32_e64 v76, v76, v84, s[80:81]\n"
      "v_pk_mul_f32 v[80:81], v[72:73], v[88:89]\n"
      "v_pk_mul_f32 v[82:83], v[74:75], v[90:91]\n"
      "v_pk_mul_f32 v[84:85], v[76:77], v[88:89]\n"
      "v_pk_mul_f32 v[86:87], v[78:79], v[90:91]\n"
      "v_pk_fma_f32 v[76:77], v[76:77], v[92:93], v[80:81]\n"
      "v_pk_fma_f32 v[72:73], v[72:73], v[92:93], v[84:85] neg_lo:[0,0,1] neg_hi:[0,0,1]\n"
      "v_pk_fma_f32 v[78:79], v[78:79], v[94:95], v[82:83]\n"
      "v_pk_fma_f32 v[74:75], v[74:75], v[94:95], v[86:87] neg_lo:[0,0,1] neg_hi:[0,0,1]\n"
      "v_cvt_pk_bf16_f32 v48, v72, v76\n"
      "v_cvt_pk_bf16_f32 v49, v73, v77\n"
      "v_cvt_pk_bf16_f32 v50, v74, v78\n"
      "v_cvt_pk_bf16_f32 v51, v75, v79\n"
      "v_cndmask_b32_e64 v56, v56, v64, s[80:81]\n"
      "v_cndmask_b32_e64 v60, v60, v68, s[80:81]\n"
      "v_mfma_f32_16x16x32_bf16 v[72:75], v[48:51], v[108:111], v[240:243]\n"
      "v_pk_mul_f32 v[64:65], v[56:57], v[88:89]\n"
      "v_pk_mul_f32 v[66:67], v[58:59], v[90:91]\n"
      "v_mfma_f32_16x16x32_bf16 v[76:79], v[48:51], v[104:107], v[244:247]\n"
      "v_pk_mul_f32 v[68:69], v[60:61], v[88:89]\n"
      "v_pk_mul_f32 v[70:71], v[62:63], v[90:91]\n"
      "v_pk_fma_f32 v[60:61], v[60:61], v[92:93], v[64:65]\n"
      "v_pk_fma_f32 v[56:57], v[56:57], v[92:93], v[68:69] neg_lo:[0,0,1] neg_hi:[0,0,1]\n"
      "v_pk_fma_f32 v[62:63], v[62:63], v[94:95], v[66:67]\n"
      "v_pk_fma_f32 v[58:59], v[58:59], v[94:95], v[70:71] neg_lo:[0,0,1] neg_hi:[0,0,1]\n"
      "v_cvt_pk_bf16_f32 v52, v56, v60\n"
      "v_cvt_pk_bf16_f32 v53, v57, v61\n"
      "v_cvt_pk_bf16_f32 v54, v58, v62\n"
      "v_cvt_pk_bf16_f32 v55, v59, v63\n"
      "s_waitcnt vmcnt(8)\n"
      "v_cvt_pk_bf16_f32 v80, v72, v73\n"
      "v_cvt_pk_bf16_f32 v81, v74, v75\n"
      "v_mfma_f32_16x16x32_bf16 v[56:59], v[52:55], v[108:111], v[248:251]\n"
      "v_cvt_pk_bf16_f32 v82, v76, v77\n"
      "v_cvt_pk_bf16_f32 v83, v78, v79\n"
      "v_mfma_f32_16x16x32_bf16 v[60:63], v[52:55], v[104:107], v[252:255]\n"
      "v_dot2c_f32_bf16 v96, 0x3b003b00, v80\n"
      "v_dot2c_f32_bf16 v97, v80, v80\n"
      "v_dot2c_f32_bf16 v96, 0x3b003b00, v81\n"
      "v_dot2c_f32_bf16 v97, v81, v81\n"
      "v_dot2c_f32_bf16 v96, 0x3b003b00, v82\n"
      "v_dot2c_f32_bf16 v97, v82, v82\n"
      "v_dot2c_f32_bf16 v96, 0x3b003b00, v83\n"
      "v_dot2c_f32_bf16 v97, v83, v83\n"
      "v_cvt_pk_bf16_f32 v64, v56, v57\n"
      "v_cvt_pk_bf16_f32 v65, v58, v59\n"
      "v_cvt_pk_bf16_f32 v66, v60, v61\n"
      "v_cvt_pk_bf16_f32 v67, v62, v63\n"
      "v_dot2c_f32_bf16 v98, 0x3b003b00, v64\n"
      "v_dot2c_f32_bf16 v99, v64, v64\n"
      "v_dot2c_f32_bf16 v98, 0x3b003b00, v65\n"
      "v_dot2c_f32_bf16 v99, v65, v65\n"
      "v_dot2c_f32_bf16 v98, 0x3b003b00, v66\n"
      "v_dot2c_f32_bf16 v99, v66, v66\n"
      "v_dot2c_f32_bf16 v98, 0x3b003b00, v67\n"
      "v_dot2c_f32_bf16 v99, v67, v67\n"
      "v_lshlrev_b32 v240, 16, v80\n"
      "v_permlane32_swap_b32 v96, v97\n"
      "v_and_b32 v241, 0xffff0000, v80\n"
      "v_permlane32_swap_b32 v98, v99\n"
      "v_add_f32 v96, v96, v97\n"
      "v_add_f32 v98, v98, v99\n"
      "v_lshlrev_b32 v242, 16, v81\n"
      "v_and_b32 v243, 0xffff0000, v81\n"
      "v_permlane16_swap_b32 v96, v98\n"
      "v_add_f32 v96, v96, v98\n"
      "v_lshlrev_b32 v244, 16, v82\n"
      "v_and_b32 v245, 0xffff0000, v82\n"
      "v_add_f32_dpp v96, v96, v96 row_ror:8 row_mask:0xf bank_mask:0xf\n"
      "v_lshlrev_b32 v246, 16, v83\n"
      "v_and_b32 v247, 0xffff0000, v83\n"
      "v_add_f32_dpp v96, v96, v96 row_ror:4 row_mask:0xf bank_mask:0xf\n"
      "v_permlane16_swap_b32 v80, v82\n"
      "v_permlane16_swap_b32 v81, v83\n"
      "v_add_f32_dpp v96, v96, v96 row_ror:2 row_mask:0xf bank_mask:0xf\n"
      "buffer_store_dwordx4 v[80:83], v14, s[20:23], s57 offen offset:0 sc1\n"
      "v_lshlrev_b32 v248, 16, v64\n"
      "v_add_f32_dpp v96, v96, v96 row_ror:1 row_mask:0xf bank_mask:0xf\n"
      "v_and_b32 v249, 0xffff0000, v64\n"
      "v_readlane_b32 s84, v96, 0\n"
      "v_readlane_b32 s85, v96, 16\n"
      "v_readlane_b32 s86, v96, 32\n"
      "v_readlane_b32 s87, v96, 48\n"
      "v_lshlrev_b32 v250, 16, v65\n"
      "v_and_b32 v251, 0xffff0000, v65\n"
      "v_lshlrev_b32 v252, 16, v66\n"
      "v_and_b32 v253, 0xffff0000, v66\n"
      "v_lshlrev_b32 v254, 16, v67\n"
      "v_and_b32 v255, 0xffff0000, v67\n"
      "v_permlane16_swap_b32 v64, v66\n"
      "v_permlane16_swap_b32 v65, v67\n"
      "buffer_store_dwordx4 v[64:67], v14, s[20:23], s57 offen offset:1024 sc1\n"
      "v_mul_f32_e64 v5, s84, s84\n"
      "v_mul_f32_e64 v6, s85, s85\n"
      "v_fma_f32 v5, s86, v30, -v5\n"
      "v_fma_f32 v6, s87, v30, -v6\n"
      "v_max_f32 v5, 0, v5\n"
      "v_max_f32 v6, 0, v6\n"
      "v_fmaak_f32 v5, v5, v31, 0x2edbe6ff\n"
      "v_fmaak_f32 v6, v6, v31, 0x2edbe6ff\n"
      "v_rsq_f32 v44, v5\n"
      "v_rsq_f32 v46, v6\n"
      "v_mul_f32_e64 v45, -s84, v44\n"
      "v_mul_f32_e64 v47, -s85, v46\n"
      "v_pk_fma_f32 v[240:241], v[240:241], v[44:45], v[44:45] op_sel:[0,0,1] op_sel_hi:[1,0,1]\n"
      "v_pk_fma_f32 v[242:243], v[242:243], v[44:45], v[44:45] op_sel:[0,0,1] op_sel_hi:[1,0,1]\n"
      "v_pk_fma_f32 v[244:245], v[244:245], v[44:45], v[44:45] op_sel:[0,0,1] op_sel_hi:[1,0,1]\n"
      "v_pk_fma_f32 v[246:247], v[246:247], v[44:45], v[44:45] op_sel:[0,0,1] op_sel_hi:[1,0,1]\n"
      "v_cvt_pk_bf16_f32 v48, v240, v241\n"
      "v_cvt_pk_bf16_f32 v49, v242, v243\n"
      "v_cvt_pk_bf16_f32 v50, v244, v245\n"
      "v_cvt_pk_bf16_f32 v51, v246, v247\n"
      "ds_write2st64_b64 v19, v[48:49], v[50:51] offset0:33 offset1:34\n"
      "v_pk_fma_f32 v[248:249], v[248:249], v[46:47], v[46:47] op_sel:[0,0,1] op_sel_hi:[1,0,1]\n"
      "v_pk_fma_f32 v[250:251], v[250:251], v[46:47], v[46:47] op_sel:[0,0,1] op_sel_hi:[1,0,1]\n"
      "v_pk_fma_f32 v[252:253], v[252:253], v[46:47], v[46:47] op_sel:[0,0,1] op_sel_hi:[1,0,1]\n"
      "v_pk_fma_f32 v[254:255], v[254:255], v[46:47], v[46:47] op_sel:[0,0,1] op_sel_hi:[1,0,1]\n"
      "v_cvt_pk_bf16_f32 v52, v248, v249\n"
      "v_cvt_pk_bf16_f32 v53, v250, v251\n"
      "v_cvt_pk_bf16_f32 v54, v252, v253\n"
      "v_cvt_pk_bf16_f32 v55, v254, v255\n"
      "ds_write2st64_b64 v19, v[52:53], v[54:55] offset0:50 offset1:51\n"
      "s_waitcnt lgkmcnt(0)\n"
      "s_barrier\n"
      "ds_read_b64_tr_b16 v[48:49], v24 offset:16896\n"
      "ds_read_b64_tr_b16 v[50:51], v24 offset:21248\n"
      "ds_read_b64_tr_b16 v[52:53], v24 offset:25600\n"
      "ds_read_b64_tr_b16 v[54:55], v24 offset:29952\n"
      "s_cmp_eq_u32 s77, 0\n"
      "s_cbranch_scc0 Lfb2%=\n"
      "s_mov_b32 s46, 20480\n"
      "buffer_store_dword v27, v26, s[20:23], s46 offen sc1\n"
      "Lfb2%=:\n"
      "s_waitcnt lgkmcnt(2)\n"
      "v_min_u32 v48, -2, v48\n"
      "v_min_u32 v49, -2, v49\n"
      "v_min_u32 v50, -2, v50\n"
      "v_min_u32 v51, -2, v51\n"
      "buffer_store_dwordx4 v[48:51], v25, s[20:23], s82 offen sc1\n"
      "s_waitcnt lgkmcnt(0)\n"
      "v_min_u32 v52, -2, v52\n"
      "v_min_u32 v53, -2, v53\n"
      "v_min_u32 v54, -2, v54\n"
      "v_min_u32 v55, -2, v55\n"
      "buffer_store_dwordx4 v[52:55], v25, s[20:23], s83 offen sc1\n"
      "s_barrier\n"
      "s_or_b32 s41, s16, s39\n"
      "s_cbranch_scc1 Lnoe%=\n"
      "buffer_store_dword v27, off, s[20:23], 0 sc1\n"
      "Lnoe%=:\n"
      "buffer_load_dwordx4 v9, s[24:27], s62 offen offset:3072 lds\n"
      "s_lshr_b32 s43, s39, 2\n"
      "s_lshl1_add_u32 s42, s48, s43\n"
      "s_cmp_lt_u32 s42, 43\n"
      "s_cselect_b32 s42, s42, 0\n"
      "s_cselect_b32 s41, 0, 1073741824\n"
      "s_add_u32 s66, s66, s41\n"
      "s_add_u32 s67, s67, s41\n"
      "s_add_u32 s68, s68, s41\n"
      "s_and_b32 s41, s39, 3\n"
      "s_lshl3_add_u32 s42, s42, s41\n"
      "s_lshl_b32 s42, s42, 3\n"
      "v_and_b32 v5, 1, v0\n"
      "v_and_b32 v6, 2, v0\n"
      "v_lshl_add_u32 v20, v5, 2, s42\n"
      "v_lshl_add_u32 v20, v6, 4, v20\n"
      "global_load_dword v15, v20, s[14:15]\n"
      "v_and_b32 v22, 0xfc, v0\n"
      "v_lshlrev_b32 v22, 2, v22\n"
      "v_mul_u32_u24 v6, 576, v6\n"
      "v_xor_b32 v22, v22, v6\n"
      "s_lshl_b32 s43, s43, 11\n"
      "v_lshl_add_u32 v5, v5, 3, s43\n"
      "v_add_u32 v22, v22, v5\n"
      "s_lshl_b32 s41, s39, 12\n"
      "v_add_u32 v20, s41, v1\n"
      "v_xor_b32 v21, 0x80, v20\n"
      "s_mul_i32 s41, s49, 768\n"
      "s_mul_i32 s42, s39, 262144\n"
      "s_add_u32 s41, s41, s42\n"
      "v_lshl_add_u32 v18, v3, 4, s41\n"
      "v_mul_u32_u24 v19, 8192, v4\n"
      "v_add_u32 v18, v18, v19\n"
      "v_add_u32 v19, s55, v18\n"
      "s_add_u32 s41, s59, 0\n"
      "s_and_b32 s41, s41, 7\n"
      "s_mul_i32 s41, s41, 32768\n"
      "s_add_u32 s75, s41, 4259840\n"
      "s_add_u32 s41, s59, 1\n"
      "s_and_b32 s41, s41, 7\n"
      "s_mul_i32 s41, s41, 32768\n"
      "s_add_u32 s76, s41, 4259840\n"
      "s_add_u32 s41, s59, 2\n"
      "s_and_b32 s41, s41, 7\n"
      "s_mul_i32 s41, s41, 32768\n"
      "s_add_u32 s77, s41, 4259840\n"
      "s_add_u32 s41, s59, 3\n"
      "s_and_b32 s41, s41, 7\n"
      "s_mul_i32 s41, s41, 32768\n"
      "s_add_u32 s78, s41, 4259840\n"
      "s_add_u32 s41, s59, 4\n"
      "s_and_b32 s41, s41, 7\n"
      "s_mul_i32 s41, s41, 32768\n"
      "s_add_u32 s79, s41, 4259840\n"
      "s_add_u32 s41, s59, 5\n"
      "s_and_b32 s41, s41, 7\n"
      "s_mul_i32 s41, s41, 32768\n"
      "s_add_u32 s80, s41, 4259840\n"
      "s_add_u32 s41, s59, 6\n"
      "s_and_b32 s41, s41, 7\n"
      "s_mul_i32 s41, s41, 32768\n"
      "s_add_u32 s81, s41, 4259840\n"
      "s_add_u32 s41, s59, 7\n"
      "s_and_b32 s41, s41, 7\n"
      "s_mul_i32 s41, s41, 32768\n"
      "s_add_u32 s82, s41, 4259840\n"
      "v_mov_b32 v16, 0xbdd2d3e8\n"
      "v_mov_b32 v17, 0xc0135761\n"
      "v_and_b32 v2, 31, v0\n"
      "v_lshlrev_b32 v2, 2, v2\n"
      "v_bfe_u32 v5, v0, 5, 1\n"
      "v_mul_u32_u24 v5, 128, v5\n"
      "v_add_u32 v2, v2, v5\n"
      "v_bfe_u32 v5, v0, 6, 2\n"
      "v_mul_u32_u24 v5, 256, v5\n"
      "v_add_u32 v2, v2, v5\n"
      "v_bfe_u32 v5, v0, 8, 1\n"
      "v_mul_u32_u24 v5, 1024, v5\n"
      "v_add_u32 v2, v2, v5\n"
      "s_and_b32 s41, s39, 1\n"
      "s_lshl1_add_u32 s42, s48, s41\n"
      "s_cmp_lt_u32 s39, 2\n"
      "s_cselect_b32 s83, s66, s67\n"
      "s_lshl_b32 s41, s41, 10\n"
      "s_add_u32 s83, s83, s41\n"
      "s_cmp_lt_u32 s42, 43\n"
      "s_cselect_b32 s83, s83, 1073741824\n"
      "s_cselect_b32 s42, s42, 0\n"
      "s_lshl_b32 s84, s42, 6\n"
      "s_waitcnt vmcnt(13)\n"
      "v_add_u32 v10, 0x8000, v10\n"
      "v_add_u32 v11, 0x8000, v11\n"
      "v_add_u32 v12, 0x8000, v12\n"
      "v_add_u32 v13, 0x8000, v13\n"
      "ds_read_b64_tr_b16 v[208:209], v10 offset:32768\n"
      "ds_read_b64_tr_b16 v[210:211], v10 offset:33792\n"
      "ds_read_b64_tr_b16 v[212:213], v11 offset:32768\n"
      "ds_read_b64_tr_b16 v[214:215], v11 offset:33792\n"
      "ds_read_b64_tr_b16 v[216:217], v12 offset:32768\n"
      "ds_read_b64_tr_b16 v[218:219], v12 offset:33792\n"
      "ds_read_b64_tr_b16 v[220:221], v13 offset:32768\n"
      "ds_read_b64_tr_b16 v[222:223], v13 offset:33792\n"
      "s_waitcnt lgkmcnt(0)\n"
      "s_add_u32 m0, s51, 65536\n"
      "s_add_u32 s52, s59, 7\n"
      "s_and_b32 s52, s52, 7\n"
      "s_mul_i32 s52, s52, 11008\n"
      "v_add_u32 v9, s52, v7\n"
      "buffer_load_dwordx4 v9, s[24:27], 0 offen offset:0 lds\n"
      "buffer_load_dwordx4 v9, s[24:27], s60 offen offset:1024 lds\n"
      "buffer_load_dwordx4 v9, s[24:27], s61 offen offset:2048 lds\n"
      "buffer_load_dwordx4 v9, s[24:27], s62 offen offset:3072 lds\n"
      "s_waitcnt vmcnt(13)\n"
      "v_add_u32 v10, 0xffff8000, v10\n"
      "v_add_u32 v11, 0xffff8000, v11\n"
      "v_add_u32 v12, 0xffff8000, v12\n"
      "v_add_u32 v13, 0xffff8000, v13\n"
      "ds_read_b64_tr_b16 v[112:113], v10 offset:0\n"
      "ds_read_b64_tr_b16 v[114:115], v10 offset:1024\n"
      "ds_read_b64_tr_b16 v[116:117], v11 offset:0\n"
      "ds_read_b64_tr_b16 v[118:119], v11 offset:1024\n"
      "ds_read_b64_tr_b16 v[120:121], v12 offset:0\n"
      "ds_read_b64_tr_b16 v[122:123], v12 offset:1024\n"
      "ds_read_b64_tr_b16 v[124:125], v13 offset:0\n"
      "ds_read_b64_tr_b16 v[126:127], v13 offset:1024\n"
      "s_waitcnt lgkmcnt(0)\n"
      "s_add_u32 m0, s51, 0\n"
      "s_lshl_b32 s52, s70, 17\n"
      "v_add_u32 v9, s52, v8\n"
      "buffer_load_dwordx4 v9, s[28:31], 0 offen offset:0 lds\n"
      "buffer_load_dwordx4 v9, s[28:31], s63 offen offset:1024 lds\n"
      "buffer_load_dwordx4 v9, s[28:31], s64 offen offset:2048 lds\n"
      "buffer_load_dwordx4 v9, s[28:31], s65 offen offset:3072 lds\n"
      "s_waitcnt vmcnt(12)\n"
      "s_waitcnt vmcnt(10)\n"
      "s_cmp_lg_u32 s39, 0\n"
      "s_cbranch_scc1 Lgd2r%=\n"
      "v_and_b32 v5, 7, v0\n"
      "s_lshl_b32 s41, s36, 10\n"
      "v_lshlrev_b32 v5, 7, v5\n"
      "s_add_u32 s41, s41, 20480\n"
      "s_mov_b32 s42, 4000000\n"
      "Lgp2r%=:\n"
      "buffer_load_dword v6, v5, s[20:23], s41 offen sc1\n"
      "s_sub_u32 s42, s42, 1\n"
      "s_cbranch_scc1 Lend%=\n"
      "s_waitcnt vmcnt(0)\n"
      "v_cmp_ne_u32 vcc, s40, v6\n"
      "s_cbranch_vccnz Lgp2r%=\n"
      "Lgd2r%=:\n"
      "s_barrier\n"
      "buffer_load_dwordx4 v[32:35], v18, s[20:23], s75 offen offset:0 sc1\n"
      "buffer_load_dwordx4 v[36:39], v18, s[20:23], s76 offen offset:0 sc1\n"
      "buffer_load_dwordx4 v[40:43], v18, s[20:23], s77 offen offset:0 sc1\n"
      "buffer_load_dwordx4 v[44:47], v18, s[20:23], s78 offen offset:0 sc1\n"
      "buffer_load_dwordx4 v[48:51], v18, s[20:23], s79 offen offset:0 sc1\n"
      "buffer_load_dwordx4 v[52:55], v18, s[20:23], s80 offen offset:0 sc1\n"
      "s_waitcnt vmcnt(15)\n"
      "ds_read_b64_tr_b16 v[224:225], v10 offset:32768\n"
      "ds_read_b64_tr_b16 v[226:227], v10 offset:33792\n"
      "ds_read_b64_tr_b16 v[228:229], v11 offset:32768\n"
      "ds_read_b64_tr_b16 v[230:231], v11 offset:33792\n"
      "ds_read_b64_tr_b16 v[232:233], v12 offset:32768\n"
      "ds_read_b64_tr_b16 v[234:235], v12 offset:33792\n"
      "ds_read_b64_tr_b16 v[236:237], v13 offset:32768\n"
      "ds_read_b64_tr_b16 v[238:239], v13 offset:33792\n"
      "s_waitcnt vmcnt(10)\n"
      "v_add_u32 v10, 0x8000, v10\n"
      "v_add_u32 v11, 0x8000, v11\n"
      "v_add_u32 v12, 0x8000, v12\n"
      "v_add_u32 v13, 0x8000, v13\n"
      "ds_read_b64_tr_b16 v[240:241], v10 offset:32768\n"
      "ds_read_b64_tr_b16 v[242:243], v10 offset:33792\n"
      "ds_read_b64_tr_b16 v[244:245], v11 offset:32768\n"
      "ds_read_b64_tr_b16 v[246:247], v11 offset:33792\n"
      "ds_read_b64_tr_b16 v[248:249], v12 offset:32768\n"
      "ds_read_b64_tr_b16 v[250:251], v12 offset:33792\n"
      "ds_read_b64_tr_b16 v[252:253], v13 offset:32768\n"
      "ds_read_b64_tr_b16 v[254:255], v13 offset:33792\n"
      "s_waitcnt vmcnt(6)\n"
      "v_add_u32 v10, 0xffff8000, v10\n"
      "v_add_u32 v11, 0xffff8000, v11\n"
      "v_add_u32 v12, 0xffff8000, v12\n"
      "v_add_u32 v13, 0xffff8000, v13\n"
      "ds_read_b64_tr_b16 v[96:97], v10 offset:0\n"
      "ds_read_b64_tr_b16 v[98:99], v10 offset:1024\n"
      "ds_read_b64_tr_b16 v[100:101], v11 offset:0\n"
      "ds_read_b64_tr_b16 v[102:103], v11 offset:1024\n"
      "ds_read_b64_tr_b16 v[104:105], v12 offset:0\n"
      "ds_read_b64_tr_b16 v[106:107], v12 offset:1024\n"
      "ds_read_b64_tr_b16 v[108:109], v13 offset:0\n"
      "ds_read_b64_tr_b16 v[110:111], v13 offset:1024\n"
      "v_lshlrev_b32 v14, 16, v15\n"
      "v_and_b32 v15, 0xffff0000, v15\n"
      "Ljoin%=:\n"
      "s_waitcnt lgkmcnt(0)\n"
      "s_waitcnt vmcnt(5)\n"
      "buffer_load_dwordx4 v[56:59], v18, s[20:23], s81 offen offset:0 sc1\n"
      "LcP0%=:\n"
      "v_max3_u32 v23, v32, v33, v34\n"
      "v_max_u32 v23, v23, v35\n"
      "v_cmp_eq_u32 vcc, -1, v23\n"
      "s_cbranch_vccnz LrP0%=\n"
      "v_mfma_f32_16x16x32_bf16 v[80:83], v[128:131], v[32:35], 0\n"
      "v_mfma_f32_16x16x32_bf16 v[84:87], v[132:135], v[32:35], 0\n"
      "v_mfma_f32_16x16x32_bf16 v[88:91], v[136:139], v[32:35], 0\n"
      "v_mfma_f32_16x16x32_bf16 v[92:95], v[140:143], v[32:35], 0\n"
      "s_waitcnt vmcnt(5)\n"
      "buffer_load_dwordx4 v[60:63], v18, s[20:23], s82 offen offset:0 sc1\n"
      "LcP1%=:\n"
      "v_max3_u32 v23, v36, v37, v38\n"
      "v_max_u32 v23, v23, v39\n"
      "v_cmp_eq_u32 vcc, -1, v23\n"
      "s_cbranch_vccnz LrP1%=\n"
      "v_mfma_f32_16x16x32_bf16 v[80:83], v[144:147], v[36:39], v[80:83]\n"
      "v_mfma_f32_16x16x32_bf16 v[84:87], v[148:151], v[36:39], v[84:87]\n"
      "v_mfma_f32_16x16x32_bf16 v[88:91], v[152:155], v[36:39], v[88:91]\n"
      "v_mfma_f32_16x16x32_bf16 v[92:95], v[156:159], v[36:39], v[92:95]\n"
      "s_waitcnt vmcnt(5)\n"
      "buffer_load_dwordx4 v[32:35], v18, s[20:23], s75 offen offset:256 sc1\n"
      "LcP2%=:\n"
      "v_max3_u32 v23, v40, v41, v42\n"
      "v_max_u32 v23, v23, v43\n"
      "v_cmp_eq_u32 vcc, -1, v23\n"
      "s_cbranch_vccnz LrP2%=\n"
      "v_mfma_f32_16x16x32_bf16 v[80:83], v[160:163], v[40:43], v[80:83]\n"
      "v_mfma_f32_16x16x32_bf16 v[84:87], v[164:167], v[40:43], v[84:87]\n"
      "v_mfma_f32_16x16x32_bf16 v[88:91], v[168:171], v[40:43], v[88:91]\n"
      "v_mfma_f32_16x16x32_bf16 v[92:95], v[172:175], v[40:43], v[92:95]\n"
      "s_waitcnt vmcnt(5)\n"
      "buffer_load_dwordx4 v[36:39], v18, s[20:23], s76 offen offset:256 sc1\n"
      "LcP3%=:\n"
      "v_max3_u32 v23, v44, v45, v46\n"
      "v_max_u32 v23, v23, v47\n"
      "v_cmp_eq_u32 vcc, -1, v23\n"
      "s_cbranch_vccnz LrP3%=\n"
      "v_mfma_f32_16x16x32_bf16 v[80:83], v[176:179], v[44:47], v[80:83]\n"
      "v_mfma_f32_16x16x32_bf16 v[84:87], v[180:183], v[44:47], v[84:87]\n"
      "v_mfma_f32_16x16x32_bf16 v[88:91], v[184:187], v[44:47], v[88:91]\n"
      "v_mfma_f32_16x16x32_bf16 v[92:95], v[188:191], v[44:47], v[92:95]\n"
      "s_waitcnt vmcnt(5)\n"
      "buffer_load_dwordx4 v[40:43], v18, s[20:23], s77 offen offset:256 sc1\n"
      "LcP4%=:\n"
      "v_max3_u32 v23, v48, v49, v50\n"
      "v_max_u32 v23, v23, v51\n"
      "v_cmp_eq_u32 vcc, -1, v23\n"
      "s_cbranch_vccnz LrP4%=\n"
      "v_mfma_f32_16x16x32_bf16 v[80:83], v[192:195], v[48:51], v[80:83]\n"
      "v_mfma_f32_16x16x32_bf16 v[84:87], v[196:199], v[48:51], v[84:87]\n"
      "v_mfma_f32_16x16x32_bf16 v[88:91], v[200:203], v[48:51], v[88:91]\n"
      "v_mfma_f32_16x16x32_bf16 v[92:95], v[204:207], v[48:51], v[92:95]\n"
      "s_waitcnt vmcnt(5)\n"
      "buffer_load_dwordx4 v[44:47], v18, s[20:23], s78 offen offset:256 sc1\n"
      "LcP5%=:\n"
      "v_max3_u32 v23, v52, v53, v54\n"
      "v_max_u32 v23, v23, v55\n"
      "v_cmp_eq_u32 vcc, -1, v23\n"
      "s_cbranch_vccnz LrP5%=\n"
      "v_mfma_f32_16x16x32_bf16 v[80:83], v[208:211], v[52:55], v[80:83]\n"
      "v_mfma_f32_16x16x32_bf16 v[84:87], v[212:215], v[52:55], v[84:87]\n"
      "v_mfma_f32_16x16x32_bf16 v[88:91], v[216:219], v[52:55], v[88:91]\n"
      "v_mfma_f32_16x16x32_bf16 v[92:95], v[220:223], v[52:55], v[92:95]\n"
      "s_waitcnt vmcnt(5)\n"
      "buffer_load_dwordx4 v[48:51], v18, s[20:23], s79 offen offset:256 sc1\n"
      "LcP6%=:\n"
      "v_max3_u32 v23, v56, v57, v58\n"
      "v_max_u32 v23, v23, v59\n"
      "v_cmp_eq_u32 vcc, -1, v23\n"
      "s_cbranch_vccnz LrP6%=\n"
      "v_mfma_f32_16x16x32_bf16 v[80:83], v[224:227], v[56:59], v[80:83]\n"
      "v_mfma_f32_16x16x32_bf16 v[84:87], v[228:231], v[56:59], v[84:87]\n"
      "v_mfma_f32_16x16x32_bf16 v[88:91], v[232:235], v[56:59], v[88:91]\n"
      "v_mfma_f32_16x16x32_bf16 v[92:95], v[236:239], v[56:59], v[92:95]\n"
      "s_waitcnt vmcnt(5)\n"
      "buffer_load_dwordx4 v[52:55], v18, s[20:23], s80 offen offset:256 sc1\n"
      "LcP7%=:\n"
      "v_max3_u32 v23, v60, v61, v62\n"
      "v_max_u32 v23, v23, v63\n"
      "v_cmp_eq_u32 vcc, -1, v23\n"
      "s_cbranch_vccnz LrP7%=\n"
      "v_mfma_f32_16x16x32_bf16 v[80:83], v[240:243], v[60:63], v[80:83]\n"
      "v_mfma_f32_16x16x32_bf16 v[84:87], v[244:247], v[60:63], v[84:87]\n"
      "v_mfma_f32_16x16x32_bf16 v[88:91], v[248:251], v[60:63], v[88:91]\n"
      "v_mfma_f32_16x16x32_bf16 v[92:95], v[252:255], v[60:63], v[92:95]\n"
      "s_waitcnt vmcnt(5)\n"
      "buffer_load_dwordx4 v[56:59], v18, s[20:23], s81 offen offset:256 sc1\n"
      "v_mfma_f32_16x16x32_bf16 v[64:67], v[128:131], v[32:35], 0\n"
      "v_max_u32 v23, v32, v33\n"
      "v_mfma_f32_16x16x32_bf16 v[68:71], v[132:135], v[32:35], 0\n"
      "v_max3_u32 v23, v23, v34, v35\n"
      "v_mfma_f32_16x16x32_bf16 v[72:75], v[136:139], v[32:35], 0\n"
      "ds_write_b128 v20, v[80:83] offset:32768\n"
      "v_mfma_f32_16x16x32_bf16 v[76:79], v[140:143], v[32:35], 0\n"
      "s_waitcnt vmcnt(5)\n"
      "buffer_load_dwordx4 v[60:63], v18, s[20:23], s82 offen offset:256 sc1\n"
      "v_mfma_f32_16x16x32_bf16 v[64:67], v[144:147], v[36:39], v[64:67]\n"
      "v_max3_u32 v23, v23, v36, v37\n"
      "v_mfma_f32_16x16x32_bf16 v[68:71], v[148:151], v[36:39], v[68:71]\n"
      "v_max3_u32 v23, v23, v38, v39\n"
      "v_mfma_f32_16x16x32_bf16 v[72:75], v[152:155], v[36:39], v[72:75]\n"
      "v_mfma_f32_16x16x32_bf16 v[76:79], v[156:159], v[36:39], v[76:79]\n"
      "s_add_u32 m0, s51, 32768\n"
      "s_lshl_b32 s52, s71, 17\n"
      "v_add_u32 v9, s52, v8\n"
      "buffer_load_dwordx4 v9, s[28:31], 0 offen offset:0 lds\n"
      "s_waitcnt vmcnt(6)\n"
      "buffer_load_dwordx4 v[32:35], v19, s[20:23], s75 offen offset:512 sc1\n"
      "v_mfma_f32_16x16x32_bf16 v[64:67], v[160:163], v[40:43], v[64:67]\n"
      "v_max3_u32 v23, v23, v40, v41\n"
      "v_mfma_f32_16x16x32_bf16 v[68:71], v[164:167], v[40:43], v[68:71]\n"
      "v_max3_u32 v23, v23, v42, v43\n"
      "v_mfma_f32_16x16x32_bf16 v[72:75], v[168:171], v[40:43], v[72:75]\n"
      "ds_write_b128 v21, v[84:87] offset:33792\n"
      "v_mfma_f32_16x16x32_bf16 v[76:79], v[172:175], v[40:43], v[76:79]\n"
      "s_waitcnt vmcnt(6)\n"
      "buffer_load_dwordx4 v[36:39], v19, s[20:23], s76 offen offset:512 sc1\n"
      "v_mfma_f32_16x16x32_bf16 v[64:67], v[176:179], v[44:47], v[64:67]\n"
      "v_max3_u32 v23, v23, v44, v45\n"
      "v_mfma_f32_16x16x32_bf16 v[68:71], v[180:183], v[44:47], v[68:71]\n"
      "v_max3_u32 v23, v23, v46, v47\n"
      "v_mfma_f32_16x16x32_bf16 v[72:75], v[184:187], v[44:47], v[72:75]\n"
      "v_mfma_f32_16x16x32_bf16 v[76:79], v[188:191], v[44:47], v[76:79]\n"
      "s_waitcnt vmcnt(6)\n"
      "buffer_load_dwordx4 v[40:43], v19, s[20:23], s77 offen offset:512 sc1\n"
      "v_mfma_f32_16x16x32_bf16 v[64:67], v[192:195], v[48:51], v[64:67]\n"
      "v_max3_u32 v23, v23, v48, v49\n"
      "v_mfma_f32_16x16x32_bf16 v[68:71], v[196:199], v[48:51], v[68:71]\n"
      "v_max3_u32 v23, v23, v50, v51\n"
      "v_mfma_f32_16x16x32_bf16 v[72:75], v[200:203], v[48:51], v[72:75]\n"
      "ds_write_b128 v20, v[88:91] offset:34816\n"
      "v_mfma_f32_16x16x32_bf16 v[76:79], v[204:207], v[48:51], v[76:79]\n"
      "s_waitcnt vmcnt(6)\n"
      "buffer_load_dwordx4 v[44:47], v19, s[20:23], s78 offen offset:512 sc1\n"
      "v_mfma_f32_16x16x32_bf16 v[64:67], v[208:211], v[52:55], v[64:67]\n"
      "v_max3_u32 v23, v23, v52, v53\n"
      "v_mfma_f32_16x16x32_bf16 v[68:71], v[212:215], v[52:55], v[68:71]\n"
      "v_max3_u32 v23, v23, v54, v55\n"
      "v_mfma_f32_16x16x32_bf16 v[72:75], v[216:219], v[52:55], v[72:75]\n"
      "v_mfma_f32_16x16x32_bf16 v[76:79], v[220:223], v[52:55], v[76:79]\n"
      "s_waitcnt vmcnt(6)\n"
      "buffer_load_dwordx4 v[48:51], v19, s[20:23], s79 offen offset:512 sc1\n"
      "v_mfma_f32_16x16x32_bf16 v[64:67], v[224:227], v[56:59], v[64:67]\n"
      "v_max3_u32 v23, v23, v56, v57\n"
      "v_mfma_f32_16x16x32_bf16 v[68:71], v[228:231], v[56:59], v[68:71]\n"
      "v_max3_u32 v23, v23, v58, v59\n"
      "v_mfma_f32_16x16x32_bf16 v[72:75], v[232:235], v[56:59], v[72:75]\n"
      "ds_write_b128 v21, v[92:95] offset:35840\n"
      "v_mfma_f32_16x16x32_bf16 v[76:79], v[236:239], v[56:59], v[76:79]\n"
      "s_waitcnt vmcnt(6)\n"
      "buffer_load_dwordx4 v[52:55], v19, s[20:23], s80 offen offset:512 sc1\n"
      "v_mfma_f32_16x16x32_bf16 v[64:67], v[240:243], v[60:63], v[64:67]\n"
      "v_max3_u32 v23, v23, v60, v61\n"
      "v_mfma_f32_16x16x32_bf16 v[68:71], v[244:247], v[60:63], v[68:71]\n"
      "v_max3_u32 v23, v23, v62, v63\n"
      "v_mfma_f32_16x16x32_bf16 v[72:75], v[248:251], v[60:63], v[72:75]\n"
      "v_mfma_f32_16x16x32_bf16 v[76:79], v[252:255], v[60:63], v[76:79]\n"
      "v_cmp_eq_u32 vcc, -1, v23\n"
      "s_cbranch_vccnz LrG1%=\n"
      "LcG1%=:\n"
      "s_waitcnt vmcnt(5)\n"
      "buffer_load_dwordx4 v[56:59], v19, s[20:23], s81 offen offset:512 sc1\n"
      "v_mfma_f32_16x16x32_bf16 v[80:83], v[128:131], v[32:35], 0\n"
      "v_max_u32 v23, v32, v33\n"
      "v_mfma_f32_16x16x32_bf16 v[84:87], v[132:135], v[32:35], 0\n"
      "v_max3_u32 v23, v23, v34, v35\n"
      "v_mfma_f32_16x16x32_bf16 v[88:91], v[136:139], v[32:35], 0\n"
      "ds_write_b128 v20, v[64:67] offset:0\n"
      "v_mfma_f32_16x16x32_bf16 v[92:95], v[140:143], v[32:35], 0\n"
      "s_waitcnt vmcnt(5)\n"
      "buffer_load_dwordx4 v[60:63], v19, s[20:23], s82 offen offset:512 sc1\n"
      "v_mfma_f32_16x16x32_bf16 v[80:83], v[144:147], v[36:39], v[80:83]\n"
      "v_max3_u32 v23, v23, v36, v37\n"
      "v_mfma_f32_16x16x32_bf16 v[84:87], v[148:151], v[36:39], v[84:87]\n"
      "v_max3_u32 v23, v23, v38, v39\n"
      "v_mfma_f32_16x16x32_bf16 v[88:91], v[152:155], v[36:39], v[88:91]\n"
      "ds_write_b128 v21, v[68:71] offset:1024\n"
      "v_mfma_f32_16x16x32_bf16 v[92:95], v[156:159], v[36:39], v[92:95]\n"
      "s_waitcnt vmcnt(5)\n"
      "v_mfma_f32_16x16x32_bf16 v[80:83], v[160:163], v[40:43], v[80:83]\n"
      "v_max3_u32 v23, v23, v40, v41\n"
      "v_mfma_f32_16x16x32_bf16 v[84:87], v[164:167], v[40:43], v[84:87]\n"
      "v_max3_u32 v23, v23, v42, v43\n"
      "v_mfma_f32_16x16x32_bf16 v[88:91], v[168:171], v[40:43], v[88:91]\n"
      "ds_write_b128 v20, v[72:75] offset:2048\n"
      "v_mfma_f32_16x16x32_bf16 v[92:95], v[172:175], v[40:43], v[92:95]\n"
      "s_cmp_lt_u32 s39, 4\n"
      "s_cbranch_scc0 Lnbe%=\n"
      "v_lshl_add_u32 v5, v4, 3, s84\n"
      "global_load_dwordx2 v[32:33], v5, s[14:15]\n"
      "global_load_dwordx2 v[34:35], v5, s[14:15] offset:32\n"
      "Lnbe%=:\n"
      "s_waitcnt vmcnt(4)\n"
      "v_mfma_f32_16x16x32_bf16 v[80:83], v[176:179], v[44:47], v[80:83]\n"
      "v_max3_u32 v23, v23, v44, v45\n"
      "v_mfma_f32_16x16x32_bf16 v[84:87], v[180:183], v[44:47], v[84:87]\n"
      "v_max3_u32 v23, v23, v46, v47\n"
      "v_mfma_f32_16x16x32_bf16 v[88:91], v[184:187], v[44:47], v[88:91]\n"
      "ds_write_b128 v21, v[76:79] offset:3072\n"
      "v_mfma_f32_16x16x32_bf16 v[92:95], v[188:191], v[44:47], v[92:95]\n"
      "buffer_load_dwordx4 v9, s[28:31], s63 offen offset:1024 lds\n"
      "s_waitcnt vmcnt(4)\n"
      "v_mfma_f32_16x16x32_bf16 v[80:83], v[192:195], v[48:51], v[80:83]\n"
      "v_max3_u32 v23, v23, v48, v49\n"
      "v_mfma_f32_16x16x32_bf16 v[84:87], v[196:199], v[48:51], v[84:87]\n"
      "v_max3_u32 v23, v23, v50, v51\n"
      "v_mfma_f32_16x16x32_bf16 v[88:91], v[200:203], v[48:51], v[88:91]\n"
      "v_mfma_f32_16x16x32_bf16 v[92:95], v[204:207], v[48:51], v[92:95]\n"
      "s_cmp_lt_u32 s39, 4\n"
      "s_cbranch_scc1 Lnb1%=\n"
      "s_waitcnt lgkmcnt(0)\n"
      "s_barrier\n"
      "Lnb1%=:\n"
      "buffer_load_dwordx4 v9, s[28:31], s64 offen offset:2048 lds\n"
      "s_waitcnt vmcnt(4)\n"
      "v_mfma_f32_16x16x32_bf16 v[80:83], v[208:211], v[52:55], v[80:83]\n"
      "v_max3_u32 v23, v23, v52, v53\n"
      "v_mfma_f32_16x16x32_bf16 v[84:87], v[212:215], v[52:55], v[84:87]\n"
      "v_max3_u32 v23, v23, v54, v55\n"
      "v_mfma_f32_16x16x32_bf16 v[88:91], v[216:219], v[52:55], v[88:91]\n"
      "v_mfma_f32_16x16x32_bf16 v[92:95], v[220:223], v[52:55], v[92:95]\n"
      "buffer_load_dwordx4 v9, s[28:31], s65 offen offset:3072 lds\n"
      "s_waitcnt vmcnt(4)\n"
      "v_mfma_f32_16x16x32_bf16 v[80:83], v[224:227], v[56:59], v[80:83]\n"
      "v_max3_u32 v23, v23, v56, v57\n"
      "v_mfma_f32_16x16x32_bf16 v[84:87], v[228:231], v[56:59], v[84:87]\n"
      "v_max3_u32 v23, v23, v58, v59\n"
      "v_mfma_f32_16x16x32_bf16 v[88:91], v[232:235], v[56:59], v[88:91]\n"
      "v_mfma_f32_16x16x32_bf16 v[92:95], v[236:239], v[56:59], v[92:95]\n"
      "s_add_u32 m0, s51, 65536\n"
      "s_lshl_b32 s52, s72, 17\n"
      "v_add_u32 v9, s52, v8\n"
      "buffer_load_dwordx4 v9, s[28:31], 0 offen offset:0 lds\n"
      "s_waitcnt vmcnt(4)\n"
      "v_mfma_f32_16x16x32_bf16 v[80:83], v[240:243], v[60:63], v[80:83]\n"
      "v_max3_u32 v23, v23, v60, v61\n"
      "v_mfma_f32_16x16x32_bf16 v[84:87], v[244:247], v[60:63], v[84:87]\n"
      "v_max3_u32 v23, v23, v62, v63\n"
      "v_mfma_f32_16x16x32_bf16 v[88:91], v[248:251], v[60:63], v[88:91]\n"
      "v_add_u32 v20, 65536, v20\n"
      "v_add_u32 v21, 65536, v21\n"
      "v_mfma_f32_16x16x32_bf16 v[92:95], v[252:255], v[60:63], v[92:95]\n"
      "buffer_load_dwordx4 v9, s[28:31], s63 offen offset:1024 lds\n"
      "v_cmp_eq_u32 vcc, -1, v23\n"
      "s_cbranch_vccnz LrG2%=\n"
      "LcG2%=:\n"
      "ds_write_b128 v20, v[80:83] offset:0\n"
      "ds_write_b128 v21, v[84:87] offset:1024\n"
      "ds_write_b128 v20, v[88:91] offset:2048\n"
      "s_nop 1\n"
      "ds_write_b128 v21, v[92:95] offset:3072\n"
      "buffer_load_dwordx4 v9, s[28:31], s64 offen offset:2048 lds\n"
      "buffer_load_dwordx4 v9, s[28:31], s65 offen offset:3072 lds\n"
      "v_add_u32 v23, 65536, v22\n"
      "s_cmp_lt_u32 s39, 4\n"
      "s_cbranch_scc0 Lne1%=\n"
      "s_and_b32 s41, s39, 1\n"
      "s_lshl_b32 s41, s41, 11\n"
      "s_cmp_lt_u32 s39, 2\n"
      "s_cselect_b32 s42, 32768, 0\n"
      "s_add_u32 s41, s41, s42\n"
      "v_add_u32 v5, s41, v1\n"
      "v_xor_b32 v6, 0x80, v5\n"
      "s_waitcnt vmcnt(7)\n"
      "v_lshlrev_b32 v40, 16, v32\n"
      "v_and_b32 v41, 0xffff0000, v32\n"
      "v_lshlrev_b32 v42, 16, v33\n"
      "v_and_b32 v43, 0xffff0000, v33\n"
      "v_lshlrev_b32 v44, 16, v34\n"
      "v_and_b32 v45, 0xffff0000, v34\n"
      "v_lshlrev_b32 v46, 16, v35\n"
      "v_and_b32 v47, 0xffff0000, v35\n"
      "s_waitcnt lgkmcnt(0)\n"
      "s_barrier\n"
      "ds_read_b128 v[128:131], v5 offset:0\n"
      "ds_read_b128 v[132:135], v6 offset:1024\n"
      "ds_read_b128 v[136:139], v5 offset:4096\n"
      "ds_read_b128 v[140:143], v6 offset:5120\n"
      "ds_read_b128 v[144:147], v5 offset:8192\n"
      "ds_read_b128 v[148:151], v6 offset:9216\n"
      "ds_read_b128 v[152:155], v5 offset:12288\n"
      "ds_read_b128 v[156:159], v6 offset:13312\n"
      "ds_read_b128 v[160:163], v5 offset:16384\n"
      "ds_read_b128 v[164:167], v6 offset:17408\n"
      "ds_read_b128 v[168:171], v5 offset:20480\n"
      "ds_read_b128 v[172:175], v6 offset:21504\n"
      "ds_read_b128 v[176:179], v5 offset:24576\n"
      "ds_read_b128 v[180:183], v6 offset:25600\n"
      "ds_read_b128 v[184:187], v5 offset:28672\n"
      "ds_read_b128 v[188:191], v6 offset:29696\n"
      "s_waitcnt lgkmcnt(14)\n"
      "v_pk_add_f32 v[40:41], v[40:41], v[128:129]\n"
      "v_pk_add_f32 v[42:43], v[42:43], v[130:131]\n"
      "v_pk_add_f32 v[44:45], v[44:45], v[132:133]\n"
      "v_pk_add_f32 v[46:47], v[46:47], v[134:135]\n"
      "s_waitcnt lgkmcnt(12)\n"
      "v_pk_add_f32 v[40:41], v[40:41], v[136:137]\n"
      "v_pk_add_f32 v[42:43], v[42:43], v[138:139]\n"
      "v_pk_add_f32 v[44:45], v[44:45], v[140:141]\n"
      "v_pk_add_f32 v[46:47], v[46:47], v[142:143]\n"
      "s_waitcnt lgkmcnt(10)\n"
      "v_pk_add_f32 v[40:41], v[40:41], v[144:145]\n"
      "v_pk_add_f32 v[42:43], v[42:43], v[146:147]\n"
      "v_pk_add_f32 v[44:45], v[44:45], v[148:149]\n"
      "v_pk_add_f32 v[46:47], v[46:47], v[150:151]\n"
      "s_waitcnt lgkmcnt(8)\n"
      "v_pk_add_f32 v[40:41], v[40:41], v[152:153]\n"
      "v_pk_add_f32 v[42:43], v[42:43], v[154:155]\n"
      "v_pk_add_f32 v[44:45], v[44:45], v[156:157]\n"
      "v_pk_add_f32 v[46:47], v[46:47], v[158:159]\n"
      "s_waitcnt lgkmcnt(6)\n"
      "v_pk_add_f32 v[40:41], v[40:41], v[160:161]\n"
      "v_pk_add_f32 v[42:43], v[42:43], v[162:163]\n"
      "v_pk_add_f32 v[44:45], v[44:45], v[164:165]\n"
      "v_pk_add_f32 v[46:47], v[46:47], v[166:167]\n"
      "s_waitcnt lgkmcnt(4)\n"
      "v_pk_add_f32 v[40:41], v[40:41], v[168:169]\n"
      "v_pk_add_f32 v[42:43], v[42:43], v[170:171]\n"
      "v_pk_add_f32 v[44:45], v[44:45], v[172:173]\n"
      "v_pk_add_f32 v[46:47], v[46:47], v[174:175]\n"
      "s_waitcnt lgkmcnt(2)\n"
      "v_pk_add_f32 v[40:41], v[40:41], v[176:177]\n"
      "v_pk_add_f32 v[42:43], v[42:43], v[178:179]\n"
      "v_pk_add_f32 v[44:45], v[44:45], v[180:181]\n"
      "v_pk_add_f32 v[46:47], v[46:47], v[182:183]\n"
      "s_waitcnt lgkmcnt(0)\n"
      "v_pk_add_f32 v[40:41], v[40:41], v[184:185]\n"
      "v_pk_add_f32 v[42:43], v[42:43], v[186:187]\n"
      "v_pk_add_f32 v[44:45], v[44:45], v[188:189]\n"
      "v_pk_add_f32 v[46:47], v[46:47], v[190:191]\n"
      "v_pk_mul_f32 v[128:129], v[40:41], v[40:41]\n"
      "v_pk_mul_f32 v[130:131], v[42:43], v[42:43]\n"
      "v_pk_mul_f32 v[132:133], v[44:45], v[44:45]\n"
      "v_pk_mul_f32 v[134:135], v[46:47], v[46:47]\n"
      "v_pk_fma_f32 v[128:129], v[128:129], v[16:17], v[16:17] op_sel:[0,0,1] op_sel_hi:[1,0,1]\n"
      "v_pk_fma_f32 v[130:131], v[130:131], v[16:17], v[16:17] op_sel:[0,0,1] op_sel_hi:[1,0,1]\n"
      "v_pk_fma_f32 v[132:133], v[132:133], v[16:17], v[16:17] op_sel:[0,0,1] op_sel_hi:[1,0,1]\n"
      "v_pk_fma_f32 v[134:135], v[134:135], v[16:17], v[16:17] op_sel:[0,0,1] op_sel_hi:[1,0,1]\n"
      "v_pk_mul_f32 v[128:129], v[128:129], v[40:41]\n"
      "v_pk_mul_f32 v[130:131], v[130:131], v[42:43]\n"
      "v_pk_mul_f32 v[132:133], v[132:133], v[44:45]\n"
      "v_pk_mul_f32 v[134:135], v[134:135], v[46:47]\n"
      "v_exp_f32 v128, v128\n"
      "v_exp_f32 v130, v130\n"
      "v_exp_f32 v132, v132\n"
      "v_exp_f32 v134, v134\n"
      "v_exp_f32 v129, v129\n"
      "v_exp_f32 v131, v131\n"
      "v_exp_f32 v133, v133\n"
      "v_exp_f32 v135, v135\n"
      "v_add_f32 v128, 1.0, v128\n"
      "v_add_f32 v130, 1.0, v130\n"
      "v_add_f32 v132, 1.0, v132\n"
      "v_add_f32 v134, 1.0, v134\n"
      "v_add_f32 v129, 1.0, v129\n"
      "v_add_f32 v131, 1.0, v131\n"
      "v_add_f32 v133, 1.0, v133\n"
      "v_add_f32 v135, 1.0, v135\n"
      "v_rcp_f32 v128, v128\n"
      "v_rcp_f32 v130, v130\n"
      "v_rcp_f32 v132, v132\n"
      "v_rcp_f32 v134, v134\n"
      "v_rcp_f32 v129, v129\n"
      "v_rcp_f32 v131, v131\n"
      "v_rcp_f32 v133, v133\n"
      "v_rcp_f32 v135, v135\n"
      "v_pk_mul_f32 v[40:41], v[40:41], v[128:129]\n"
      "v_pk_mul_f32 v[42:43], v[42:43], v[130:131]\n"
      "v_pk_mul_f32 v[44:45], v[44:45], v[132:133]\n"
      "v_pk_mul_f32 v[46:47], v[46:47], v[134:135]\n"
      "v_cvt_pk_bf16_f32 v32, v40, v41\n"
      "v_cvt_pk_bf16_f32 v33, v42, v43\n"
      "v_cvt_pk_bf16_f32 v34, v44, v45\n"
      "v_cvt_pk_bf16_f32 v35, v46, v47\n"
      "v_min_u32 v32, -2, v32\n"
      "v_min_u32 v33, -2, v33\n"
      "v_min_u32 v34, -2, v34\n"
      "v_min_u32 v35, -2, v35\n"
      "buffer_store_dwordx4 v[32:35], v1, s[20:23], s83 offen sc1\n"
      "Lne1%=:\n"
      "s_waitcnt lgkmcnt(0)\n"
      "s_barrier\n"
      "ds_read_b64 v[48:49], v23 offset:0\n"
      "ds_read_b64 v[50:51], v23 offset:4096\n"
      "ds_read_b64 v[52:53], v23 offset:8192\n"
      "ds_read_b64 v[54:55], v23 offset:12288\n"
      "ds_read_b64 v[56:57], v23 offset:16384\n"
      "ds_read_b64 v[58:59], v23 offset:20480\n"
      "ds_read_b64 v[60:61], v23 offset:24576\n"
      "ds_read_b64 v[62:63], v23 offset:28672\n"
      "s_lshr_b32 s41, s16, 3\n"
      "s_and_b32 s41, s41, 1\n"
      "s_or_b32 s41, s41, s39\n"
      "s_cbranch_scc1 Lnohf%=\n"
      "s_lshl2_add_u32 s41, s38, 36864\n"
      "v_mov_b32 v5, s40\n"
      "buffer_store_dword v5, off, s[20:23], s41 sc1\n"
      "Lnohf%=:\n"
      "s_waitcnt lgkmcnt(4)\n"
      "v_pk_add_f32 v[48:49], v[48:49], v[14:15]\n"
      "v_pk_add_f32 v[50:51], v[50:51], v[52:53]\n"
      "v_pk_add_f32 v[48:49], v[48:49], v[54:55]\n"
      "s_waitcnt lgkmcnt(0)\n"
      "v_pk_add_f32 v[50:51], v[50:51], v[56:57]\n"
      "v_pk_add_f32 v[48:49], v[48:49], v[58:59]\n"
      "v_pk_add_f32 v[50:51], v[50:51], v[60:61]\n"
      "v_pk_add_f32 v[48:49], v[48:49], v[62:63]\n"
      "v_pk_add_f32 v[48:49], v[48:49], v[50:51]\n"
      "v_pk_mul_f32 v[50:51], v[48:49], v[48:49]\n"
      "v_pk_fma_f32 v[50:51], v[50:51], v[16:17], v[16:17] op_sel:[0,0,1] op_sel_hi:[1,0,1]\n"
      "v_pk_mul_f32 v[50:51], v[50:51], v[48:49]\n"
      "v_exp_f32 v50, v50\n"
      "v_exp_f32 v51, v51\n"
      "v_add_f32 v50, 1.0, v50\n"
      "v_add_f32 v51, 1.0, v51\n"
      "v_rcp_f32 v50, v50\n"
      "v_rcp_f32 v51, v51\n"
      "s_nop 0\n"
      "v_pk_mul_f32 v[48:49], v[48:49], v[50:51]\n"
      "v_cvt_pk_bf16_f32 v48, v48, v49\n"
      "v_min_u32 v48, -2, v48\n"
      "buffer_store_dword v48, v2, s[20:23], s68 offen sc1\n"
      "s_waitcnt vmcnt(5)\n"
      "ds_read_b64_tr_b16 v[128:129], v10 offset:32768\n"
      "ds_read_b64_tr_b16 v[130:131], v10 offset:33792\n"
      "ds_read_b64_tr_b16 v[132:133], v11 offset:32768\n"
      "ds_read_b64_tr_b16 v[134:135], v11 offset:33792\n"
      "ds_read_b64_tr_b16 v[136:137], v12 offset:32768\n"
      "ds_read_b64_tr_b16 v[138:139], v12 offset:33792\n"
      "ds_read_b64_tr_b16 v[140:141], v13 offset:32768\n"
      "ds_read_b64_tr_b16 v[142:143], v13 offset:33792\n"
      "s_barrier\n"
      "s_add_u32 m0, s51, 0\n"
      "s_lshl_b32 s52, s73, 17\n"
      "v_add_u32 v9, s52, v8\n"
      "buffer_load_dwordx4 v9, s[28:31], 0 offen offset:0 lds\n"
      "buffer_load_dwordx4 v9, s[28:31], s63 offen offset:1024 lds\n"
      "buffer_load_dwordx4 v9, s[28:31], s64 offen offset:2048 lds\n"
      "buffer_load_dwordx4 v9, s[28:31], s65 offen offset:3072 lds\n"
      "s_waitcnt lgkmcnt(0)\n"
      "s_add_u32 m0, s51, 32768\n"
      "s_lshl_b32 s52, s74, 17\n"
      "v_add_u32 v9, s52, v8\n"
      "s_cmp_lt_u32 s39, 3\n"
      "s_cbranch_scc0 Lno5%=\n"
      "buffer_load_dwordx4 v9, s[28:31], 0 offen offset:0 lds\n"
      "buffer_load_dwordx4 v9, s[28:31], s63 offen offset:1024 lds\n"
      "buffer_load_dwordx4 v9, s[28:31], s64 offen offset:2048 lds\n"
      "buffer_load_dwordx4 v9, s[28:31], s65 offen offset:3072 lds\n"
      "Lno5%=:\n"
      "v_and_b32 v5, 7, v0\n"
      "v_lshrrev_b32 v6, 3, v0\n"
      "s_lshl_b32 s41, s54, 6\n"
      "v_add_u32 v18, s41, v6\n"
      "v_lshlrev_b32 v19, 1, v18\n"
      "s_lshl_b32 s41, s53, 7\n"
      "v_lshl_add_u32 v18, v18, 10, s41\n"
      "v_lshl_add_u32 v18, v5, 4, v18\n"
      "v_and_b32 v6, 15, v6\n"
      "v_and_b32 v16, 3, v5\n"
      "v_lshlrev_b32 v17, 2, v16\n"
      "v_xor_b32 v6, v6, v17\n"
      "v_lshlrev_b32 v22, 8, v16\n"
      "v_lshl_add_u32 v22, v6, 4, v22\n"
      "v_and_b32 v5, 4, v5\n"
      "v_lshl_add_u32 v22, v5, 14, v22\n"
      "s_lshr_b32 s43, s39, 1\n"
      "s_lshl_b32 s43, s43, 10\n"
      "v_add_u32 v22, s43, v22\n"
      "v_lshlrev_b32 v5, 6, v4\n"
      "v_xor_b32 v20, v20, v5\n"
      "v_subrev_u32 v20, 65536, v20\n"
      "s_mul_i32 s41, s53, 180224\n"
      "s_add_u32 s41, s41, 10551296\n"
      "s_mul_i32 s75, s69, 1024\n"
      "s_add_u32 s75, s75, s41\n"
      "s_mul_i32 s76, s70, 1024\n"
      "s_add_u32 s76, s76, s41\n"
      "s_mul_i32 s77, s71, 1024\n"
      "s_add_u32 s77, s77, s41\n"
      "s_mul_i32 s78, s72, 1024\n"
      "s_add_u32 s78, s78, s41\n"
      "s_mul_i32 s79, s73, 1024\n"
      "s_add_u32 s79, s79, s41\n"
      "s_mul_i32 s80, s74, 1024\n"
      "s_add_u32 s80, s80, s41\n"
      "v_and_b32 v2, 0x70, v1\n"
      "v_bfe_u32 v5, v0, 3, 1\n"
      "v_mul_u32_u24 v5, 128, v5\n"
      "v_add_u32 v2, v2, v5\n"
      "v_bfe_u32 v5, v0, 4, 2\n"
      "v_mul_u32_u24 v5, 256, v5\n"
      "v_add_u32 v2, v2, v5\n"
      "v_add_u32 v14, 45056, v2\n"
      "v_add_u32 v21, 90112, v2\n"
      "v_add_u32 v16, 135168, v2\n"
      "v_and_b32 v17, 0x1fc, v19\n"
      "s_mov_b32 s43, 36864\n"
      "s_waitcnt vmcnt(4)\n"
      "s_barrier\n"
      "buffer_load_dwordx4 v[32:35], v2, s[20:23], s75 offen sc1\n"
      "buffer_load_dwordx4 v[36:39], v2, s[20:23], s76 offen sc1\n"
      "buffer_load_dwordx4 v[40:43], v2, s[20:23], s77 offen sc1\n"
      "buffer_load_dwordx4 v[44:47], v2, s[20:23], s78 offen sc1\n"
      "buffer_load_dwordx4 v[48:51], v2, s[20:23], s79 offen sc1\n"
      "s_cmp_lt_u32 s39, 3\n"
      "s_cbranch_scc0 Lnu5%=\n"
      "buffer_load_dwordx4 v[52:55], v2, s[20:23], s80 offen sc1\n"
      "Lnu5%=:\n"
      "v_add_u32 v10, 0x8000, v10\n"
      "v_add_u32 v11, 0x8000, v11\n"
      "v_add_u32 v12, 0x8000, v12\n"
      "v_add_u32 v13, 0x8000, v13\n"
      "ds_read_b64_tr_b16 v[144:145], v10 offset:32768\n"
      "ds_read_b64_tr_b16 v[146:147], v10 offset:33792\n"
      "ds_read_b64_tr_b16 v[148:149], v11 offset:32768\n"
      "ds_read_b64_tr_b16 v[150:151], v11 offset:33792\n"
      "ds_read_b64_tr_b16 v[152:153], v12 offset:32768\n"
      "ds_read_b64_tr_b16 v[154:155], v12 offset:33792\n"
      "ds_read_b64_tr_b16 v[156:157], v13 offset:32768\n"
      "ds_read_b64_tr_b16 v[158:159], v13 offset:33792\n"
      "s_waitcnt vmcnt(5)\n"
      "v_add_u32 v10, 0xffff8000, v10\n"
      "v_add_u32 v11, 0xffff8000, v11\n"
      "v_add_u32 v12, 0xffff8000, v12\n"
      "v_add_u32 v13, 0xffff8000, v13\n"
      "ds_read_b64_tr_b16 v[160:161], v10 offset:0\n"
      "ds_read_b64_tr_b16 v[162:163], v10 offset:1024\n"
      "ds_read_b64_tr_b16 v[164:165], v11 offset:0\n"
      "ds_read_b64_tr_b16 v[166:167], v11 offset:1024\n"
      "ds_read_b64_tr_b16 v[168:169], v12 offset:0\n"
      "ds_read_b64_tr_b16 v[170:171], v12 offset:1024\n"
      "ds_read_b64_tr_b16 v[172:173], v13 offset:0\n"
      "ds_read_b64_tr_b16 v[174:175], v13 offset:1024\n"
      "s_waitcnt vmcnt(5)\n"
      "ds_read_b64_tr_b16 v[176:177], v10 offset:32768\n"
      "ds_read_b64_tr_b16 v[178:179], v10 offset:33792\n"
      "ds_read_b64_tr_b16 v[180:181], v11 offset:32768\n"
      "ds_read_b64_tr_b16 v[182:183], v11 offset:33792\n"
      "ds_read_b64_tr_b16 v[184:185], v12 offset:32768\n"
      "ds_read_b64_tr_b16 v[186:187], v12 offset:33792\n"
      "ds_read_b64_tr_b16 v[188:189], v13 offset:32768\n"
      "ds_read_b64_tr_b16 v[190:191], v13 offset:33792\n"
      "s_waitcnt lgkmcnt(0)\n"
      "s_waitcnt vmcnt(4)\n"
      "buffer_load_dwordx4 v[56:59], v14, s[20:23], s75 offen sc1\n"
      "LcQ0%=:\n"
      "v_max3_u32 v23, v32, v33, v34\n"
      "v_max_u32 v23, v23, v35\n"
      "v_cmp_eq_u32 vcc, -1, v23\n"
      "s_cbranch_vccnz LrQ0%=\n"
      "v_mfma_f32_16x16x32_bf16 v[80:83], v[32:35], v[112:115], 0\n"
      "v_mfma_f32_16x16x32_bf16 v[84:87], v[32:35], v[116:119], 0\n"
      "v_mfma_f32_16x16x32_bf16 v[88:91], v[32:35], v[120:123], 0\n"
      "v_mfma_f32_16x16x32_bf16 v[92:95], v[32:35], v[124:127], 0\n"
      "s_waitcnt vmcnt(4)\n"
      "buffer_load_dwordx4 v[60:63], v14, s[20:23], s76 offen sc1\n"
      "LcQ1%=:\n"
      "v_max3_u32 v23, v36, v37, v38\n"
      "v_max_u32 v23, v23, v39\n"
      "v_cmp_eq_u32 vcc, -1, v23\n"
      "s_cbranch_vccnz LrQ1%=\n"
      "v_mfma_f32_16x16x32_bf16 v[80:83], v[36:39], v[96:99], v[80:83]\n"
      "v_mfma_f32_16x16x32_bf16 v[84:87], v[36:39], v[100:103], v[84:87]\n"
      "v_mfma_f32_16x16x32_bf16 v[88:91], v[36:39], v[104:107], v[88:91]\n"
      "v_mfma_f32_16x16x32_bf16 v[92:95], v[36:39], v[108:111], v[92:95]\n"
      "s_waitcnt vmcnt(4)\n"
      "buffer_load_dwordx4 v[224:227], v14, s[20:23], s77 offen sc1\n"
      "LcQ2%=:\n"
      "v_max3_u32 v23, v40, v41, v42\n"
      "v_max_u32 v23, v23, v43\n"
      "v_cmp_eq_u32 vcc, -1, v23\n"
      "s_cbranch_vccnz LrQ2%=\n"
      "v_mfma_f32_16x16x32_bf16 v[80:83], v[40:43], v[128:131], v[80:83]\n"
      "v_mfma_f32_16x16x32_bf16 v[84:87], v[40:43], v[132:135], v[84:87]\n"
      "v_mfma_f32_16x16x32_bf16 v[88:91], v[40:43], v[136:139], v[88:91]\n"
      "v_mfma_f32_16x16x32_bf16 v[92:95], v[40:43], v[140:143], v[92:95]\n"
      "s_waitcnt vmcnt(4)\n"
      "buffer_load_dwordx4 v[228:231], v14, s[20:23], s78 offen sc1\n"
      "LcQ3%=:\n"
      "v_max3_u32 v23, v44, v45, v46\n"
      "v_max_u32 v23, v23, v47\n"
      "v_cmp_eq_u32 vcc, -1, v23\n"
      "s_cbranch_vccnz LrQ3%=\n"
      "v_mfma_f32_16x16x32_bf16 v[80:83], v[44:47], v[144:147], v[80:83]\n"
      "v_mfma_f32_16x16x32_bf16 v[84:87], v[44:47], v[148:151], v[84:87]\n"
      "v_mfma_f32_16x16x32_bf16 v[88:91], v[44:47], v[152:155], v[88:91]\n"
      "v_mfma_f32_16x16x32_bf16 v[92:95], v[44:47], v[156:159], v[92:95]\n"
      "s_waitcnt vmcnt(4)\n"
      "buffer_load_dwordx4 v[232:235], v14, s[20:23], s79 offen sc1\n"
      "LcQ4%=:\n"
      "v_max3_u32 v23, v48, v49, v50\n"
      "v_max_u32 v23, v23, v51\n"
      "v_cmp_eq_u32 vcc, -1, v23\n"
      "s_cbranch_vccnz LrQ4%=\n"
      "v_mfma_f32_16x16x32_bf16 v[80:83], v[48:51], v[160:163], v[80:83]\n"
      "v_mfma_f32_16x16x32_bf16 v[84:87], v[48:51], v[164:167], v[84:87]\n"
      "v_mfma_f32_16x16x32_bf16 v[88:91], v[48:51], v[168:171], v[88:91]\n"
      "v_mfma_f32_16x16x32_bf16 v[92:95], v[48:51], v[172:175], v[92:95]\n"
      "s_waitcnt vmcnt(5)\n"
      "s_cmp_lt_u32 s39, 3\n"
      "s_cbranch_scc0 Lnu11%=\n"
      "buffer_load_dwordx4 v[236:239], v14, s[20:23], s80 offen sc1\n"
      "Lnu11%=:\n"
      "s_cmp_lt_u32 s39, 3\n"
      "s_cbranch_scc0 Lsk0%=\n"
      "LcQ5%=:\n"
      "v_max3_u32 v23, v52, v53, v54\n"
      "v_max_u32 v23, v23, v55\n"
      "v_cmp_eq_u32 vcc, -1, v23\n"
      "s_cbranch_vccnz LrQ5%=\n"
      "v_mfma_f32_16x16x32_bf16 v[80:83], v[52:55], v[176:179], v[80:83]\n"
      "v_mfma_f32_16x16x32_bf16 v[84:87], v[52:55], v[180:183], v[84:87]\n"
      "v_mfma_f32_16x16x32_bf16 v[88:91], v[52:55], v[184:187], v[88:91]\n"
      "v_mfma_f32_16x16x32_bf16 v[92:95], v[52:55], v[188:191], v[92:95]\n"
      "Lsk0%=:\n"
      "s_waitcnt vmcnt(4)\n"
      "buffer_load_dwordx4 v[32:35], v21, s[20:23], s75 offen sc1\n"
      "v_mfma_f32_16x16x32_bf16 v[64:67], v[56:59], v[112:115], 0\n"
      "v_max_u32 v23, v56, v57\n"
      "v_mfma_f32_16x16x32_bf16 v[68:71], v[56:59], v[116:119], 0\n"
      "v_max3_u32 v23, v23, v58, v59\n"
      "v_mfma_f32_16x16x32_bf16 v[72:75], v[56:59], v[120:123], 0\n"
      "ds_write_b128 v20, v[80:83] offset:0\n"
      "v_mfma_f32_16x16x32_bf16 v[76:79], v[56:59], v[124:127], 0\n"
      "s_waitcnt vmcnt(4)\n"
      "buffer_load_dwordx4 v[36:39], v21, s[20:23], s76 offen sc1\n"
      "v_mfma_f32_16x16x32_bf16 v[64:67], v[60:63], v[96:99], v[64:67]\n"
      "v_max3_u32 v23, v23, v60, v61\n"
      "v_mfma_f32_16x16x32_bf16 v[68:71], v[60:63], v[100:103], v[68:71]\n"
      "v_max3_u32 v23, v23, v62, v63\n"
      "v_mfma_f32_16x16x32_bf16 v[72:75], v[60:63], v[104:107], v[72:75]\n"
      "ds_write_b128 v20, v[84:87] offset:1024\n"
      "v_mfma_f32_16x16x32_bf16 v[76:79], v[60:63], v[108:111], v[76:79]\n"
      "s_waitcnt vmcnt(4)\n"
      "buffer_load_dwordx4 v[40:43], v21, s[20:23], s77 offen sc1\n"
      "v_mfma_f32_16x16x32_bf16 v[64:67], v[224:227], v[128:131], v[64:67]\n"
      "v_max3_u32 v23, v23, v224, v225\n"
      "v_mfma_f32_16x16x32_bf16 v[68:71], v[224:227], v[132:135], v[68:71]\n"
      "v_max3_u32 v23, v23, v226, v227\n"
      "v_mfma_f32_16x16x32_bf16 v[72:75], v[224:227], v[136:139], v[72:75]\n"
      "ds_write_b128 v20, v[88:91] offset:2048\n"
      "v_mfma_f32_16x16x32_bf16 v[76:79], v[224:227], v[140:143], v[76:79]\n"
      "s_waitcnt vmcnt(4)\n"
      "buffer_load_dwordx4 v[44:47], v21, s[20:23], s78 offen sc1\n"
      "v_mfma_f32_16x16x32_bf16 v[64:67], v[228:231], v[144:147], v[64:67]\n"
      "v_max3_u32 v23, v23, v228, v229\n"
      "v_mfma_f32_16x16x32_bf16 v[68:71], v[228:231], v[148:151], v[68:71]\n"
      "v_max3_u32 v23, v23, v230, v231\n"
      "v_mfma_f32_16x16x32_bf16 v[72:75], v[228:231], v[152:155], v[72:75]\n"
      "ds_write_b128 v20, v[92:95] offset:3072\n"
      "v_mfma_f32_16x16x32_bf16 v[76:79], v[228:231], v[156:159], v[76:79]\n"
      "s_waitcnt vmcnt(4)\n"
      "buffer_load_dwordx4 v[48:51], v21, s[20:23], s79 offen sc1\n"
      "v_mfma_f32_16x16x32_bf16 v[64:67], v[232:235], v[160:163], v[64:67]\n"
      "v_max3_u32 v23, v23, v232, v233\n"
      "v_mfma_f32_16x16x32_bf16 v[68:71], v[232:235], v[164:167], v[68:71]\n"
      "v_max3_u32 v23, v23, v234, v235\n"
      "v_mfma_f32_16x16x32_bf16 v[72:75], v[232:235], v[168:171], v[72:75]\n"
      "v_mfma_f32_16x16x32_bf16 v[76:79], v[232:235], v[172:175], v[76:79]\n"
      "s_waitcnt vmcnt(5)\n"
      "s_cmp_lt_u32 s39, 3\n"
      "s_cbranch_scc0 Lnu17%=\n"
      "buffer_load_dwordx4 v[52:55], v21, s[20:23], s80 offen sc1\n"
      "Lnu17%=:\n"
      "buffer_load_dword v15, v17, s[20:23], s43 offen sc1\n"
      "s_cmp_lt_u32 s39, 3\n"
      "s_cbranch_scc0 Lsk1%=\n"
      "v_mfma_f32_16x16x32_bf16 v[64:67], v[236:239], v[176:179], v[64:67]\n"
      "v_max3_u32 v23, v23, v236, v237\n"
      "v_mfma_f32_16x16x32_bf16 v[68:71], v[236:239], v[180:183], v[68:71]\n"
      "v_max3_u32 v23, v23, v238, v239\n"
      "v_mfma_f32_16x16x32_bf16 v[72:75], v[236:239], v[184:187], v[72:75]\n"
      "v_mfma_f32_16x16x32_bf16 v[76:79], v[236:239], v[188:191], v[76:79]\n"
      "Lsk1%=:\n"
      "v_cmp_eq_u32 vcc, -1, v23\n"
      "s_cbranch_vccnz LrE1%=\n"
      "LcE1%=:\n"
      "s_waitcnt vmcnt(5)\n"
      "buffer_load_dwordx4 v[56:59], v16, s[20:23], s75 offen sc1\n"
      "v_mfma_f32_16x16x32_bf16 v[80:83], v[32:35], v[112:115], 0\n"
      "v_max_u32 v23, v32, v33\n"
      "v_mfma_f32_16x16x32_bf16 v[84:87], v[32:35], v[116:119], 0\n"
      "v_max3_u32 v23, v23, v34, v35\n"
      "v_mfma_f32_16x16x32_bf16 v[88:91], v[32:35], v[120:123], 0\n"
      "ds_write_b128 v20, v[64:67] offset:32768\n"
      "v_mfma_f32_16x16x32_bf16 v[92:95], v[32:35], v[124:127], 0\n"
      "s_waitcnt vmcnt(5)\n"
      "buffer_load_dwordx4 v[60:63], v16, s[20:23], s76 offen sc1\n"
      "v_mfma_f32_16x16x32_bf16 v[80:83], v[36:39], v[96:99], v[80:83]\n"
      "v_max3_u32 v23, v23, v36, v37\n"
      "v_mfma_f32_16x16x32_bf16 v[84:87], v[36:39], v[100:103], v[84:87]\n"
      "v_max3_u32 v23, v23, v38, v39\n"
      "v_mfma_f32_16x16x32_bf16 v[88:91], v[36:39], v[104:107], v[88:91]\n"
      "ds_write_b128 v20, v[68:71] offset:33792\n"
      "v_mfma_f32_16x16x32_bf16 v[92:95], v[36:39], v[108:111], v[92:95]\n"
      "s_waitcnt vmcnt(5)\n"
      "buffer_load_dwordx4 v[224:227], v16, s[20:23], s77 offen sc1\n"
      "v_mfma_f32_16x16x32_bf16 v[80:83], v[40:43], v[128:131], v[80:83]\n"
      "v_max3_u32 v23, v23, v40, v41\n"
      "v_mfma_f32_16x16x32_bf16 v[84:87], v[40:43], v[132:135], v[84:87]\n"
      "v_max3_u32 v23, v23, v42, v43\n"
      "v_mfma_f32_16x16x32_bf16 v[88:91], v[40:43], v[136:139], v[88:91]\n"
      "ds_write_b128 v20, v[72:75] offset:34816\n"
      "v_mfma_f32_16x16x32_bf16 v[92:95], v[40:43], v[140:143], v[92:95]\n"
      "s_waitcnt vmcnt(5)\n"
      "buffer_load_dwordx4 v[228:231], v16, s[20:23], s78 offen sc1\n"
      "v_mfma_f32_16x16x32_bf16 v[80:83], v[44:47], v[144:147], v[80:83]\n"
      "v_max3_u32 v23, v23, v44, v45\n"
      "v_mfma_f32_16x16x32_bf16 v[84:87], v[44:47], v[148:151], v[84:87]\n"
      "v_max3_u32 v23, v23, v46, v47\n"
      "v_mfma_f32_16x16x32_bf16 v[88:91], v[44:47], v[152:155], v[88:91]\n"
      "ds_write_b128 v20, v[76:79] offset:35840\n"
      "v_mfma_f32_16x16x32_bf16 v[92:95], v[44:47], v[156:159], v[92:95]\n"
      "s_waitcnt vmcnt(5)\n"
      "buffer_load_dwordx4 v[232:235], v16, s[20:23], s79 offen sc1\n"
      "v_mfma_f32_16x16x32_bf16 v[80:83], v[48:51], v[160:163], v[80:83]\n"
      "v_max3_u32 v23, v23, v48, v49\n"
      "v_mfma_f32_16x16x32_bf16 v[84:87], v[48:51], v[164:167], v[84:87]\n"
      "v_max3_u32 v23, v23, v50, v51\n"
      "v_mfma_f32_16x16x32_bf16 v[88:91], v[48:51], v[168:171], v[88:91]\n"
      "v_add_u32 v20, 65536, v20\n"
      "v_mfma_f32_16x16x32_bf16 v[92:95], v[48:51], v[172:175], v[92:95]\n"
      "s_waitcnt vmcnt(6)\n"
      "s_cmp_lt_u32 s39, 3\n"
      "s_cbranch_scc0 Lnu23%=\n"
      "buffer_load_dwordx4 v[236:239], v16, s[20:23], s80 offen sc1\n"
      "Lnu23%=:\n"
      "s_cmp_lt_u32 s39, 3\n"
      "s_cbranch_scc0 Lsk2%=\n"
      "v_mfma_f32_16x16x32_bf16 v[80:83], v[52:55], v[176:179], v[80:83]\n"
      "v_max3_u32 v23, v23, v52, v53\n"
      "v_mfma_f32_16x16x32_bf16 v[84:87], v[52:55], v[180:183], v[84:87]\n"
      "v_max3_u32 v23, v23, v54, v55\n"
      "v_mfma_f32_16x16x32_bf16 v[88:91], v[52:55], v[184:187], v[88:91]\n"
      "v_mfma_f32_16x16x32_bf16 v[92:95], v[52:55], v[188:191], v[92:95]\n"
      "Lsk2%=:\n"
      "v_cmp_eq_u32 vcc, -1, v23\n"
      "s_cbranch_vccnz LrE2%=\n"
      "LcE2%=:\n"
      "s_waitcnt vmcnt(4)\n"
      "LcF%=:\n"
      "v_cmp_ne_u32 vcc, s40, v15\n"
      "s_cbranch_vccnz LrF%=\n"
      "buffer_load_dwordx4 v[244:247], v18, s[20:23], s57 offen sc1\n"
      "global_load_ushort v19, v19, s[32:33]\n"
      "v_mfma_f32_16x16x32_bf16 v[64:67], v[56:59], v[112:115], 0\n"
      "v_max_u32 v23, v56, v57\n"
      "v_mfma_f32_16x16x32_bf16 v[68:71], v[56:59], v[116:119], 0\n"
      "v_max3_u32 v23, v23, v58, v59\n"
      "v_mfma_f32_16x16x32_bf16 v[72:75], v[56:59], v[120:123], 0\n"
      "ds_write_b128 v20, v[80:83] offset:0\n"
      "v_mfma_f32_16x16x32_bf16 v[76:79], v[56:59], v[124:127], 0\n"
      "s_waitcnt vmcnt(5)\n"
      "v_mfma_f32_16x16x32_bf16 v[64:67], v[60:63], v[96:99], v[64:67]\n"
      "v_max3_u32 v23, v23, v60, v61\n"
      "v_mfma_f32_16x16x32_bf16 v[68:71], v[60:63], v[100:103], v[68:71]\n"
      "v_max3_u32 v23, v23, v62, v63\n"
      "v_mfma_f32_16x16x32_bf16 v[72:75], v[60:63], v[104:107], v[72:75]\n"
      "ds_write_b128 v20, v[84:87] offset:1024\n"
      "v_mfma_f32_16x16x32_bf16 v[76:79], v[60:63], v[108:111], v[76:79]\n"
      "s_waitcnt vmcnt(4)\n"
      "v_mfma_f32_16x16x32_bf16 v[64:67], v[224:227], v[128:131], v[64:67]\n"
      "v_max3_u32 v23, v23, v224, v225\n"
      "v_mfma_f32_16x16x32_bf16 v[68:71], v[224:227], v[132:135], v[68:71]\n"
      "v_max3_u32 v23, v23, v226, v227\n"
      "v_mfma_f32_16x16x32_bf16 v[72:75], v[224:227], v[136:139], v[72:75]\n"
      "ds_write_b128 v20, v[88:91] offset:2048\n"
      "v_mfma_f32_16x16x32_bf16 v[76:79], v[224:227], v[140:143], v[76:79]\n"
      "s_waitcnt vmcnt(3)\n"
      "v_mfma_f32_16x16x32_bf16 v[64:67], v[228:231], v[144:147], v[64:67]\n"
      "v_max3_u32 v23, v23, v228, v229\n"
      "v_mfma_f32_16x16x32_bf16 v[68:71], v[228:231], v[148:151], v[68:71]\n"
      "v_max3_u32 v23, v23, v230, v231\n"
      "v_mfma_f32_16x16x32_bf16 v[72:75], v[228:231], v[152:155], v[72:75]\n"
      "ds_write_b128 v20, v[92:95] offset:3072\n"
      "v_mfma_f32_16x16x32_bf16 v[76:79], v[228:231], v[156:159], v[76:79]\n"
      "s_waitcnt vmcnt(2)\n"
      "v_mfma_f32_16x16x32_bf16 v[64:67], v[232:235], v[160:163], v[64:67]\n"
      "v_max3_u32 v23, v23, v232, v233\n"
      "v_mfma_f32_16x16x32_bf16 v[68:71], v[232:235], v[164:167], v[68:71]\n"
      "v_max3_u32 v23, v23, v234, v235\n"
      "v_mfma_f32_16x16x32_bf16 v[72:75], v[232:235], v[168:171], v[72:75]\n"
      "v_mfma_f32_16x16x32_bf16 v[76:79], v[232:235], v[172:175], v[76:79]\n"
      "s_waitcnt vmcnt(2)\n"
      "s_cmp_lt_u32 s39, 3\n"
      "s_cbranch_scc0 Lsk3%=\n"
      "v_mfma_f32_16x16x32_bf16 v[64:67], v[236:239], v[176:179], v[64:67]\n"
      "v_max3_u32 v23, v23, v236, v237\n"
      "v_mfma_f32_16x16x32_bf16 v[68:71], v[236:239], v[180:183], v[68:71]\n"
      "v_max3_u32 v23, v23, v238, v239\n"
      "v_mfma_f32_16x16x32_bf16 v[72:75], v[236:239], v[184:187], v[72:75]\n"
      "v_mfma_f32_16x16x32_bf16 v[76:79], v[236:239], v[188:191], v[76:79]\n"
      "Lsk3%=:\n"
      "v_cmp_eq_u32 vcc, -1, v23\n"
      "s_cbranch_vccnz LrE3%=\n"
      "LcE3%=:\n"
      "s_nop 0\n"
      "ds_write_b128 v20, v[64:67] offset:32768\n"
      "s_nop 0\n"
      "ds_write_b128 v20, v[68:71] offset:33792\n"
      "s_nop 0\n"
      "ds_write_b128 v20, v[72:75] offset:34816\n"
      "ds_write_b128 v20, v[76:79] offset:35840\n"
      "s_waitcnt vmcnt(0)\n"
      "v_lshlrev_b32 v19, 16, v19\n"
      "v_lshlrev_b32 v248, 16, v244\n"
      "v_and_b32 v249, 0xffff0000, v244\n"
      "v_lshlrev_b32 v250, 16, v245\n"
      "v_and_b32 v251, 0xffff0000, v245\n"
      "v_lshlrev_b32 v252, 16, v246\n"
      "v_and_b32 v253, 0xffff0000, v246\n"
      "v_lshlrev_b32 v254, 16, v247\n"
      "v_and_b32 v255, 0xffff0000, v247\n"
      "v_add_f32 v248, v248, v19\n"
      "v_add_f32 v249, v249, v19\n"
      "v_add_f32 v250, v250, v19\n"
      "v_add_f32 v251, v251, v19\n"
      "v_add_f32 v252, v252, v19\n"
      "v_add_f32 v253, v253, v19\n"
      "v_add_f32 v254, v254, v19\n"
      "v_add_f32 v255, v255, v19\n"
      "s_waitcnt lgkmcnt(0)\n"
      "s_barrier\n"
      "ds_read_b128 v[128:131], v22 offset:0\n"
      "ds_read_b128 v[132:135], v22 offset:32768\n"
      "ds_read_b128 v[136:139], v22 offset:4096\n"
      "ds_read_b128 v[140:143], v22 offset:36864\n"
      "ds_read_b128 v[144:147], v22 offset:8192\n"
      "ds_read_b128 v[148:151], v22 offset:40960\n"
      "ds_read_b128 v[152:155], v22 offset:12288\n"
      "ds_read_b128 v[156:159], v22 offset:45056\n"
      "ds_read_b128 v[160:163], v22 offset:16384\n"
      "ds_read_b128 v[164:167], v22 offset:49152\n"
      "ds_read_b128 v[168:171], v22 offset:20480\n"
      "ds_read_b128 v[172:175], v22 offset:53248\n"
      "ds_read_b128 v[176:179], v22 offset:24576\n"
      "ds_read_b128 v[180:183], v22 offset:57344\n"
      "ds_read_b128 v[184:187], v22 offset:28672\n"
      "ds_read_b128 v[188:191], v22 offset:61440\n"
      "s_waitcnt lgkmcnt(14)\n"
      "v_pk_add_f32 v[248:249], v[248:249], v[128:129]\n"
      "v_pk_add_f32 v[250:251], v[250:251], v[130:131]\n"
      "v_pk_add_f32 v[252:253], v[252:253], v[132:133]\n"
      "v_pk_add_f32 v[254:255], v[254:255], v[134:135]\n"
      "s_waitcnt lgkmcnt(12)\n"
      "v_pk_add_f32 v[248:249], v[248:249], v[136:137]\n"
      "v_pk_add_f32 v[250:251], v[250:251], v[138:139]\n"
      "v_pk_add_f32 v[252:253], v[252:253], v[140:141]\n"
      "v_pk_add_f32 v[254:255], v[254:255], v[142:143]\n"
      "s_waitcnt lgkmcnt(10)\n"
      "v_pk_add_f32 v[248:249], v[248:249], v[144:145]\n"
      "v_pk_add_f32 v[250:251], v[250:251], v[146:147]\n"
      "v_pk_add_f32 v[252:253], v[252:253], v[148:149]\n"
      "v_pk_add_f32 v[254:255], v[254:255], v[150:151]\n"
      "s_waitcnt lgkmcnt(8)\n"
      "v_pk_add_f32 v[248:249], v[248:249], v[152:153]\n"
      "v_pk_add_f32 v[250:251], v[250:251], v[154:155]\n"
      "v_pk_add_f32 v[252:253], v[252:253], v[156:157]\n"
      "v_pk_add_f32 v[254:255], v[254:255], v[158:159]\n"
      "s_waitcnt lgkmcnt(6)\n"
      "v_pk_add_f32 v[248:249], v[248:249], v[160:161]\n"
      "v_pk_add_f32 v[250:251], v[250:251], v[162:163]\n"
      "v_pk_add_f32 v[252:253], v[252:253], v[164:165]\n"
      "v_pk_add_f32 v[254:255], v[254:255], v[166:167]\n"
      "s_waitcnt lgkmcnt(4)\n"
      "v_pk_add_f32 v[248:249], v[248:249], v[168:169]\n"
      "v_pk_add_f32 v[250:251], v[250:251], v[170:171]\n"
      "v_pk_add_f32 v[252:253], v[252:253], v[172:173]\n"
      "v_pk_add_f32 v[254:255], v[254:255], v[174:175]\n"
      "s_waitcnt lgkmcnt(2)\n"
      "v_pk_add_f32 v[248:249], v[248:249], v[176:177]\n"
      "v_pk_add_f32 v[250:251], v[250:251], v[178:179]\n"
      "v_pk_add_f32 v[252:253], v[252:253], v[180:181]\n"
      "v_pk_add_f32 v[254:255], v[254:255], v[182:183]\n"
      "s_waitcnt lgkmcnt(0)\n"
      "v_pk_add_f32 v[248:249], v[248:249], v[184:185]\n"
      "v_pk_add_f32 v[250:251], v[250:251], v[186:187]\n"
      "v_pk_add_f32 v[252:253], v[252:253], v[188:189]\n"
      "v_pk_add_f32 v[254:255], v[254:255], v[190:191]\n"
      "v_cvt_pk_bf16_f32 v240, v248, v249\n"
      "v_cvt_pk_bf16_f32 v241, v250, v251\n"
      "v_cvt_pk_bf16_f32 v242, v252, v253\n"
      "v_cvt_pk_bf16_f32 v243, v254, v255\n"
      "global_store_dwordx4 v18, v[240:243], s[34:35] sc1\n"
      "Lend%=:\n"
      "s_endpgm\n"
      "Lrole2%=:\n"
      "s_and_b32 s41, s38, 15\n"
      "s_lshl3_add_u32 s41, s41, s39\n"
      "s_lshl4_add_u32 s41, s41, s36\n"
      "s_mul_i32 s41, s41, 514\n"
      "s_and_b32 s41, s41, -4\n"
      "v_lshrrev_b32 v5, 1, v4\n"
      "v_mul_u32_u24 v16, 4112, v5\n"
      "v_and_b32 v5, 31, v0\n"
      "v_mul_u32_u24 v5, 20, v5\n"
      "v_min_u32 v5, 0x200, v5\n"
      "v_add3_u32 v16, v16, v5, s41\n"
      "s_mul_i32 s76, s38, 16384\n"
      "s_add_u32 s75, s76, 4259840\n"
      "s_add_u32 s76, s76, 4268032\n"
      "s_waitcnt lgkmcnt(0)\n"
      "s_load_dword s17, s[8:9], s41\n"
      "global_load_dword v100, v16, s[6:7]\n"
      "global_load_dword v104, v16, s[8:9]\n"
      "s_mov_b64 s[20:21], s[4:5]\n"
      "s_load_dword s40, s[4:5], 0x0\n"
      "s_load_dwordx4 s[32:35], s[0:1], 0x38\n"
      "s_mov_b64 s[24:25], s[10:11]\n"
      "s_and_b32 s41, s36, 3\n"
      "s_lshl_b32 s41, s41, 3\n"
      "s_lshr_b32 s42, 0x5d3d1d00, s41\n"
      "s_lshr_b32 s43, 0x1c20201d, s41\n"
      "s_and_b32 s42, s42, 0xff\n"
      "s_and_b32 s43, s43, 0xff\n"
      "s_cmp_lt_u32 s37, s43\n"
      "s_cselect_b32 s50, -1, 0\n"
      "s_cmp_lt_u32 s36, 4\n"
      "s_cselect_b32 s41, 0, 121\n"
      "s_add_u32 s42, s42, s41\n"
      "s_add_u32 s42, s42, s37\n"
      "s_mul_i32 s48, s42, 373\n"
      "s_lshr_b32 s48, s48, 12\n"
      "s_mul_i32 s41, s48, 11\n"
      "s_sub_u32 s49, s42, s41\n"
      "s_mul_i32 s59, s49, 0\n"
      "v_lshlrev_b32 v7, 5, v4\n"
      "s_mul_i32 s41, s39, 88064\n"
      "v_xor_b32 v8, v1, v7\n"
      "s_lshl_b32 s42, s48, 7\n"
      "v_and_b32 v8, 0x70, v8\n"
      "s_add_u32 s41, s41, s42\n"
      "v_bfe_u32 v7, v0, 3, 2\n"
      "s_lshl_b32 s51, s39, 12\n"
      "v_lshrrev_b32 v9, 1, v4\n"
      "s_add_u32 s51, s51, 65536\n"
      "v_lshl_add_u32 v7, v7, 8, v9\n"
      "s_and_b32 s26, s50, 5636096\n"
      "v_mul_u32_u24 v7, 2752, v7\n"
      "s_mov_b32 s27, 0x20000\n"
      "v_add3_u32 v7, v8, v7, s41\n"
      "s_cmp_eq_u32 s48, 21\n"
      "s_cselect_b32 s41, 64, 0\n"
      "v_and_b32 v9, s41, v8\n"
      "v_lshl_add_u32 v7, v9, 24, v7\n"
      "s_mov_b32 s60, 2817024\n"
      "s_mov_b32 s61, 3456\n"
      "s_mov_b32 s62, 2820480\n"
      "s_and_b32 s41, s38, 7\n"
      "s_lshl3_add_u32 s41, s41, s39\n"
      "s_min_u32 s41, s41, 42\n"
      "s_lshl_b32 s41, s41, 17\n"
      "s_bitcmp1_b32 s38, 3\n"
      "s_cselect_b64 s[46:47], s[12:13], s[10:11]\n"
      "s_load_dword s17, s[46:47], s41\n"
      "s_mov_b64 s[28:29], s[12:13]\n"
      "s_mov_b32 s30, 5636096\n"
      "s_mov_b32 s31, 0x20000\n"
      "s_mov_b32 s63, 64512\n"
      "s_mov_b32 s64, 30720\n"
      "s_mov_b32 s65, 95232\n"
      "s_and_b32 s53, s37, 7\n"
      "s_lshr_b32 s54, s38, 2\n"
      "s_mul_i32 s41, s39, 6\n"
      "s_mul_i32 s42, s39, 5\n"
      "s_add_u32 s42, s42, 3\n"
      "s_cmp_lt_u32 s39, 3\n"
      "s_cselect_b32 s41, s41, s42\n"
      "s_cselect_b32 s42, 6, 5\n"
      "s_sub_u32 s43, s53, s42\n"
      "s_cselect_b32 s43, s53, s43\n"
      "s_add_u32 s69, s41, s43\n"
      "s_add_u32 s43, s43, 1\n"
      "s_cmp_eq_u32 s43, s42\n"
      "s_cselect_b32 s43, 0, s43\n"
      "s_add_u32 s70, s41, s43\n"
      "s_add_u32 s43, s43, 1\n"
      "s_cmp_eq_u32 s43, s42\n"
      "s_cselect_b32 s43, 0, s43\n"
      "s_add_u32 s71, s41, s43\n"
      "s_add_u32 s43, s43, 1\n"
      "s_cmp_eq_u32 s43, s42\n"
      "s_cselect_b32 s43, 0, s43\n"
      "s_add_u32 s72, s41, s43\n"
      "s_add_u32 s43, s43, 1\n"
      "s_cmp_eq_u32 s43, s42\n"
      "s_cselect_b32 s43, 0, s43\n"
      "s_add_u32 s73, s41, s43\n"
      "s_add_u32 s43, s43, 1\n"
      "s_cmp_eq_u32 s43, s42\n"
      "s_cselect_b32 s43, 0, s43\n"
      "s_add_u32 s74, s41, s43\n"
      "s_cmp_lt_u32 s39, 3\n"
      "s_cselect_b32 s74, s74, 0x3000\n"
      "v_bfe_u32 v5, v0, 3, 3\n"
      "s_lshl_b32 s41, s54, 7\n"
      "v_lshlrev_b32 v5, 12, v5\n"
      "v_add3_u32 v8, v8, v5, s41\n"
      "v_bfe_u32 v5, v0, 2, 3\n"
      "v_and_b32 v6, 3, v0\n"
      "v_lshl_add_u32 v6, v6, 3, s51\n"
      "v_lshl_add_u32 v6, v5, 7, v6\n"
      "v_lshrrev_b32 v10, 1, v4\n"
      "v_lshl_add_u32 v6, v10, 11, v6\n"
      "v_lshrrev_b32 v5, 1, v5\n"
      "v_lshl_add_u32 v10, v5, 5, v6\n"
      "v_xor_b32 v11, 32, v10\n"
      "v_xor_b32 v12, 64, v10\n"
      "v_xor_b32 v13, 96, v10\n"
      "s_mul_i32 s41, s49, 135168\n"
      "s_mul_i32 s42, s48, 2048\n"
      "s_add_u32 s41, s41, s42\n"
      "s_andn2_b32 s42, 536870912, s50\n"
      "s_add_u32 s41, s41, s42\n"
      "s_add_u32 s66, s41, 10551296\n"
      "s_add_u32 s67, s41, 10596352\n"
      "s_add_u32 s68, s41, 10641408\n"
      "s_cmp_eq_u32 s49, 10\n"
      "s_cselect_b32 s55, 1073741824, 0\n"
      "s_add_u32 s68, s68, s55\n"
      "s_cmp_lt_u32 s39, 6\n"
      "s_cselect_b32 s56, s68, 1073741824\n"
      "s_cmp_lt_u32 s39, 4\n"
      "s_cselect_b32 s56, s67, s56\n"
      "s_cmp_lt_u32 s39, 2\n"
      "s_cselect_b32 s56, s66, s56\n"
      "v_cvt_f32_ubyte0 v72, v3\n"
      "v_lshlrev_b32 v5, 2, v4\n"
      "v_cvt_f32_ubyte0 v5, v5\n"
      "v_mul_f32 v72, 0x3d800000, v72\n"
      "v_mul_f32 v60, v72, v5\n"
      "v_add_f32 v61, v60, v72\n"
      "v_add_f32 v62, v61, v72\n"
      "v_add_f32 v63, v62, v72\n"
      "v_cos_f32 v64, v60\n"
      "v_sin_f32 v68, v60\n"
      "v_cos_f32 v65, v61\n"
      "v_sin_f32 v69, v61\n"
      "v_cos_f32 v66, v62\n"
      "v_sin_f32 v70, v62\n"
      "v_cos_f32 v67, v63\n"
      "v_sin_f32 v71, v63\n"
      "v_mul_f32 v60, 0x3d800000, v60\n"
      "v_mul_f32 v61, 0x3d800000, v61\n"
      "v_mul_f32 v62, 0x3d800000, v62\n"
      "v_mul_f32 v63, 0x3d800000, v63\n"
      "v_cvt_pk_bf16_f32 v88, v64, v68\n"
      "v_cvt_pk_bf16_f32 v84, -v68, v64\n"
      "v_cvt_pk_f16_f32 v96, v64, v68\n"
      "v_cvt_pk_f16_f32 v92, -v68, v64\n"
      "v_cvt_pk_bf16_f32 v89, v65, v69\n"
      "v_cvt_pk_bf16_f32 v85, -v69, v65\n"
      "v_cvt_pk_f16_f32 v97, v65, v69\n"
      "v_cvt_pk_f16_f32 v93, -v69, v65\n"
      "v_cvt_pk_bf16_f32 v90, v66, v70\n"
      "v_cvt_pk_bf16_f32 v86, -v70, v66\n"
      "v_cvt_pk_f16_f32 v98, v66, v70\n"
      "v_cvt_pk_f16_f32 v94, -v70, v66\n"
      "v_cvt_pk_bf16_f32 v91, v67, v71\n"
      "v_cvt_pk_bf16_f32 v87, -v71, v67\n"
      "v_cvt_pk_f16_f32 v99, v67, v71\n"
      "v_cvt_pk_f16_f32 v95, -v71, v67\n"
      "v_cos_f32 v80, v60\n"
      "v_sin_f32 v76, v60\n"
      "v_cos_f32 v81, v61\n"
      "v_sin_f32 v77, v61\n"
      "v_cos_f32 v82, v62\n"
      "v_sin_f32 v78, v62\n"
      "v_cos_f32 v83, v63\n"
      "v_sin_f32 v79, v63\n"
      "v_mul_f32 v80, 0x3a000000, v80\n"
      "v_mul_f32 v76, 0x3a000000, v76\n"
      "v_mul_f32 v81, 0x3a000000, v81\n"
      "v_mul_f32 v77, 0x3a000000, v77\n"
      "v_mul_f32 v82, 0x3a000000, v82\n"
      "v_mul_f32 v78, 0x3a000000, v78\n"
      "v_mul_f32 v83, 0x3a000000, v83\n"
      "v_mul_f32 v79, 0x3a000000, v79\n"
      "s_mul_i32 s41, s36, 514\n"
      "s_and_b32 s42, s38, 15\n"
      "s_lshl_b32 s43, s42, 5\n"
      "s_add_u32 s41, s41, s43\n"
      "s_and_b32 s41, s41, -4\n"
      "s_cmp_lt_u32 s39, 4\n"
      "s_cselect_b64 s[80:81], s[6:7], s[8:9]\n"
      "s_cselect_b32 s43, 2, -1\n"
      "s_lshl_b32 s43, s43, 8\n"
      "v_mov_b32 v5, v0\n"
      "v_mul_u32_u24 v6, 21846, v5\n"
      "v_lshrrev_b32 v6, 16, v6\n"
      "v_mul_u32_u24 v6, 4076, v6\n"
      "v_mul_u32_u24 v5, 12, v5\n"
      "v_add3_u32 v16, v5, v6, s41\n"
      "v_add_u32 v5, s43, v0\n"
      "v_mul_u32_u24 v6, 21846, v5\n"
      "v_lshrrev_b32 v6, 16, v6\n"
      "v_mul_u32_u24 v6, 4076, v6\n"
      "v_mul_u32_u24 v5, 12, v5\n"
      "v_add3_u32 v17, v5, v6, s41\n"
      "v_add_u32 v5, 256, v0\n"
      "v_mul_u32_u24 v6, 21846, v5\n"
      "v_lshrrev_b32 v6, 16, v6\n"
      "v_mul_u32_u24 v6, 4076, v6\n"
      "v_mul_u32_u24 v5, 12, v5\n"
      "v_add3_u32 v18, v5, v6, s41\n"
      "s_lshl3_add_u32 s42, s42, s36\n"
      "s_lshl_b32 s42, s42, 7\n"
      "s_add_u32 s84, s42, 2162688\n"
      "s_add_u32 s85, s42, 3211264\n"
      "s_add_u32 s82, s58, 65536\n"
      "s_add_u32 s83, s58, 73728\n"
      "v_and_b32 v5, 3, v0\n"
      "v_mul_u32_u24 v15, 4128, v5\n"
      "v_and_b32 v5, 0x1e0, v0\n"
      "v_add_u32 v15, v15, v5\n"
      "v_and_b32 v5, 4, v0\n"
      "v_lshl_add_u32 v15, v5, 2, v15\n"
      "v_bfe_u32 v5, v0, 3, 2\n"
      "v_lshl_add_u32 v15, v5, 2, v15\n"
      "v_bfe_u32 v6, v0, 5, 3\n"
      "v_lshl_add_u32 v5, v5, 3, v6\n"
      "v_lshrrev_b32 v6, 8, v0\n"
      "v_lshl_add_u32 v5, v6, 5, v5\n"
      "v_and_b32 v6, 0x70, v1\n"
      "v_lshl_add_u32 v20, v5, 14, v6\n"
      "s_lshr_b32 s41, s39, 1\n"
      "s_lshl_b32 s41, s41, 5\n"
      "s_lshl_b32 s42, s39, 11\n"
      "s_add_u32 s41, s41, s42\n"
      "v_add_u32 v19, s41, v1\n"
      "s_and_b32 s41, s36, 1\n"
      "s_cselect_b32 s79, 0, 16\n"
      "s_add_u32 s42, s39, s41\n"
      "s_lshl2_add_u32 s42, s42, 16896\n"
      "v_lshl_add_u32 v5, v4, 6, v3\n"
      "v_mul_u32_u24 v5, 36, v5\n"
      "v_add_u32 v22, s42, v5\n"
      "v_mov_b32 v21, 0xfff8fff8\n"
      "s_mov_b32 s78, 0x40004\n"
      "s_and_b32 s41, s38, 15\n"
      "s_lshl3_add_u32 s41, s41, s39\n"
      "s_mul_i32 s41, s41, 45056\n"
      "s_lshl_b32 s42, s36, 9\n"
      "s_add_u32 s86, s41, s42\n"
      "v_bfe_u32 v5, v0, 2, 4\n"
      "v_and_b32 v6, 3, v0\n"
      "v_lshlrev_b32 v6, 7, v6\n"
      "v_min_u32 v26, 6, v5\n"
      "v_lshl_add_u32 v26, v26, 12, v6\n"
      "v_add_u32 v5, 7, v5\n"
      "v_min_u32 v5, 10, v5\n"
      "v_lshl_add_u32 v27, v5, 12, v6\n"
      "global_load_dwordx3 v[44:46], v16, s[6:7]\n"
      "global_load_dwordx3 v[48:50], v17, s[80:81]\n"
      "global_load_dwordx3 v[52:54], v18, s[8:9]\n"
      "s_barrier\n"
      "s_add_u32 m0, s51, 0\n"
      "s_add_u32 s52, s59, 0\n"
      "s_and_b32 s52, s52, 7\n"
      "s_mul_i32 s52, s52, 11008\n"
      "v_add_u32 v9, s52, v7\n"
      "buffer_load_dwordx4 v9, s[24:27], s61 offen offset:2048 lds\n"
      "buffer_load_dwordx4 v9, s[24:27], s62 offen offset:3072 lds\n"
      "buffer_load_dwordx4 v9, s[24:27], 0 offen offset:0 lds\n"
      "buffer_load_dwordx4 v9, s[24:27], s60 offen offset:1024 lds\n"
      "s_waitcnt lgkmcnt(0)\n"
      "s_add_u32 s40, s40, 1\n"
      "s_and_b32 s45, s40, 7\n"
      "s_bfe_u32 s41, s40, 0x30003\n"
      "s_lshl_b32 s41, s41, 16\n"
      "s_or_b32 s45, s45, s41\n"
      "s_lshr_b32 s41, s37, 1\n"
      "s_xor_b32 s41, s41, 6\n"
      "s_or_b32 s77, s41, s39\n"
      "v_and_b32 v24, 7, v0\n"
      "s_lshl_b32 s41, s36, 7\n"
      "v_lshl_add_u32 v24, v24, 10, s41\n"
      "v_mov_b32 v25, s40\n"
      "s_and_b32 s41, s38, 7\n"
      "s_lshl3_add_u32 s41, s41, s39\n"
      "s_min_u32 s42, s41, 42\n"
      "s_bitcmp1_b32 s38, 3\n"
      "s_cselect_b64 s[46:47], s[32:33], s[14:15]\n"
      "s_cselect_b32 s41, s41, s42\n"
      "s_lshl_b32 s41, s41, 6\n"
      "s_load_dword s17, s[46:47], s41\n"
      "s_waitcnt vmcnt(4)\n"
      "v_mul_u32_u24 v5, 12, v0\n"
      "ds_write_b32 v5, v44 offset:16896\n"
      "ds_write_b32 v5, v45 offset:16900\n"
      "ds_write_b32 v5, v46 offset:16904\n"
      "ds_write_b32 v5, v48 offset:23040\n"
      "ds_write_b32 v5, v49 offset:23044\n"
      "ds_write_b32 v5, v50 offset:23048\n"
      "ds_write_b32 v5, v52 offset:29184\n"
      "ds_write_b32 v5, v53 offset:29188\n"
      "ds_write_b32 v5, v54 offset:29192\n"
      "s_add_u32 m0, s51, 32768\n"
      "s_add_u32 s52, s59, 1\n"
      "s_and_b32 s52, s52, 7\n"
      "s_mul_i32 s52, s52, 11008\n"
      "v_add_u32 v9, s52, v7\n"
      "buffer_load_dwordx4 v9, s[24:27], s61 offen offset:2048 lds\n"
      "buffer_load_dwordx4 v9, s[24:27], s62 offen offset:3072 lds\n"
      "buffer_load_dwordx4 v9, s[24:27], 0 offen offset:0 lds\n"
      "buffer_load_dwordx4 v9, s[24:27], s60 offen offset:1024 lds\n"
      "s_waitcnt lgkmcnt(0)\n"
      "s_barrier\n"
      "ds_read_b32 v60, v22 offset:0\n"
      "ds_read_b32 v61, v22 offset:4\n"
      "ds_read_b32 v62, v22 offset:576\n"
      "ds_read_b32 v63, v22 offset:580\n"
      "ds_read_b32 v64, v22 offset:1152\n"
      "ds_read_b32 v65, v22 offset:1156\n"
      "ds_read_b32 v66, v22 offset:1728\n"
      "ds_read_b32 v67, v22 offset:1732\n"
      "ds_read_b32 v68, v22 offset:9216\n"
      "ds_read_b32 v69, v22 offset:9220\n"
      "ds_read_b32 v70, v22 offset:9792\n"
      "ds_read_b32 v71, v22 offset:9796\n"
      "ds_read_b32 v72, v22 offset:10368\n"
      "ds_read_b32 v73, v22 offset:10372\n"
      "ds_read_b32 v74, v22 offset:10944\n"
      "ds_read_b32 v75, v22 offset:10948\n"
      "s_add_u32 m0, s51, 65536\n"
      "s_add_u32 s52, s59, 2\n"
      "s_and_b32 s52, s52, 7\n"
      "s_mul_i32 s52, s52, 11008\n"
      "v_add_u32 v9, s52, v7\n"
      "buffer_load_dwordx4 v9, s[24:27], s61 offen offset:2048 lds\n"
      "buffer_load_dwordx4 v9, s[24:27], s62 offen offset:3072 lds\n"
      "buffer_load_dwordx4 v9, s[24:27], 0 offen offset:0 lds\n"
      "buffer_load_dwordx4 v9, s[24:27], s60 offen offset:1024 lds\n"
      "s_waitcnt lgkmcnt(0)\n"
      "v_alignbit_b32 v60, v61, v60, s79\n"
      "v_lshlrev_b32 v28, 16, v60\n"
      "v_and_b32 v32, 0xffff0000, v60\n"
      "v_alignbit_b32 v62, v63, v62, s79\n"
      "v_lshlrev_b32 v29, 16, v62\n"
      "v_and_b32 v33, 0xffff0000, v62\n"
      "v_alignbit_b32 v64, v65, v64, s79\n"
      "v_lshlrev_b32 v30, 16, v64\n"
      "v_and_b32 v34, 0xffff0000, v64\n"
      "v_alignbit_b32 v66, v67, v66, s79\n"
      "v_lshlrev_b32 v31, 16, v66\n"
      "v_and_b32 v35, 0xffff0000, v66\n"
      "v_alignbit_b32 v68, v69, v68, s79\n"
      "v_lshlrev_b32 v36, 16, v68\n"
      "v_and_b32 v40, 0xffff0000, v68\n"
      "v_alignbit_b32 v70, v71, v70, s79\n"
      "v_lshlrev_b32 v37, 16, v70\n"
      "v_and_b32 v41, 0xffff0000, v70\n"
      "v_alignbit_b32 v72, v73, v72, s79\n"
      "v_lshlrev_b32 v38, 16, v72\n"
      "v_and_b32 v42, 0xffff0000, v72\n"
      "v_alignbit_b32 v74, v75, v74, s79\n"
      "v_lshlrev_b32 v39, 16, v74\n"
      "v_and_b32 v43, 0xffff0000, v74\n"
      "s_waitcnt vmcnt(8)\n"
      "ds_read_b64_tr_b16 v[128:129], v10 offset:0\n"
      "ds_read_b64_tr_b16 v[130:131], v10 offset:1024\n"
      "ds_read_b64_tr_b16 v[132:133], v11 offset:0\n"
      "ds_read_b64_tr_b16 v[134:135], v11 offset:1024\n"
      "ds_read_b64_tr_b16 v[136:137], v12 offset:0\n"
      "ds_read_b64_tr_b16 v[138:139], v12 offset:1024\n"
      "ds_read_b64_tr_b16 v[140:141], v13 offset:0\n"
      "ds_read_b64_tr_b16 v[142:143], v13 offset:1024\n"
      "s_waitcnt vmcnt(4)\n"
      "ds_read_b64_tr_b16 v[144:145], v10 offset:32768\n"
      "ds_read_b64_tr_b16 v[146:147], v10 offset:33792\n"
      "ds_read_b64_tr_b16 v[148:149], v11 offset:32768\n"
      "ds_read_b64_tr_b16 v[150:151], v11 offset:33792\n"
      "ds_read_b64_tr_b16 v[152:153], v12 offset:32768\n"
      "ds_read_b64_tr_b16 v[154:155], v12 offset:33792\n"
      "ds_read_b64_tr_b16 v[156:157], v13 offset:32768\n"
      "ds_read_b64_tr_b16 v[158:159], v13 offset:33792\n"
      "s_waitcnt lgkmcnt(0)\n"
      "s_cmp_lg_u32 s39, 0\n"
      "s_cbranch_scc1 Lgd0%=\n"
      "v_and_b32 v5, 7, v0\n"
      "s_lshl_b32 s41, s36, 10\n"
      "v_lshlrev_b32 v5, 7, v5\n"
      "s_add_u32 s41, s41, 4096\n"
      "s_mov_b32 s42, 4000000\n"
      "Lgp0%=:\n"
      "buffer_load_dword v6, v5, s[20:23], s41 offen sc1\n"
      "s_sub_u32 s42, s42, 1\n"
      "s_cbranch_scc1 Lend%=\n"
      "s_waitcnt vmcnt(0)\n"
      "v_cmp_ne_u32 vcc, s40, v6\n"
      "s_cbranch_vccnz Lgp0%=\n"
      "Lgd0%=:\n"
      "s_barrier\n"
      "buffer_load_dwordx4 v[100:103], v2, s[20:23], s82 offen sc1\n"
      "buffer_load_dwordx4 v[104:107], v2, s[20:23], s83 offen sc1\n"
      "s_barrier\n"
      "buffer_store_dwordx4 v[108:111], v2, s[20:23], s75 offen sc1\n"
      "buffer_store_dwordx4 v[108:111], v2, s[20:23], s76 offen sc1\n"
      "v_and_b32 v6, 0x70, v2\n"
      "v_bfe_u32 v5, v0, 3, 1\n"
      "v_mul_u32_u24 v5, 128, v5\n"
      "v_add_u32 v6, v6, v5\n"
      "v_bfe_u32 v5, v0, 4, 2\n"
      "v_mul_u32_u24 v5, 256, v5\n"
      "v_add_u32 v6, v6, v5\n"
      "v_bfe_u32 v5, v0, 6, 1\n"
      "v_mul_u32_u24 v5, 1024, v5\n"
      "v_add_u32 v6, v6, v5\n"
      "buffer_store_dwordx4 v[108:111], v6, s[20:23], s56 offen sc1\n"
      "s_waitcnt vmcnt(5)\n"
      "v_add_u32 v10, 0x8000, v10\n"
      "v_add_u32 v11, 0x8000, v11\n"
      "v_add_u32 v12, 0x8000, v12\n"
      "v_add_u32 v13, 0x8000, v13\n"
      "ds_read_b64_tr_b16 v[160:161], v10 offset:32768\n"
      "ds_read_b64_tr_b16 v[162:163], v10 offset:33792\n"
      "ds_read_b64_tr_b16 v[164:165], v11 offset:32768\n"
      "ds_read_b64_tr_b16 v[166:167], v11 offset:33792\n"
      "ds_read_b64_tr_b16 v[168:169], v12 offset:32768\n"
      "ds_read_b64_tr_b16 v[170:171], v12 offset:33792\n"
      "ds_read_b64_tr_b16 v[172:173], v13 offset:32768\n"
      "ds_read_b64_tr_b16 v[174:175], v13 offset:33792\n"
      "buffer_load_dword v241, v26, s[28:31], s86 offen\n"
      "s_waitcnt vmcnt(4)\n"
      "LcB%=:\n"
      "v_bitop3_b32 v60, v100, v101, s45 bitop3:0x7e\n"
      "v_bitop3_b32 v61, v102, v103, s45 bitop3:0x7e\n"
      "v_bitop3_b32 v62, v104, v105, s45 bitop3:0x7e\n"
      "v_bitop3_b32 v63, v106, v107, s45 bitop3:0x7e\n"
      "v_or3_b32 v60, v60, v61, v62\n"
      "v_bitop3_b32 v60, v60, v63, v21 bitop3:0x54\n"
      "v_cmp_ne_u32 vcc, 0, v60\n"
      "s_cbranch_vccnz LrB%=\n"
      "v_bfi_b32 v100, v21, v100, s78\n"
      "v_bfi_b32 v101, v21, v101, s78\n"
      "ds_write2st64_b32 v15, v100, v101 offset0:0 offset1:4\n"
      "v_bfi_b32 v102, v21, v102, s78\n"
      "v_bfi_b32 v103, v21, v103, s78\n"
      "ds_write2st64_b32 v15, v102, v103 offset0:8 offset1:12\n"
      "v_bfi_b32 v104, v21, v104, s78\n"
      "v_bfi_b32 v105, v21, v105, s78\n"
      "ds_write2st64_b32 v15, v104, v105 offset0:2 offset1:6\n"
      "v_bfi_b32 v106, v21, v106, s78\n"
      "v_bfi_b32 v107, v21, v107, s78\n"
      "ds_write2st64_b32 v15, v106, v107 offset0:10 offset1:14\n"
      "s_waitcnt lgkmcnt(0)\n"
      "s_barrier\n"
      "ds_read_b128 v[100:103], v19 offset:0\n"
      "ds_read_b128 v[104:107], v19 offset:1024\n"
      "s_waitcnt lgkmcnt(1)\n"
      "v_mfma_f32_16x16x32_f16 v[60:63], v[100:103], v[96:99], 0\n"
      "v_mfma_f32_16x16x32_f16 v[64:67], v[100:103], v[92:95], 0\n"
      "s_waitcnt lgkmcnt(0)\n"
      "v_mfma_f32_16x16x32_f16 v[44:47], v[104:107], v[96:99], 0\n"
      "v_mfma_f32_16x16x32_f16 v[48:51], v[104:107], v[92:95], 0\n"
      "s_nop 3\n"
      "v_pk_mul_f32 v[68:69], v[60:61], v[76:77]\n"
      "v_pk_mul_f32 v[70:71], v[62:63], v[78:79]\n"
      "v_pk_mul_f32 v[72:73], v[64:65], v[76:77]\n"
      "v_pk_mul_f32 v[74:75], v[66:67], v[78:79]\n"
      "v_pk_fma_f32 v[64:65], v[64:65], v[80:81], v[68:69] neg_lo:[0,0,1] neg_hi:[0,0,1]\n"
      "v_pk_fma_f32 v[60:61], v[60:61], v[80:81], v[72:73]\n"
      "v_pk_fma_f32 v[66:67], v[66:67], v[82:83], v[70:71] neg_lo:[0,0,1] neg_hi:[0,0,1]\n"
      "v_pk_fma_f32 v[62:63], v[62:63], v[82:83], v[74:75]\n"
      "v_cvt_pk_bf16_f32 v100, v60, v64\n"
      "v_cvt_pk_bf16_f32 v101, v61, v65\n"
      "v_cvt_pk_bf16_f32 v102, v62, v66\n"
      "v_cvt_pk_bf16_f32 v103, v63, v67\n"
      "v_pk_mul_f32 v[52:53], v[44:45], v[76:77]\n"
      "v_pk_mul_f32 v[54:55], v[46:47], v[78:79]\n"
      "v_mfma_f32_16x16x32_bf16 v[60:63], v[88:91], v[100:103], 0\n"
      "v_mfma_f32_16x16x32_bf16 v[64:67], v[84:87], v[100:103], 0\n"
      "v_pk_mul_f32 v[56:57], v[48:49], v[76:77]\n"
      "v_pk_mul_f32 v[58:59], v[50:51], v[78:79]\n"
      "v_pk_fma_f32 v[48:49], v[48:49], v[80:81], v[52:53] neg_lo:[0,0,1] neg_hi:[0,0,1]\n"
      "v_pk_fma_f32 v[44:45], v[44:45], v[80:81], v[56:57]\n"
      "v_pk_fma_f32 v[50:51], v[50:51], v[82:83], v[54:55] neg_lo:[0,0,1] neg_hi:[0,0,1]\n"
      "v_pk_fma_f32 v[46:47], v[46:47], v[82:83], v[58:59]\n"
      "v_cvt_pk_bf16_f32 v104, v44, v48\n"
      "v_cvt_pk_bf16_f32 v105, v45, v49\n"
      "v_cvt_pk_bf16_f32 v106, v46, v50\n"
      "v_cvt_pk_bf16_f32 v107, v47, v51\n"
      "v_pk_mul_f32 v[68:69], v[60:61], v[36:37]\n"
      "v_pk_mul_f32 v[70:71], v[62:63], v[38:39]\n"
      "v_mfma_f32_16x16x32_bf16 v[44:47], v[88:91], v[104:107], 0\n"
      "v_mfma_f32_16x16x32_bf16 v[48:51], v[84:87], v[104:107], 0\n"
      "v_pk_mul_f32 v[72:73], v[64:65], v[36:37]\n"
      "v_pk_mul_f32 v[74:75], v[66:67], v[38:39]\n"
      "v_pk_fma_f32 v[64:65], v[64:65], v[28:29], v[68:69]\n"
      "v_pk_fma_f32 v[60:61], v[60:61], v[28:29], v[72:73] neg_lo:[0,0,1] neg_hi:[0,0,1]\n"
      "v_pk_fma_f32 v[66:67], v[66:67], v[30:31], v[70:71]\n"
      "v_pk_fma_f32 v[62:63], v[62:63], v[30:31], v[74:75] neg_lo:[0,0,1] neg_hi:[0,0,1]\n"
      "v_cvt_pk_bf16_f32 v100, v64, v60\n"
      "v_cvt_pk_bf16_f32 v101, v65, v61\n"
      "v_cvt_pk_bf16_f32 v102, v66, v62\n"
      "v_cvt_pk_bf16_f32 v103, v67, v63\n"
      "v_pk_mul_f32 v[52:53], v[44:45], v[40:41]\n"
      "v_pk_mul_f32 v[54:55], v[46:47], v[42:43]\n"
      "v_mfma_f32_16x16x32_bf16 v[64:67], v[100:103], v[88:91], 0\n"
      "v_mfma_f32_16x16x32_bf16 v[60:63], v[100:103], v[84:87], 0\n"
      "v_pk_mul_f32 v[56:57], v[48:49], v[40:41]\n"
      "v_pk_mul_f32 v[58:59], v[50:51], v[42:43]\n"
      "v_pk_fma_f32 v[48:49], v[48:49], v[32:33], v[52:53]\n"
      "v_pk_fma_f32 v[44:45], v[44:45], v[32:33], v[56:57] neg_lo:[0,0,1] neg_hi:[0,0,1]\n"
      "v_pk_fma_f32 v[50:51], v[50:51], v[34:35], v[54:55]\n"
      "v_pk_fma_f32 v[46:47], v[46:47], v[34:35], v[58:59] neg_lo:[0,0,1] neg_hi:[0,0,1]\n"
      "v_cvt_pk_bf16_f32 v104, v48, v44\n"
      "v_cvt_pk_bf16_f32 v105, v49, v45\n"
      "v_cvt_pk_bf16_f32 v106, v50, v46\n"
      "v_cvt_pk_bf16_f32 v107, v51, v47\n"
      "v_pk_mul_f32 v[72:73], v[64:65], v[76:77]\n"
      "v_pk_mul_f32 v[74:75], v[66:67], v[78:79]\n"
      "v_mfma_f32_16x16x32_bf16 v[48:51], v[104:107], v[88:91], 0\n"
      "v_mfma_f32_16x16x32_bf16 v[44:47], v[104:107], v[84:87], 0\n"
      "v_pk_mul_f32 v[68:69], v[60:61], v[76:77]\n"
      "v_pk_mul_f32 v[70:71], v[62:63], v[78:79]\n"
      "v_pk_fma_f32 v[64:65], v[64:65], v[80:81], v[68:69]\n"
      "v_pk_fma_f32 v[60:61], v[60:61], v[80:81], v[72:73] neg_lo:[0,0,1] neg_hi:[0,0,1]\n"
      "v_pk_fma_f32 v[66:67], v[66:67], v[82:83], v[70:71]\n"
      "v_pk_fma_f32 v[62:63], v[62:63], v[82:83], v[74:75] neg_lo:[0,0,1] neg_hi:[0,0,1]\n"
      "v_cvt_pk_bf16_f32 v100, v64, v60\n"
      "v_cvt_pk_bf16_f32 v101, v65, v61\n"
      "v_cvt_pk_bf16_f32 v102, v66, v62\n"
      "v_cvt_pk_bf16_f32 v103, v67, v63\n"
      "v_pk_mul_f32 v[56:57], v[48:49], v[76:77]\n"
      "v_pk_mul_f32 v[58:59], v[50:51], v[78:79]\n"
      "v_mfma_f32_16x16x32_bf16 v[64:67], v[88:91], v[100:103], 0\n"
      "v_mfma_f32_16x16x32_bf16 v[60:63], v[84:87], v[100:103], 0\n"
      "v_pk_mul_f32 v[52:53], v[44:45], v[76:77]\n"
      "v_pk_mul_f32 v[54:55], v[46:47], v[78:79]\n"
      "v_pk_fma_f32 v[48:49], v[48:49], v[80:81], v[52:53]\n"
      "v_pk_fma_f32 v[44:45], v[44:45], v[80:81], v[56:57] neg_lo:[0,0,1] neg_hi:[0,0,1]\n"
      "v_pk_fma_f32 v[50:51], v[50:51], v[82:83], v[54:55]\n"
      "v_pk_fma_f32 v[46:47], v[46:47], v[82:83], v[58:59] neg_lo:[0,0,1] neg_hi:[0,0,1]\n"
      "v_cvt_pk_bf16_f32 v104, v48, v44\n"
      "v_cvt_pk_bf16_f32 v105, v49, v45\n"
      "v_cvt_pk_bf16_f32 v106, v50, v46\n"
      "v_cvt_pk_bf16_f32 v107, v51, v47\n"
      "v_cvt_pk_bf16_f32 v100, v60, v64\n"
      "v_cvt_pk_bf16_f32 v101, v61, v65\n"
      "v_mfma_f32_16x16x32_bf16 v[48:51], v[88:91], v[104:107], 0\n"
      "v_mfma_f32_16x16x32_bf16 v[44:47], v[84:87], v[104:107], 0\n"
      "v_cvt_pk_bf16_f32 v102, v62, v66\n"
      "v_cvt_pk_bf16_f32 v103, v63, v67\n"
      "v_min_u32 v100, -2, v100\n"
      "v_min_u32 v101, -2, v101\n"
      "v_min_u32 v102, -2, v102\n"
      "v_min_u32 v103, -2, v103\n"
      "ds_write_b128 v19, v[100:103] offset:0\n"
      "s_nop 0\n"
      "v_cvt_pk_bf16_f32 v104, v44, v48\n"
      "v_cvt_pk_bf16_f32 v105, v45, v49\n"
      "v_cvt_pk_bf16_f32 v106, v46, v50\n"
      "v_cvt_pk_bf16_f32 v107, v47, v51\n"
      "v_min_u32 v104, -2, v104\n"
      "v_min_u32 v105, -2, v105\n"
      "v_min_u32 v106, -2, v106\n"
      "v_min_u32 v107, -2, v107\n"
      "ds_write_b128 v19, v[104:107] offset:1024\n"
      "s_waitcnt vmcnt(1)\n"
      "s_waitcnt lgkmcnt(0)\n"
      "s_barrier\n"
      "ds_read2st64_b32 v[100:101], v15 offset0:0 offset1:4\n"
      "ds_read2st64_b32 v[102:103], v15 offset0:8 offset1:12\n"
      "ds_read2st64_b32 v[104:105], v15 offset0:2 offset1:6\n"
      "ds_read2st64_b32 v[106:107], v15 offset0:10 offset1:14\n"
      "s_waitcnt lgkmcnt(2)\n"
      "buffer_store_dwordx4 v[100:103], v20, s[20:23], s84 offen sc1\n"
      "s_waitcnt lgkmcnt(0)\n"
      "buffer_store_dwordx4 v[104:107], v20, s[20:23], s85 offen sc1\n"
      "s_cmp_eq_u32 s77, 0\n"
      "s_cbranch_scc0 Lfb1%=\n"
      "s_mov_b32 s46, 12288\n"
      "buffer_store_dword v25, v24, s[20:23], s46 offen sc1\n"
      "Lfb1%=:\n"
      "s_barrier\n"
      "buffer_load_dword v241, v27, s[28:31], s86 offen\n"
      "s_add_u32 m0, s51, 0\n"
      "s_add_u32 s52, s59, 3\n"
      "s_and_b32 s52, s52, 7\n"
      "s_mul_i32 s52, s52, 11008\n"
      "v_add_u32 v9, s52, v7\n"
      "buffer_load_dwordx4 v9, s[24:27], s61 offen offset:2048 lds\n"
      "buffer_load_dwordx4 v9, s[24:27], s62 offen offset:3072 lds\n"
      "buffer_load_dwordx4 v9, s[24:27], 0 offen offset:0 lds\n"
      "buffer_load_dwordx4 v9, s[24:27], s60 offen offset:1024 lds\n"
      "s_lshr_b32 s43, s39, 2\n"
      "s_lshl1_add_u32 s42, s48, s43\n"
      "s_cmp_lt_u32 s42, 43\n"
      "s_cselect_b32 s42, s42, 0\n"
      "s_cselect_b32 s41, 0, 1073741824\n"
      "s_add_u32 s66, s66, s41\n"
      "s_add_u32 s67, s67, s41\n"
      "s_add_u32 s68, s68, s41\n"
      "s_and_b32 s41, s39, 3\n"
      "s_lshl3_add_u32 s42, s42, s41\n"
      "s_lshl_b32 s42, s42, 3\n"
      "v_and_b32 v5, 1, v0\n"
      "v_and_b32 v6, 2, v0\n"
      "v_lshl_add_u32 v20, v5, 2, s42\n"
      "v_lshl_add_u32 v20, v6, 4, v20\n"
      "global_load_dword v15, v20, s[14:15]\n"
      "v_and_b32 v22, 0xfc, v0\n"
      "v_lshlrev_b32 v22, 2, v22\n"
      "v_mul_u32_u24 v6, 576, v6\n"
      "v_xor_b32 v22, v22, v6\n"
      "s_lshl_b32 s43, s43, 11\n"
      "v_lshl_add_u32 v5, v5, 3, s43\n"
      "v_add_u32 v22, v22, v5\n"
      "s_lshl_b32 s41, s39, 12\n"
      "v_add_u32 v20, s41, v1\n"
      "v_xor_b32 v21, 0x80, v20\n"
      "s_mul_i32 s41, s49, 768\n"
      "s_mul_i32 s42, s39, 262144\n"
      "s_add_u32 s41, s41, s42\n"
      "v_lshl_add_u32 v18, v3, 4, s41\n"
      "v_mul_u32_u24 v19, 8192, v4\n"
      "v_add_u32 v18, v18, v19\n"
      "v_add_u32 v19, s55, v18\n"
      "s_add_u32 s41, s59, 0\n"
      "s_and_b32 s41, s41, 7\n"
      "s_mul_i32 s41, s41, 32768\n"
      "s_add_u32 s75, s41, 4259840\n"
      "s_add_u32 s41, s59, 1\n"
      "s_and_b32 s41, s41, 7\n"
      "s_mul_i32 s41, s41, 32768\n"
      "s_add_u32 s76, s41, 4259840\n"
      "s_add_u32 s41, s59, 2\n"
      "s_and_b32 s41, s41, 7\n"
      "s_mul_i32 s41, s41, 32768\n"
      "s_add_u32 s77, s41, 4259840\n"
      "s_add_u32 s41, s59, 3\n"
      "s_and_b32 s41, s41, 7\n"
      "s_mul_i32 s41, s41, 32768\n"
      "s_add_u32 s78, s41, 4259840\n"
      "s_add_u32 s41, s59, 4\n"
      "s_and_b32 s41, s41, 7\n"
      "s_mul_i32 s41, s41, 32768\n"
      "s_add_u32 s79, s41, 4259840\n"
      "s_add_u32 s41, s59, 5\n"
      "s_and_b32 s41, s41, 7\n"
      "s_mul_i32 s41, s41, 32768\n"
      "s_add_u32 s80, s41, 4259840\n"
      "s_add_u32 s41, s59, 6\n"
      "s_and_b32 s41, s41, 7\n"
      "s_mul_i32 s41, s41, 32768\n"
      "s_add_u32 s81, s41, 4259840\n"
      "s_add_u32 s41, s59, 7\n"
      "s_and_b32 s41, s41, 7\n"
      "s_mul_i32 s41, s41, 32768\n"
      "s_add_u32 s82, s41, 4259840\n"
      "v_mov_b32 v16, 0xbdd2d3e8\n"
      "v_mov_b32 v17, 0xc0135761\n"
      "v_and_b32 v2, 31, v0\n"
      "v_lshlrev_b32 v2, 2, v2\n"
      "v_bfe_u32 v5, v0, 5, 1\n"
      "v_mul_u32_u24 v5, 128, v5\n"
      "v_add_u32 v2, v2, v5\n"
      "v_bfe_u32 v5, v0, 6, 2\n"
      "v_mul_u32_u24 v5, 256, v5\n"
      "v_add_u32 v2, v2, v5\n"
      "v_bfe_u32 v5, v0, 8, 1\n"
      "v_mul_u32_u24 v5, 1024, v5\n"
      "v_add_u32 v2, v2, v5\n"
      "s_and_b32 s41, s39, 1\n"
      "s_lshl1_add_u32 s42, s48, s41\n"
      "s_cmp_lt_u32 s39, 2\n"
      "s_cselect_b32 s83, s66, s67\n"
      "s_lshl_b32 s41, s41, 10\n"
      "s_add_u32 s83, s83, s41\n"
      "s_cmp_lt_u32 s42, 43\n"
      "s_cselect_b32 s83, s83, 1073741824\n"
      "s_cselect_b32 s42, s42, 0\n"
      "s_lshl_b32 s84, s42, 6\n"
      "s_waitcnt vmcnt(5)\n"
      "s_add_u32 m0, s51, 32768\n"
      "s_add_u32 s52, s59, 4\n"
      "s_and_b32 s52, s52, 7\n"
      "s_mul_i32 s52, s52, 11008\n"
      "v_add_u32 v9, s52, v7\n"
      "buffer_load_dwordx4 v9, s[24:27], s61 offen offset:2048 lds\n"
      "buffer_load_dwordx4 v9, s[24:27], s62 offen offset:3072 lds\n"
      "buffer_load_dwordx4 v9, s[24:27], 0 offen offset:0 lds\n"
      "buffer_load_dwordx4 v9, s[24:27], s60 offen offset:1024 lds\n"
      "s_add_u32 m0, s51, 65536\n"
      "s_add_u32 s52, s59, 5\n"
      "s_and_b32 s52, s52, 7\n"
      "s_mul_i32 s52, s52, 11008\n"
      "v_add_u32 v9, s52, v7\n"
      "buffer_load_dwordx4 v9, s[24:27], s61 offen offset:2048 lds\n"
      "buffer_load_dwordx4 v9, s[24:27], s62 offen offset:3072 lds\n"
      "buffer_load_dwordx4 v9, s[24:27], 0 offen offset:0 lds\n"
      "buffer_load_dwordx4 v9, s[24:27], s60 offen offset:1024 lds\n"
      "s_waitcnt vmcnt(9)\n"
      "v_add_u32 v10, 0xffff8000, v10\n"
      "v_add_u32 v11, 0xffff8000, v11\n"
      "v_add_u32 v12, 0xffff8000, v12\n"
      "v_add_u32 v13, 0xffff8000, v13\n"
      "ds_read_b64_tr_b16 v[176:177], v10 offset:0\n"
      "ds_read_b64_tr_b16 v[178:179], v10 offset:1024\n"
      "ds_read_b64_tr_b16 v[180:181], v11 offset:0\n"
      "ds_read_b64_tr_b16 v[182:183], v11 offset:1024\n"
      "ds_read_b64_tr_b16 v[184:185], v12 offset:0\n"
      "ds_read_b64_tr_b16 v[186:187], v12 offset:1024\n"
      "ds_read_b64_tr_b16 v[188:189], v13 offset:0\n"
      "ds_read_b64_tr_b16 v[190:191], v13 offset:1024\n"
      "s_waitcnt lgkmcnt(0)\n"
      "s_add_u32 m0, s51, 0\n"
      "s_add_u32 s52, s59, 6\n"
      "s_and_b32 s52, s52, 7\n"
      "s_mul_i32 s52, s52, 11008\n"
      "v_add_u32 v9, s52, v7\n"
      "buffer_load_dwordx4 v9, s[24:27], s61 offen offset:2048 lds\n"
      "buffer_load_dwordx4 v9, s[24:27], s62 offen offset:3072 lds\n"
      "buffer_load_dwordx4 v9, s[24:27], 0 offen offset:0 lds\n"
      "buffer_load_dwordx4 v9, s[24:27], s60 offen offset:1024 lds\n"
      "s_waitcnt vmcnt(8)\n"
      "ds_read_b64_tr_b16 v[192:193], v10 offset:32768\n"
      "ds_read_b64_tr_b16 v[194:195], v10 offset:33792\n"
      "ds_read_b64_tr_b16 v[196:197], v11 offset:32768\n"
      "ds_read_b64_tr_b16 v[198:199], v11 offset:33792\n"
      "ds_read_b64_tr_b16 v[200:201], v12 offset:32768\n"
      "ds_read_b64_tr_b16 v[202:203], v12 offset:33792\n"
      "ds_read_b64_tr_b16 v[204:205], v13 offset:32768\n"
      "ds_read_b64_tr_b16 v[206:207], v13 offset:33792\n"
      "s_waitcnt lgkmcnt(0)\n"
      "s_add_u32 m0, s51, 32768\n"
      "s_add_u32 s52, s59, 7\n"
      "s_and_b32 s52, s52, 7\n"
      "s_mul_i32 s52, s52, 11008\n"
      "v_add_u32 v9, s52, v7\n"
      "buffer_load_dwordx4 v9, s[24:27], s61 offen offset:2048 lds\n"
      "buffer_load_dwordx4 v9, s[24:27], s62 offen offset:3072 lds\n"
      "buffer_load_dwordx4 v9, s[24:27], 0 offen offset:0 lds\n"
      "buffer_load_dwordx4 v9, s[24:27], s60 offen offset:1024 lds\n"
      "s_waitcnt vmcnt(8)\n"
      "v_add_u32 v10, 0x8000, v10\n"
      "v_add_u32 v11, 0x8000, v11\n"
      "v_add_u32 v12, 0x8000, v12\n"
      "v_add_u32 v13, 0x8000, v13\n"
      "ds_read_b64_tr_b16 v[208:209], v10 offset:32768\n"
      "ds_read_b64_tr_b16 v[210:211], v10 offset:33792\n"
      "ds_read_b64_tr_b16 v[212:213], v11 offset:32768\n"
      "ds_read_b64_tr_b16 v[214:215], v11 offset:33792\n"
      "ds_read_b64_tr_b16 v[216:217], v12 offset:32768\n"
      "ds_read_b64_tr_b16 v[218:219], v12 offset:33792\n"
      "ds_read_b64_tr_b16 v[220:221], v13 offset:32768\n"
      "ds_read_b64_tr_b16 v[222:223], v13 offset:33792\n"
      "s_waitcnt lgkmcnt(0)\n"
      "s_add_u32 m0, s51, 65536\n"
      "s_lshl_b32 s52, s69, 17\n"
      "v_add_u32 v9, s52, v8\n"
      "buffer_load_dwordx4 v9, s[28:31], s64 offen offset:2048 lds\n"
      "buffer_load_dwordx4 v9, s[28:31], s65 offen offset:3072 lds\n"
      "buffer_load_dwordx4 v9, s[28:31], 0 offen offset:0 lds\n"
      "buffer_load_dwordx4 v9, s[28:31], s63 offen offset:1024 lds\n"
      "s_waitcnt vmcnt(8)\n"
      "v_add_u32 v10, 0xffff8000, v10\n"
      "v_add_u32 v11, 0xffff8000, v11\n"
      "v_add_u32 v12, 0xffff8000, v12\n"
      "v_add_u32 v13, 0xffff8000, v13\n"
      "ds_read_b64_tr_b16 v[224:225], v10 offset:0\n"
      "ds_read_b64_tr_b16 v[226:227], v10 offset:1024\n"
      "ds_read_b64_tr_b16 v[228:229], v11 offset:0\n"
      "ds_read_b64_tr_b16 v[230:231], v11 offset:1024\n"
      "ds_read_b64_tr_b16 v[232:233], v12 offset:0\n"
      "ds_read_b64_tr_b16 v[234:235], v12 offset:1024\n"
      "ds_read_b64_tr_b16 v[236:237], v13 offset:0\n"
      "ds_read_b64_tr_b16 v[238:239], v13 offset:1024\n"
      "s_waitcnt lgkmcnt(0)\n"
      "s_add_u32 m0, s51, 0\n"
      "s_lshl_b32 s52, s70, 17\n"
      "v_add_u32 v9, s52, v8\n"
      "buffer_load_dwordx4 v9, s[28:31], s64 offen offset:2048 lds\n"
      "buffer_load_dwordx4 v9, s[28:31], s65 offen offset:3072 lds\n"
      "buffer_load_dwordx4 v9, s[28:31], 0 offen offset:0 lds\n"
      "buffer_load_dwordx4 v9, s[28:31], s63 offen offset:1024 lds\n"
      "s_cmp_lg_u32 s39, 0\n"
      "s_cbranch_scc1 Lgd2%=\n"
      "v_and_b32 v5, 7, v0\n"
      "s_lshl_b32 s41, s36, 10\n"
      "v_lshlrev_b32 v5, 7, v5\n"
      "s_add_u32 s41, s41, 20480\n"
      "s_mov_b32 s42, 4000000\n"
      "Lgp2%=:\n"
      "buffer_load_dword v6, v5, s[20:23], s41 offen sc1\n"
      "s_sub_u32 s42, s42, 1\n"
      "s_cbranch_scc1 Lend%=\n"
      "s_waitcnt vmcnt(0)\n"
      "v_cmp_ne_u32 vcc, s40, v6\n"
      "s_cbranch_vccnz Lgp2%=\n"
      "Lgd2%=:\n"
      "s_barrier\n"
      "buffer_load_dwordx4 v[32:35], v18, s[20:23], s75 offen offset:0 sc1\n"
      "buffer_load_dwordx4 v[36:39], v18, s[20:23], s76 offen offset:0 sc1\n"
      "buffer_load_dwordx4 v[40:43], v18, s[20:23], s77 offen offset:0 sc1\n"
      "buffer_load_dwordx4 v[44:47], v18, s[20:23], s78 offen offset:0 sc1\n"
      "buffer_load_dwordx4 v[48:51], v18, s[20:23], s79 offen offset:0 sc1\n"
      "buffer_load_dwordx4 v[52:55], v18, s[20:23], s80 offen offset:0 sc1\n"
      "s_waitcnt vmcnt(14)\n"
      "ds_read_b64_tr_b16 v[240:241], v10 offset:32768\n"
      "ds_read_b64_tr_b16 v[242:243], v10 offset:33792\n"
      "ds_read_b64_tr_b16 v[244:245], v11 offset:32768\n"
      "ds_read_b64_tr_b16 v[246:247], v11 offset:33792\n"
      "ds_read_b64_tr_b16 v[248:249], v12 offset:32768\n"
      "ds_read_b64_tr_b16 v[250:251], v12 offset:33792\n"
      "ds_read_b64_tr_b16 v[252:253], v13 offset:32768\n"
      "ds_read_b64_tr_b16 v[254:255], v13 offset:33792\n"
      "s_waitcnt vmcnt(10)\n"
      "v_add_u32 v10, 0x8000, v10\n"
      "v_add_u32 v11, 0x8000, v11\n"
      "v_add_u32 v12, 0x8000, v12\n"
      "v_add_u32 v13, 0x8000, v13\n"
      "ds_read_b64_tr_b16 v[112:113], v10 offset:32768\n"
      "ds_read_b64_tr_b16 v[114:115], v10 offset:33792\n"
      "ds_read_b64_tr_b16 v[116:117], v11 offset:32768\n"
      "ds_read_b64_tr_b16 v[118:119], v11 offset:33792\n"
      "ds_read_b64_tr_b16 v[120:121], v12 offset:32768\n"
      "ds_read_b64_tr_b16 v[122:123], v12 offset:33792\n"
      "ds_read_b64_tr_b16 v[124:125], v13 offset:32768\n"
      "ds_read_b64_tr_b16 v[126:127], v13 offset:33792\n"
      "s_waitcnt vmcnt(6)\n"
      "v_add_u32 v10, 0xffff8000, v10\n"
      "v_add_u32 v11, 0xffff8000, v11\n"
      "v_add_u32 v12, 0xffff8000, v12\n"
      "v_add_u32 v13, 0xffff8000, v13\n"
      "ds_read_b64_tr_b16 v[96:97], v10 offset:0\n"
      "ds_read_b64_tr_b16 v[98:99], v10 offset:1024\n"
      "ds_read_b64_tr_b16 v[100:101], v11 offset:0\n"
      "ds_read_b64_tr_b16 v[102:103], v11 offset:1024\n"
      "ds_read_b64_tr_b16 v[104:105], v12 offset:0\n"
      "ds_read_b64_tr_b16 v[106:107], v12 offset:1024\n"
      "ds_read_b64_tr_b16 v[108:109], v13 offset:0\n"
      "ds_read_b64_tr_b16 v[110:111], v13 offset:1024\n"
      "v_lshlrev_b32 v14, 16, v15\n"
      "v_and_b32 v15, 0xffff0000, v15\n"
      "s_branch Ljoin%=\n"
      "Lcold%=:\n"
      "LrC%=:\n"
      "s_sub_u32 s44, s44, 1\n"
      "s_cbranch_scc1 Lend%=\n"
      "s_nop 7\n"
      "buffer_load_dword v48, v17, s[20:23], s75 offen offset:0 sc1\n"
      "buffer_load_dword v49, v17, s[20:23], s75 offen offset:128 sc1\n"
      "buffer_load_dword v50, v17, s[20:23], s75 offen offset:256 sc1\n"
      "buffer_load_dword v51, v17, s[20:23], s75 offen offset:384 sc1\n"
      "buffer_load_dword v52, v17, s[20:23], s75 offen offset:1024 sc1\n"
      "buffer_load_dword v53, v17, s[20:23], s75 offen offset:1152 sc1\n"
      "buffer_load_dword v54, v17, s[20:23], s75 offen offset:1280 sc1\n"
      "buffer_load_dword v55, v17, s[20:23], s75 offen offset:1408 sc1\n"
      "s_waitcnt vmcnt(0)\n"
      "s_branch LcC%=\n"
      "LrB%=:\n"
      "s_sub_u32 s44, s44, 1\n"
      "s_cbranch_scc1 Lend%=\n"
      "buffer_load_dwordx4 v[100:103], v2, s[20:23], s82 offen sc1\n"
      "buffer_load_dwordx4 v[104:107], v2, s[20:23], s83 offen sc1\n"
      "s_waitcnt vmcnt(0)\n"
      "s_branch LcB%=\n"
      "LrP0%=:\n"
      "s_sub_u32 s44, s44, 1\n"
      "s_cbranch_scc1 Lend%=\n"
      "buffer_load_dwordx4 v[32:35], v18, s[20:23], s75 offen offset:0 sc1\n"
      "buffer_load_dwordx4 v[36:39], v18, s[20:23], s76 offen offset:0 sc1\n"
      "buffer_load_dwordx4 v[40:43], v18, s[20:23], s77 offen offset:0 sc1\n"
      "buffer_load_dwordx4 v[44:47], v18, s[20:23], s78 offen offset:0 sc1\n"
      "buffer_load_dwordx4 v[48:51], v18, s[20:23], s79 offen offset:0 sc1\n"
      "buffer_load_dwordx4 v[52:55], v18, s[20:23], s80 offen offset:0 sc1\n"
      "buffer_load_dwordx4 v[56:59], v18, s[20:23], s81 offen offset:0 sc1\n"
      "s_waitcnt vmcnt(0)\n"
      "s_branch LcP0%=\n"
      "LrP1%=:\n"
      "s_sub_u32 s44, s44, 1\n"
      "s_cbranch_scc1 Lend%=\n"
      "buffer_load_dwordx4 v[36:39], v18, s[20:23], s76 offen offset:0 sc1\n"
      "buffer_load_dwordx4 v[40:43], v18, s[20:23], s77 offen offset:0 sc1\n"
      "buffer_load_dwordx4 v[44:47], v18, s[20:23], s78 offen offset:0 sc1\n"
      "buffer_load_dwordx4 v[48:51], v18, s[20:23], s79 offen offset:0 sc1\n"
      "buffer_load_dwordx4 v[52:55], v18, s[20:23], s80 offen offset:0 sc1\n"
      "buffer_load_dwordx4 v[56:59], v18, s[20:23], s81 offen offset:0 sc1\n"
      "buffer_load_dwordx4 v[60:63], v18, s[20:23], s82 offen offset:0 sc1\n"
      "s_waitcnt vmcnt(0)\n"
      "s_branch LcP1%=\n"
      "LrP2%=:\n"
      "s_sub_u32 s44, s44, 1\n"
      "s_cbranch_scc1 Lend%=\n"
      "buffer_load_dwordx4 v[40:43], v18, s[20:23], s77 offen offset:0 sc1\n"
      "buffer_load_dwordx4 v[44:47], v18, s[20:23], s78 offen offset:0 sc1\n"
      "buffer_load_dwordx4 v[48:51], v18, s[20:23], s79 offen offset:0 sc1\n"
      "buffer_load_dwordx4 v[52:55], v18, s[20:23], s80 offen offset:0 sc1\n"
      "buffer_load_dwordx4 v[56:59], v18, s[20:23], s81 offen offset:0 sc1\n"
      "buffer_load_dwordx4 v[60:63], v18, s[20:23], s82 offen offset:0 sc1\n"
      "buffer_load_dwordx4 v[32:35], v18, s[20:23], s75 offen offset:256 sc1\n"
      "s_waitcnt vmcnt(0)\n"
      "s_branch LcP2%=\n"
      "LrP3%=:\n"
      "s_sub_u32 s44, s44, 1\n"
      "s_cbranch_scc1 Lend%=\n"
      "buffer_load_dwordx4 v[44:47], v18, s[20:23], s78 offen offset:0 sc1\n"
      "buffer_load_dwordx4 v[48:51], v18, s[20:23], s79 offen offset:0 sc1\n"
      "buffer_load_dwordx4 v[52:55], v18, s[20:23], s80 offen offset:0 sc1\n"
      "buffer_load_dwordx4 v[56:59], v18, s[20:23], s81 offen offset:0 sc1\n"
      "buffer_load_dwordx4 v[60:63], v18, s[20:23], s82 offen offset:0 sc1\n"
      "buffer_load_dwordx4 v[32:35], v18, s[20:23], s75 offen offset:256 sc1\n"
      "buffer_load_dwordx4 v[36:39], v18, s[20:23], s76 offen offset:256 sc1\n"
      "s_waitcnt vmcnt(0)\n"
      "s_branch LcP3%=\n"
      "LrP4%=:\n"
      "s_sub_u32 s44, s44, 1\n"
      "s_cbranch_scc1 Lend%=\n"
      "buffer_load_dwordx4 v[48:51], v18, s[20:23], s79 offen offset:0 sc1\n"
      "buffer_load_dwordx4 v[52:55], v18, s[20:23], s80 offen offset:0 sc1\n"
      "buffer_load_dwordx4 v[56:59], v18, s[20:23], s81 offen offset:0 sc1\n"
      "buffer_load_dwordx4 v[60:63], v18, s[20:23], s82 offen offset:0 sc1\n"
      "buffer_load_dwordx4 v[32:35], v18, s[20:23], s75 offen offset:256 sc1\n"
      "buffer_load_dwordx4 v[36:39], v18, s[20:23], s76 offen offset:256 sc1\n"
      "buffer_load_dwordx4 v[40:43], v18, s[20:23], s77 offen offset:256 sc1\n"
      "s_waitcnt vmcnt(0)\n"
      "s_branch LcP4%=\n"
      "LrP5%=:\n"
      "s_sub_u32 s44, s44, 1\n"
      "s_cbranch_scc1 Lend%=\n"
      "buffer_load_dwordx4 v[52:55], v18, s[20:23], s80 offen offset:0 sc1\n"
      "buffer_load_dwordx4 v[56:59], v18, s[20:23], s81 offen offset:0 sc1\n"
      "buffer_load_dwordx4 v[60:63], v18, s[20:23], s82 offen offset:0 sc1\n"
      "buffer_load_dwordx4 v[32:35], v18, s[20:23], s75 offen offset:256 sc1\n"
      "buffer_load_dwordx4 v[36:39], v18, s[20:23], s76 offen offset:256 sc1\n"
      "buffer_load_dwordx4 v[40:43], v18, s[20:23], s77 offen offset:256 sc1\n"
      "buffer_load_dwordx4 v[44:47], v18, s[20:23], s78 offen offset:256 sc1\n"
      "s_waitcnt vmcnt(0)\n"
      "s_branch LcP5%=\n"
      "LrP6%=:\n"
      "s_sub_u32 s44, s44, 1\n"
      "s_cbranch_scc1 Lend%=\n"
      "buffer_load_dwordx4 v[56:59], v18, s[20:23], s81 offen offset:0 sc1\n"
      "buffer_load_dwordx4 v[60:63], v18, s[20:23], s82 offen offset:0 sc1\n"
      "buffer_load_dwordx4 v[32:35], v18, s[20:23], s75 offen offset:256 sc1\n"
      "buffer_load_dwordx4 v[36:39], v18, s[20:23], s76 offen offset:256 sc1\n"
      "buffer_load_dwordx4 v[40:43], v18, s[20:23], s77 offen offset:256 sc1\n"
      "buffer_load_dwordx4 v[44:47], v18, s[20:23], s78 offen offset:256 sc1\n"
      "buffer_load_dwordx4 v[48:51], v18, s[20:23], s79 offen offset:256 sc1\n"
      "s_waitcnt vmcnt(0)\n"
      "s_branch LcP6%=\n"
      "LrP7%=:\n"
      "s_sub_u32 s44, s44, 1\n"
      "s_cbranch_scc1 Lend%=\n"
      "buffer_load_dwordx4 v[60:63], v18, s[20:23], s82 offen offset:0 sc1\n"
      "buffer_load_dwordx4 v[32:35], v18, s[20:23], s75 offen offset:256 sc1\n"
      "buffer_load_dwordx4 v[36:39], v18, s[20:23], s76 offen offset:256 sc1\n"
      "buffer_load_dwordx4 v[40:43], v18, s[20:23], s77 offen offset:256 sc1\n"
      "buffer_load_dwordx4 v[44:47], v18, s[20:23], s78 offen offset:256 sc1\n"
      "buffer_load_dwordx4 v[48:51], v18, s[20:23], s79 offen offset:256 sc1\n"
      "buffer_load_dwordx4 v[52:55], v18, s[20:23], s80 offen offset:256 sc1\n"
      "s_waitcnt vmcnt(0)\n"
      "s_branch LcP7%=\n"
      "LrG1%=:\n"
      "s_sub_u32 s44, s44, 1\n"
      "s_cbranch_scc1 Lend%=\n"
      "s_waitcnt vmcnt(0)\n"
      "buffer_load_dwordx4 v[32:35], v18, s[20:23], s75 offen offset:256 sc1\n"
      "buffer_load_dwordx4 v[36:39], v18, s[20:23], s76 offen offset:256 sc1\n"
      "buffer_load_dwordx4 v[40:43], v18, s[20:23], s77 offen offset:256 sc1\n"
      "buffer_load_dwordx4 v[44:47], v18, s[20:23], s78 offen offset:256 sc1\n"
      "buffer_load_dwordx4 v[48:51], v18, s[20:23], s79 offen offset:256 sc1\n"
      "buffer_load_dwordx4 v[52:55], v18, s[20:23], s80 offen offset:256 sc1\n"
      "buffer_load_dwordx4 v[56:59], v18, s[20:23], s81 offen offset:256 sc1\n"
      "buffer_load_dwordx4 v[60:63], v18, s[20:23], s82 offen offset:256 sc1\n"
      "s_waitcnt vmcnt(0)\n"
      "v_mfma_f32_16x16x32_bf16 v[64:67], v[128:131], v[32:35], 0\n"
      "v_max_u32 v23, v32, v33\n"
      "v_mfma_f32_16x16x32_bf16 v[68:71], v[132:135], v[32:35], 0\n"
      "v_max3_u32 v23, v23, v34, v35\n"
      "v_mfma_f32_16x16x32_bf16 v[72:75], v[136:139], v[32:35], 0\n"
      "v_mfma_f32_16x16x32_bf16 v[76:79], v[140:143], v[32:35], 0\n"
      "v_mfma_f32_16x16x32_bf16 v[64:67], v[144:147], v[36:39], v[64:67]\n"
      "v_max3_u32 v23, v23, v36, v37\n"
      "v_mfma_f32_16x16x32_bf16 v[68:71], v[148:151], v[36:39], v[68:71]\n"
      "v_max3_u32 v23, v23, v38, v39\n"
      "v_mfma_f32_16x16x32_bf16 v[72:75], v[152:155], v[36:39], v[72:75]\n"
      "v_mfma_f32_16x16x32_bf16 v[76:79], v[156:159], v[36:39], v[76:79]\n"
      "v_mfma_f32_16x16x32_bf16 v[64:67], v[160:163], v[40:43], v[64:67]\n"
      "v_max3_u32 v23, v23, v40, v41\n"
      "v_mfma_f32_16x16x32_bf16 v[68:71], v[164:167], v[40:43], v[68:71]\n"
      "v_max3_u32 v23, v23, v42, v43\n"
      "v_mfma_f32_16x16x32_bf16 v[72:75], v[168:171], v[40:43], v[72:75]\n"
      "v_mfma_f32_16x16x32_bf16 v[76:79], v[172:175], v[40:43], v[76:79]\n"
      "v_mfma_f32_16x16x32_bf16 v[64:67], v[176:179], v[44:47], v[64:67]\n"
      "v_max3_u32 v23, v23, v44, v45\n"
      "v_mfma_f32_16x16x32_bf16 v[68:71], v[180:183], v[44:47], v[68:71]\n"
      "v_max3_u32 v23, v23, v46, v47\n"
      "v_mfma_f32_16x16x32_bf16 v[72:75], v[184:187], v[44:47], v[72:75]\n"
      "v_mfma_f32_16x16x32_bf16 v[76:79], v[188:191], v[44:47], v[76:79]\n"
      "v_mfma_f32_16x16x32_bf16 v[64:67], v[192:195], v[48:51], v[64:67]\n"
      "v_max3_u32 v23, v23, v48, v49\n"
      "v_mfma_f32_16x16x32_bf16 v[68:71], v[196:199], v[48:51], v[68:71]\n"
      "v_max3_u32 v23, v23, v50, v51\n"
      "v_mfma_f32_16x16x32_bf16 v[72:75], v[200:203], v[48:51], v[72:75]\n"
      "v_mfma_f32_16x16x32_bf16 v[76:79], v[204:207], v[48:51], v[76:79]\n"
      "v_mfma_f32_16x16x32_bf16 v[64:67], v[208:211], v[52:55], v[64:67]\n"
      "v_max3_u32 v23, v23, v52, v53\n"
      "v_mfma_f32_16x16x32_bf16 v[68:71], v[212:215], v[52:55], v[68:71]\n"
      "v_max3_u32 v23, v23, v54, v55\n"
      "v_mfma_f32_16x16x32_bf16 v[72:75], v[216:219], v[52:55], v[72:75]\n"
      "v_mfma_f32_16x16x32_bf16 v[76:79], v[220:223], v[52:55], v[76:79]\n"
      "v_mfma_f32_16x16x32_bf16 v[64:67], v[224:227], v[56:59], v[64:67]\n"
      "v_max3_u32 v23, v23, v56, v57\n"
      "v_mfma_f32_16x16x32_bf16 v[68:71], v[228:231], v[56:59], v[68:71]\n"
      "v_max3_u32 v23, v23, v58, v59\n"
      "v_mfma_f32_16x16x32_bf16 v[72:75], v[232:235], v[56:59], v[72:75]\n"
      "v_mfma_f32_16x16x32_bf16 v[76:79], v[236:239], v[56:59], v[76:79]\n"
      "v_mfma_f32_16x16x32_bf16 v[64:67], v[240:243], v[60:63], v[64:67]\n"
      "v_max3_u32 v23, v23, v60, v61\n"
      "v_mfma_f32_16x16x32_bf16 v[68:71], v[244:247], v[60:63], v[68:71]\n"
      "v_max3_u32 v23, v23, v62, v63\n"
      "v_mfma_f32_16x16x32_bf16 v[72:75], v[248:251], v[60:63], v[72:75]\n"
      "v_mfma_f32_16x16x32_bf16 v[76:79], v[252:255], v[60:63], v[76:79]\n"
      "v_cmp_eq_u32 vcc, -1, v23\n"
      "s_cbranch_vccnz LrG1%=\n"
      "s_nop 7\n"
      "buffer_load_dwordx4 v[32:35], v19, s[20:23], s75 offen offset:512 sc1\n"
      "buffer_load_dwordx4 v[36:39], v19, s[20:23], s76 offen offset:512 sc1\n"
      "buffer_load_dwordx4 v[40:43], v19, s[20:23], s77 offen offset:512 sc1\n"
      "buffer_load_dwordx4 v[44:47], v19, s[20:23], s78 offen offset:512 sc1\n"
      "buffer_load_dwordx4 v[48:51], v19, s[20:23], s79 offen offset:512 sc1\n"
      "buffer_load_dwordx4 v[52:55], v19, s[20:23], s80 offen offset:512 sc1\n"
      "s_waitcnt vmcnt(0)\n"
      "s_branch LcG1%=\n"
      "LrG2%=:\n"
      "s_sub_u32 s44, s44, 1\n"
      "s_cbranch_scc1 Lend%=\n"
      "s_waitcnt vmcnt(0)\n"
      "buffer_load_dwordx4 v[32:35], v19, s[20:23], s75 offen offset:512 sc1\n"
      "buffer_load_dwordx4 v[36:39], v19, s[20:23], s76 offen offset:512 sc1\n"
      "buffer_load_dwordx4 v[40:43], v19, s[20:23], s77 offen offset:512 sc1\n"
      "buffer_load_dwordx4 v[44:47], v19, s[20:23], s78 offen offset:512 sc1\n"
      "buffer_load_dwordx4 v[48:51], v19, s[20:23], s79 offen offset:512 sc1\n"
      "buffer_load_dwordx4 v[52:55], v19, s[20:23], s80 offen offset:512 sc1\n"
      "buffer_load_dwordx4 v[56:59], v19, s[20:23], s81 offen offset:512 sc1\n"
      "buffer_load_dwordx4 v[60:63], v19, s[20:23], s82 offen offset:512 sc1\n"
      "s_waitcnt vmcnt(0)\n"
      "v_mfma_f32_16x16x32_bf16 v[80:83], v[128:131], v[32:35], 0\n"
      "v_max_u32 v23, v32, v33\n"
      "v_mfma_f32_16x16x32_bf16 v[84:87], v[132:135], v[32:35], 0\n"
      "v_max3_u32 v23, v23, v34, v35\n"
      "v_mfma_f32_16x16x32_bf16 v[88:91], v[136:139], v[32:35], 0\n"
      "v_mfma_f32_16x16x32_bf16 v[92:95], v[140:143], v[32:35], 0\n"
      "v_mfma_f32_16x16x32_bf16 v[80:83], v[144:147], v[36:39], v[80:83]\n"
      "v_max3_u32 v23, v23, v36, v37\n"
      "v_mfma_f32_16x16x32_bf16 v[84:87], v[148:151], v[36:39], v[84:87]\n"
      "v_max3_u32 v23, v23, v38, v39\n"
      "v_mfma_f32_16x16x32_bf16 v[88:91], v[152:155], v[36:39], v[88:91]\n"
      "v_mfma_f32_16x16x32_bf16 v[92:95], v[156:159], v[36:39], v[92:95]\n"
      "v_mfma_f32_16x16x32_bf16 v[80:83], v[160:163], v[40:43], v[80:83]\n"
      "v_max3_u32 v23, v23, v40, v41\n"
      "v_mfma_f32_16x16x32_bf16 v[84:87], v[164:167], v[40:43], v[84:87]\n"
      "v_max3_u32 v23, v23, v42, v43\n"
      "v_mfma_f32_16x16x32_bf16 v[88:91], v[168:171], v[40:43], v[88:91]\n"
      "v_mfma_f32_16x16x32_bf16 v[92:95], v[172:175], v[40:43], v[92:95]\n"
      "v_mfma_f32_16x16x32_bf16 v[80:83], v[176:179], v[44:47], v[80:83]\n"
      "v_max3_u32 v23, v23, v44, v45\n"
      "v_mfma_f32_16x16x32_bf16 v[84:87], v[180:183], v[44:47], v[84:87]\n"
      "v_max3_u32 v23, v23, v46, v47\n"
      "v_mfma_f32_16x16x32_bf16 v[88:91], v[184:187], v[44:47], v[88:91]\n"
      "v_mfma_f32_16x16x32_bf16 v[92:95], v[188:191], v[44:47], v[92:95]\n"
      "v_mfma_f32_16x16x32_bf16 v[80:83], v[192:195], v[48:51], v[80:83]\n"
      "v_max3_u32 v23, v23, v48, v49\n"
      "v_mfma_f32_16x16x32_bf16 v[84:87], v[196:199], v[48:51], v[84:87]\n"
      "v_max3_u32 v23, v23, v50, v51\n"
      "v_mfma_f32_16x16x32_bf16 v[88:91], v[200:203], v[48:51], v[88:91]\n"
      "v_mfma_f32_16x16x32_bf16 v[92:95], v[204:207], v[48:51], v[92:95]\n"
      "v_mfma_f32_16x16x32_bf16 v[80:83], v[208:211], v[52:55], v[80:83]\n"
      "v_max3_u32 v23, v23, v52, v53\n"
      "v_mfma_f32_16x16x32_bf16 v[84:87], v[212:215], v[52:55], v[84:87]\n"
      "v_max3_u32 v23, v23, v54, v55\n"
      "v_mfma_f32_16x16x32_bf16 v[88:91], v[216:219], v[52:55], v[88:91]\n"
      "v_mfma_f32_16x16x32_bf16 v[92:95], v[220:223], v[52:55], v[92:95]\n"
      "v_mfma_f32_16x16x32_bf16 v[80:83], v[224:227], v[56:59], v[80:83]\n"
      "v_max3_u32 v23, v23, v56, v57\n"
      "v_mfma_f32_16x16x32_bf16 v[84:87], v[228:231], v[56:59], v[84:87]\n"
      "v_max3_u32 v23, v23, v58, v59\n"
      "v_mfma_f32_16x16x32_bf16 v[88:91], v[232:235], v[56:59], v[88:91]\n"
      "v_mfma_f32_16x16x32_bf16 v[92:95], v[236:239], v[56:59], v[92:95]\n"
      "v_mfma_f32_16x16x32_bf16 v[80:83], v[240:243], v[60:63], v[80:83]\n"
      "v_max3_u32 v23, v23, v60, v61\n"
      "v_mfma_f32_16x16x32_bf16 v[84:87], v[244:247], v[60:63], v[84:87]\n"
      "v_max3_u32 v23, v23, v62, v63\n"
      "v_mfma_f32_16x16x32_bf16 v[88:91], v[248:251], v[60:63], v[88:91]\n"
      "v_mfma_f32_16x16x32_bf16 v[92:95], v[252:255], v[60:63], v[92:95]\n"
      "v_cmp_eq_u32 vcc, -1, v23\n"
      "s_cbranch_vccnz LrG2%=\n"
      "s_nop 7\n"
      "v_lshl_add_u32 v5, v4, 3, s84\n"
      "global_load_dwordx2 v[32:33], v5, s[14:15]\n"
      "global_load_dwordx2 v[34:35], v5, s[14:15] offset:32\n"
      "s_waitcnt vmcnt(0)\n"
      "s_branch LcG2%=\n"
      "LrQ0%=:\n"
      "s_sub_u32 s44, s44, 1\n"
      "s_cbranch_scc1 Lend%=\n"
      "buffer_load_dwordx4 v[32:35], v2, s[20:23], s75 offen sc1\n"
      "buffer_load_dwordx4 v[36:39], v2, s[20:23], s76 offen sc1\n"
      "buffer_load_dwordx4 v[40:43], v2, s[20:23], s77 offen sc1\n"
      "buffer_load_dwordx4 v[44:47], v2, s[20:23], s78 offen sc1\n"
      "buffer_load_dwordx4 v[48:51], v2, s[20:23], s79 offen sc1\n"
      "buffer_load_dwordx4 v[52:55], v2, s[20:23], s80 offen sc1\n"
      "buffer_load_dwordx4 v[56:59], v14, s[20:23], s75 offen sc1\n"
      "s_waitcnt vmcnt(0)\n"
      "s_branch LcQ0%=\n"
      "LrQ1%=:\n"
      "s_sub_u32 s44, s44, 1\n"
      "s_cbranch_scc1 Lend%=\n"
      "buffer_load_dwordx4 v[36:39], v2, s[20:23], s76 offen sc1\n"
      "buffer_load_dwordx4 v[40:43], v2, s[20:23], s77 offen sc1\n"
      "buffer_load_dwordx4 v[44:47], v2, s[20:23], s78 offen sc1\n"
      "buffer_load_dwordx4 v[48:51], v2, s[20:23], s79 offen sc1\n"
      "buffer_load_dwordx4 v[52:55], v2, s[20:23], s80 offen sc1\n"
      "buffer_load_dwordx4 v[56:59], v14, s[20:23], s75 offen sc1\n"
      "buffer_load_dwordx4 v[60:63], v14, s[20:23], s76 offen sc1\n"
      "s_waitcnt vmcnt(0)\n"
      "s_branch LcQ1%=\n"
      "LrQ2%=:\n"
      "s_sub_u32 s44, s44, 1\n"
      "s_cbranch_scc1 Lend%=\n"
      "buffer_load_dwordx4 v[40:43], v2, s[20:23], s77 offen sc1\n"
      "buffer_load_dwordx4 v[44:47], v2, s[20:23], s78 offen sc1\n"
      "buffer_load_dwordx4 v[48:51], v2, s[20:23], s79 offen sc1\n"
      "buffer_load_dwordx4 v[52:55], v2, s[20:23], s80 offen sc1\n"
      "buffer_load_dwordx4 v[56:59], v14, s[20:23], s75 offen sc1\n"
      "buffer_load_dwordx4 v[60:63], v14, s[20:23], s76 offen sc1\n"
      "buffer_load_dwordx4 v[224:227], v14, s[20:23], s77 offen sc1\n"
      "s_waitcnt vmcnt(0)\n"
      "s_branch LcQ2%=\n"
      "LrQ3%=:\n"
      "s_sub_u32 s44, s44, 1\n"
      "s_cbranch_scc1 Lend%=\n"
      "buffer_load_dwordx4 v[44:47], v2, s[20:23], s78 offen sc1\n"
      "buffer_load_dwordx4 v[48:51], v2, s[20:23], s79 offen sc1\n"
      "buffer_load_dwordx4 v[52:55], v2, s[20:23], s80 offen sc1\n"
      "buffer_load_dwordx4 v[56:59], v14, s[20:23], s75 offen sc1\n"
      "buffer_load_dwordx4 v[60:63], v14, s[20:23], s76 offen sc1\n"
      "buffer_load_dwordx4 v[224:227], v14, s[20:23], s77 offen sc1\n"
      "buffer_load_dwordx4 v[228:231], v14, s[20:23], s78 offen sc1\n"
      "s_waitcnt vmcnt(0)\n"
      "s_branch LcQ3%=\n"
      "LrQ4%=:\n"
      "s_sub_u32 s44, s44, 1\n"
      "s_cbranch_scc1 Lend%=\n"
      "buffer_load_dwordx4 v[48:51], v2, s[20:23], s79 offen sc1\n"
      "buffer_load_dwordx4 v[52:55], v2, s[20:23], s80 offen sc1\n"
      "buffer_load_dwordx4 v[56:59], v14, s[20:23], s75 offen sc1\n"
      "buffer_load_dwordx4 v[60:63], v14, s[20:23], s76 offen sc1\n"
      "buffer_load_dwordx4 v[224:227], v14, s[20:23], s77 offen sc1\n"
      "buffer_load_dwordx4 v[228:231], v14, s[20:23], s78 offen sc1\n"
      "buffer_load_dwordx4 v[232:235], v14, s[20:23], s79 offen sc1\n"
      "s_waitcnt vmcnt(0)\n"
      "s_branch LcQ4%=\n"
      "LrQ5%=:\n"
      "s_sub_u32 s44, s44, 1\n"
      "s_cbranch_scc1 Lend%=\n"
      "buffer_load_dwordx4 v[52:55], v2, s[20:23], s80 offen sc1\n"
      "buffer_load_dwordx4 v[56:59], v14, s[20:23], s75 offen sc1\n"
      "buffer_load_dwordx4 v[60:63], v14, s[20:23], s76 offen sc1\n"
      "buffer_load_dwordx4 v[224:227], v14, s[20:23], s77 offen sc1\n"
      "buffer_load_dwordx4 v[228:231], v14, s[20:23], s78 offen sc1\n"
      "buffer_load_dwordx4 v[232:235], v14, s[20:23], s79 offen sc1\n"
      "buffer_load_dwordx4 v[236:239], v14, s[20:23], s80 offen sc1\n"
      "s_waitcnt vmcnt(0)\n"
      "s_branch LcQ5%=\n"
      "LrE1%=:\n"
      "s_sub_u32 s44, s44, 1\n"
      "s_cbranch_scc1 Lend%=\n"
      "s_waitcnt vmcnt(0)\n"
      "buffer_load_dwordx4 v[56:59], v14, s[20:23], s75 offen sc1\n"
      "buffer_load_dwordx4 v[60:63], v14, s[20:23], s76 offen sc1\n"
      "buffer_load_dwordx4 v[224:227], v14, s[20:23], s77 offen sc1\n"
      "buffer_load_dwordx4 v[228:231], v14, s[20:23], s78 offen sc1\n"
      "buffer_load_dwordx4 v[232:235], v14, s[20:23], s79 offen sc1\n"
      "buffer_load_dwordx4 v[236:239], v14, s[20:23], s80 offen sc1\n"
      "s_waitcnt vmcnt(0)\n"
      "v_mfma_f32_16x16x32_bf16 v[64:67], v[56:59], v[112:115], 0\n"
      "v_max_u32 v23, v56, v57\n"
      "v_mfma_f32_16x16x32_bf16 v[68:71], v[56:59], v[116:119], 0\n"
      "v_max3_u32 v23, v23, v58, v59\n"
      "v_mfma_f32_16x16x32_bf16 v[72:75], v[56:59], v[120:123], 0\n"
      "v_mfma_f32_16x16x32_bf16 v[76:79], v[56:59], v[124:127], 0\n"
      "v_mfma_f32_16x16x32_bf16 v[64:67], v[60:63], v[96:99], v[64:67]\n"
      "v_max3_u32 v23, v23, v60, v61\n"
      "v_mfma_f32_16x16x32_bf16 v[68:71], v[60:63], v[100:103], v[68:71]\n"
      "v_max3_u32 v23, v23, v62, v63\n"
      "v_mfma_f32_16x16x32_bf16 v[72:75], v[60:63], v[104:107], v[72:75]\n"
      "v_mfma_f32_16x16x32_bf16 v[76:79], v[60:63], v[108:111], v[76:79]\n"
      "v_mfma_f32_16x16x32_bf16 v[64:67], v[224:227], v[128:131], v[64:67]\n"
      "v_max3_u32 v23, v23, v224, v225\n"
      "v_mfma_f32_16x16x32_bf16 v[68:71], v[224:227], v[132:135], v[68:71]\n"
      "v_max3_u32 v23, v23, v226, v227\n"
      "v_mfma_f32_16x16x32_bf16 v[72:75], v[224:227], v[136:139], v[72:75]\n"
      "v_mfma_f32_16x16x32_bf16 v[76:79], v[224:227], v[140:143], v[76:79]\n"
      "v_mfma_f32_16x16x32_bf16 v[64:67], v[228:231], v[144:147], v[64:67]\n"
      "v_max3_u32 v23, v23, v228, v229\n"
      "v_mfma_f32_16x16x32_bf16 v[68:71], v[228:231], v[148:151], v[68:71]\n"
      "v_max3_u32 v23, v23, v230, v231\n"
      "v_mfma_f32_16x16x32_bf16 v[72:75], v[228:231], v[152:155], v[72:75]\n"
      "v_mfma_f32_16x16x32_bf16 v[76:79], v[228:231], v[156:159], v[76:79]\n"
      "v_mfma_f32_16x16x32_bf16 v[64:67], v[232:235], v[160:163], v[64:67]\n"
      "v_max3_u32 v23, v23, v232, v233\n"
      "v_mfma_f32_16x16x32_bf16 v[68:71], v[232:235], v[164:167], v[68:71]\n"
      "v_max3_u32 v23, v23, v234, v235\n"
      "v_mfma_f32_16x16x32_bf16 v[72:75], v[232:235], v[168:171], v[72:75]\n"
      "v_mfma_f32_16x16x32_bf16 v[76:79], v[232:235], v[172:175], v[76:79]\n"
      "s_cmp_lt_u32 s39, 3\n"
      "s_cbranch_scc0 Lrs1%=\n"
      "v_mfma_f32_16x16x32_bf16 v[64:67], v[236:239], v[176:179], v[64:67]\n"
      "v_max3_u32 v23, v23, v236, v237\n"
      "v_mfma_f32_16x16x32_bf16 v[68:71], v[236:239], v[180:183], v[68:71]\n"
      "v_max3_u32 v23, v23, v238, v239\n"
      "v_mfma_f32_16x16x32_bf16 v[72:75], v[236:239], v[184:187], v[72:75]\n"
      "v_mfma_f32_16x16x32_bf16 v[76:79], v[236:239], v[188:191], v[76:79]\n"
      "Lrs1%=:\n"
      "v_cmp_eq_u32 vcc, -1, v23\n"
      "s_cbranch_vccnz LrE1%=\n"
      "s_nop 7\n"
      "buffer_load_dwordx4 v[32:35], v21, s[20:23], s75 offen sc1\n"
      "buffer_load_dwordx4 v[36:39], v21, s[20:23], s76 offen sc1\n"
      "buffer_load_dwordx4 v[40:43], v21, s[20:23], s77 offen sc1\n"
      "buffer_load_dwordx4 v[44:47], v21, s[20:23], s78 offen sc1\n"
      "buffer_load_dwordx4 v[48:51], v21, s[20:23], s79 offen sc1\n"
      "buffer_load_dwordx4 v[52:55], v21, s[20:23], s80 offen sc1\n"
      "s_waitcnt vmcnt(0)\n"
      "s_branch LcE1%=\n"
      "LrE2%=:\n"
      "s_sub_u32 s44, s44, 1\n"
      "s_cbranch_scc1 Lend%=\n"
      "s_waitcnt vmcnt(0)\n"
      "buffer_load_dwordx4 v[32:35], v21, s[20:23], s75 offen sc1\n"
      "buffer_load_dwordx4 v[36:39], v21, s[20:23], s76 offen sc1\n"
      "buffer_load_dwordx4 v[40:43], v21, s[20:23], s77 offen sc1\n"
      "buffer_load_dwordx4 v[44:47], v21, s[20:23], s78 offen sc1\n"
      "buffer_load_dwordx4 v[48:51], v21, s[20:23], s79 offen sc1\n"
      "buffer_load_dwordx4 v[52:55], v21, s[20:23], s80 offen sc1\n"
      "s_waitcnt vmcnt(0)\n"
      "v_mfma_f32_16x16x32_bf16 v[80:83], v[32:35], v[112:115], 0\n"
      "v_max_u32 v23, v32, v33\n"
      "v_mfma_f32_16x16x32_bf16 v[84:87], v[32:35], v[116:119], 0\n"
      "v_max3_u32 v23, v23, v34, v35\n"
      "v_mfma_f32_16x16x32_bf16 v[88:91], v[32:35], v[120:123], 0\n"
      "v_mfma_f32_16x16x32_bf16 v[92:95], v[32:35], v[124:127], 0\n"
      "v_mfma_f32_16x16x32_bf16 v[80:83], v[36:39], v[96:99], v[80:83]\n"
      "v_max3_u32 v23, v23, v36, v37\n"
      "v_mfma_f32_16x16x32_bf16 v[84:87], v[36:39], v[100:103], v[84:87]\n"
      "v_max3_u32 v23, v23, v38, v39\n"
      "v_mfma_f32_16x16x32_bf16 v[88:91], v[36:39], v[104:107], v[88:91]\n"
      "v_mfma_f32_16x16x32_bf16 v[92:95], v[36:39], v[108:111], v[92:95]\n"
      "v_mfma_f32_16x16x32_bf16 v[80:83], v[40:43], v[128:131], v[80:83]\n"
      "v_max3_u32 v23, v23, v40, v41\n"
      "v_mfma_f32_16x16x32_bf16 v[84:87], v[40:43], v[132:135], v[84:87]\n"
      "v_max3_u32 v23, v23, v42, v43\n"
      "v_mfma_f32_16x16x32_bf16 v[88:91], v[40:43], v[136:139], v[88:91]\n"
      "v_mfma_f32_16x16x32_bf16 v[92:95], v[40:43], v[140:143], v[92:95]\n"
      "v_mfma_f32_16x16x32_bf16 v[80:83], v[44:47], v[144:147], v[80:83]\n"
      "v_max3_u32 v23, v23, v44, v45\n"
      "v_mfma_f32_16x16x32_bf16 v[84:87], v[44:47], v[148:151], v[84:87]\n"
      "v_max3_u32 v23, v23, v46, v47\n"
      "v_mfma_f32_16x16x32_bf16 v[88:91], v[44:47], v[152:155], v[88:91]\n"
      "v_mfma_f32_16x16x32_bf16 v[92:95], v[44:47], v[156:159], v[92:95]\n"
      "v_mfma_f32_16x16x32_bf16 v[80:83], v[48:51], v[160:163], v[80:83]\n"
      "v_max3_u32 v23, v23, v48, v49\n"
      "v_mfma_f32_16x16x32_bf16 v[84:87], v[48:51], v[164:167], v[84:87]\n"
      "v_max3_u32 v23, v23, v50, v51\n"
      "v_mfma_f32_16x16x32_bf16 v[88:91], v[48:51], v[168:171], v[88:91]\n"
      "v_mfma_f32_16x16x32_bf16 v[92:95], v[48:51], v[172:175], v[92:95]\n"
      "s_cmp_lt_u32 s39, 3\n"
      "s_cbranch_scc0 Lrs2%=\n"
      "v_mfma_f32_16x16x32_bf16 v[80:83], v[52:55], v[176:179], v[80:83]\n"
      "v_max3_u32 v23, v23, v52, v53\n"
      "v_mfma_f32_16x16x32_bf16 v[84:87], v[52:55], v[180:183], v[84:87]\n"
      "v_max3_u32 v23, v23, v54, v55\n"
      "v_mfma_f32_16x16x32_bf16 v[88:91], v[52:55], v[184:187], v[88:91]\n"
      "v_mfma_f32_16x16x32_bf16 v[92:95], v[52:55], v[188:191], v[92:95]\n"
      "Lrs2%=:\n"
      "v_cmp_eq_u32 vcc, -1, v23\n"
      "s_cbranch_vccnz LrE2%=\n"
      "s_nop 7\n"
      "buffer_load_dwordx4 v[56:59], v16, s[20:23], s75 offen sc1\n"
      "buffer_load_dwordx4 v[60:63], v16, s[20:23], s76 offen sc1\n"
      "buffer_load_dwordx4 v[224:227], v16, s[20:23], s77 offen sc1\n"
      "buffer_load_dwordx4 v[228:231], v16, s[20:23], s78 offen sc1\n"
      "buffer_load_dwordx4 v[232:235], v16, s[20:23], s79 offen sc1\n"
      "buffer_load_dwordx4 v[236:239], v16, s[20:23], s80 offen sc1\n"
      "s_waitcnt vmcnt(0)\n"
      "s_branch LcE2%=\n"
      "LrF%=:\n"
      "s_sub_u32 s44, s44, 1\n"
      "s_cbranch_scc1 Lend%=\n"
      "buffer_load_dword v15, v17, s[20:23], s43 offen sc1\n"
      "s_waitcnt vmcnt(0)\n"
      "s_branch LcF%=\n"
      "LrE3%=:\n"
      "s_sub_u32 s44, s44, 1\n"
      "s_cbranch_scc1 Lend%=\n"
      "s_waitcnt vmcnt(0)\n"
      "buffer_load_dwordx4 v[56:59], v16, s[20:23], s75 offen sc1\n"
      "buffer_load_dwordx4 v[60:63], v16, s[20:23], s76 offen sc1\n"
      "buffer_load_dwordx4 v[224:227], v16, s[20:23], s77 offen sc1\n"
      "buffer_load_dwordx4 v[228:231], v16, s[20:23], s78 offen sc1\n"
      "buffer_load_dwordx4 v[232:235], v16, s[20:23], s79 offen sc1\n"
      "buffer_load_dwordx4 v[236:239], v16, s[20:23], s80 offen sc1\n"
      "s_waitcnt vmcnt(0)\n"
      "v_mfma_f32_16x16x32_bf16 v[64:67], v[56:59], v[112:115], 0\n"
      "v_max_u32 v23, v56, v57\n"
      "v_mfma_f32_16x16x32_bf16 v[68:71], v[56:59], v[116:119], 0\n"
      "v_max3_u32 v23, v23, v58, v59\n"
      "v_mfma_f32_16x16x32_bf16 v[72:75], v[56:59], v[120:123], 0\n"
      "v_mfma_f32_16x16x32_bf16 v[76:79], v[56:59], v[124:127], 0\n"
      "v_mfma_f32_16x16x32_bf16 v[64:67], v[60:63], v[96:99], v[64:67]\n"
      "v_max3_u32 v23, v23, v60, v61\n"
      "v_mfma_f32_16x16x32_bf16 v[68:71], v[60:63], v[100:103], v[68:71]\n"
      "v_max3_u32 v23, v23, v62, v63\n"
      "v_mfma_f32_16x16x32_bf16 v[72:75], v[60:63], v[104:107], v[72:75]\n"
      "v_mfma_f32_16x16x32_bf16 v[76:79], v[60:63], v[108:111], v[76:79]\n"
      "v_mfma_f32_16x16x32_bf16 v[64:67], v[224:227], v[128:131], v[64:67]\n"
      "v_max3_u32 v23, v23, v224, v225\n"
      "v_mfma_f32_16x16x32_bf16 v[68:71], v[224:227], v[132:135], v[68:71]\n"
      "v_max3_u32 v23, v23, v226, v227\n"
      "v_mfma_f32_16x16x32_bf16 v[72:75], v[224:227], v[136:139], v[72:75]\n"
      "v_mfma_f32_16x16x32_bf16 v[76:79], v[224:227], v[140:143], v[76:79]\n"
      "v_mfma_f32_16x16x32_bf16 v[64:67], v[228:231], v[144:147], v[64:67]\n"
      "v_max3_u32 v23, v23, v228, v229\n"
      "v_mfma_f32_16x16x32_bf16 v[68:71], v[228:231], v[148:151], v[68:71]\n"
      "v_max3_u32 v23, v23, v230, v231\n"
      "v_mfma_f32_16x16x32_bf16 v[72:75], v[228:231], v[152:155], v[72:75]\n"
      "v_mfma_f32_16x16x32_bf16 v[76:79], v[228:231], v[156:159], v[76:79]\n"
      "v_mfma_f32_16x16x32_bf16 v[64:67], v[232:235], v[160:163], v[64:67]\n"
      "v_max3_u32 v23, v23, v232, v233\n"
      "v_mfma_f32_16x16x32_bf16 v[68:71], v[232:235], v[164:167], v[68:71]\n"
      "v_max3_u32 v23, v23, v234, v235\n"
      "v_mfma_f32_16x16x32_bf16 v[72:75], v[232:235], v[168:171], v[72:75]\n"
      "v_mfma_f32_16x16x32_bf16 v[76:79], v[232:235], v[172:175], v[76:79]\n"
      "s_cmp_lt_u32 s39, 3\n"
      "s_cbranch_scc0 Lrs3%=\n"
      "v_mfma_f32_16x16x32_bf16 v[64:67], v[236:239], v[176:179], v[64:67]\n"
      "v_max3_u32 v23, v23, v236, v237\n"
      "v_mfma_f32_16x16x32_bf16 v[68:71], v[236:239], v[180:183], v[68:71]\n"
      "v_max3_u32 v23, v23, v238, v239\n"
      "v_mfma_f32_16x16x32_bf16 v[72:75], v[236:239], v[184:187], v[72:75]\n"
      "v_mfma_f32_16x16x32_bf16 v[76:79], v[236:239], v[188:191], v[76:79]\n"
      "Lrs3%=:\n"
      "v_cmp_eq_u32 vcc, -1, v23\n"
      "s_cbranch_vccnz LrE3%=\n"
      "s_nop 7\n"
      "s_waitcnt vmcnt(0)\n"
      "s_branch LcE3%=\n"
      :
      : "{s[0:1]}"(__builtin_amdgcn_kernarg_segment_ptr()), "{s2}"(blockIdx.x), "{v0}"(threadIdx.x)
      : "memory", "vcc", "scc", "s3", "s4", "s5", "s6", "s7", "s8", "s9", "s10", "s11", "s12", "s13", "s14", "s15", "s16", "s17", "s18", "s19", "s20", "s21", "s22", "s23", "s24", "s25", "s26", "s27", "s28", "s29", "s30", "s31", "s33", "s34", "s35", "s36", "s37", "s38", "s39", "s40", "s41", "s42", "s43", "s44", "s45", "s46", "s47", "s48", "s49", "s50", "s51", "s52", "s53", "s54", "s55", "s56", "s57", "s58", "s59", "s60", "s61", "s62", "s63", "s64", "s65", "s66", "s67", "s68", "s69", "s70", "s71", "s72", "s73", "s74", "s75", "s76", "s77", "s78", "s79", "s80", "s81", "s82", "s83", "s84", "s85", "s86", "s87", "s88", "s89", "s90", "s91", "s92", "s93", "s94", "s95", "s96", "s97", "s98", "s99", "v1", "v2", "v3", "v4", "v5", "v6", "v7", "v8", "v9", "v10", "v11", "v12", "v13", "v14", "v15", "v16", "v17", "v18", "v19", "v20", "v21", "v22", "v23", "v24", "v25", "v26", "v27", "v28", "v29", "v30", "v31", "v32", "v33", "v34", "v35", "v36", "v37", "v38", "v39", "v40", "v41", "v42", "v43", "v44", "v45", "v46", "v47", "v48", "v49", "v50", "v51", "v52", "v53", "v54", "v55", "v56", "v57", "v58", "v59", "v60", "v61", "v62", "v63", "v64", "v65", "v66", "v67", "v68", "v69", "v70", "v71", "v72", "v73", "v74", "v75", "v76", "v77", "v78", "v79", "v80", "v81", "v82", "v83", "v84", "v85", "v86", "v87", "v88", "v89", "v90", "v91", "v92", "v93", "v94", "v95", "v96", "v97", "v98", "v99", "v100", "v101", "v102", "v103", "v104", "v105", "v106", "v107", "v108", "v109", "v110", "v111", "v112", "v113", "v114", "v115", "v116", "v117", "v118", "v119", "v120", "v121", "v122", "v123", "v124", "v125", "v126", "v127", "v128", "v129", "v130", "v131", "v132", "v133", "v134", "v135", "v136", "v137", "v138", "v139", "v140", "v141", "v142", "v143", "v144", "v145", "v146", "v147", "v148", "v149", "v150", "v151", "v152", "v153", "v154", "v155", "v156", "v157", "v158", "v159", "v160", "v161", "v162", "v163", "v164", "v165", "v166", "v167", "v168", "v169", "v170", "v171", "v172", "v173", "v174", "v175", "v176", "v177", "v178", "v179", "v180", "v181", "v182", "v183", "v184", "v185", "v186", "v187", "v188", "v189", "v190", "v191", "v192", "v193", "v194", "v195", "v196", "v197", "v198", "v199", "v200", "v201", "v202", "v203", "v204", "v205", "v206", "v207", "v208", "v209", "v210", "v211", "v212", "v213", "v214", "v215", "v216", "v217", "v218", "v219", "v220", "v221", "v222", "v223", "v224", "v225", "v226", "v227", "v228", "v229", "v230", "v231", "v232", "v233", "v234", "v235", "v236", "v237", "v238", "v239", "v240", "v241", "v242", "v243", "v244", "v245", "v246", "v247", "v248", "v249", "v250", "v251", "v252", "v253", "v254", "v255");
}

extern "C" void kernel_launch(void* const* d_in, const int* in_sizes, int n_in, void* d_out, int out_size, void* d_ws, size_t ws_size,
                              hipStream_t stream) {
  (void)hipFuncSetAttribute((const void*)fused_kernel, hipFuncAttributeMaxDynamicSharedMemorySize, 163840);
  fused_kernel<<<256, 512, 163840, stream>>>((const u16*)d_in[0], (unsigned char*)d_ws, (const u16*)d_in[1], (const u16*)d_in[2],
                                            (const u16*)d_in[4], (const u16*)d_in[6], (const u16*)d_in[5], (const u16*)d_in[7],
                                            (u16*)d_out);
}
```
